```python
import torch

def _softmax(x, axis=-1):
    return torch.softmax(x, dim=axis)
def _gelu(x):
    return torch.nn.functional.gelu(x, approximate="tanh")
def _silu(x):
    return torch.nn.functional.silu(x)
def _sigmoid(x):
    return torch.sigmoid(x)
def _layernorm(x, axis=-1, eps=1e-5):
    m = x.mean(dim=axis, keepdim=True)
    v = x.var(dim=axis, keepdim=True, unbiased=False)
    return (x - m) / torch.sqrt(v + eps)
def _scatter_add(tgt, idx, src, axis=0):
    return tgt.clone().index_add_(axis, idx, src)
def _segment_sum(src, seg, n):
    out = torch.zeros((n,) + src.shape[1:], dtype=src.dtype)
    return out.index_add_(0, seg, src)
def _pad(x, pw):
    flat = tuple(v for pair in reversed(pw) for v in pair)
    return torch.nn.functional.pad(x, flat)

kernel_name = 'interleave_layers__sliding_block__linear_attn_block__prompt'

def setup_inputs():

    _w = (lambda a: (lambda t: t.to(torch.bfloat16) if t.is_floating_point() else t)(torch.as_tensor(a)))
    def _A_setup():
        B, S, D = 1, 2048, 512
        return {
            "x": _w((lambda *s: torch.randn(*s, dtype=torch.bfloat16) * 0.05)(B, S, D)),
            "wqkv": _w((lambda *s: torch.randn(*s, dtype=torch.bfloat16) * 0.1)(D, 3 * D)),
            "wo": _w((lambda *s: torch.randn(*s, dtype=torch.bfloat16) * 0.1)(D, D)),
        }
    _A_in = _A_setup()

    def _B_setup():
        B, S, D = 1, 2048, 512
        return {
            "x": _w((lambda *s: torch.randn(*s, dtype=torch.bfloat16) * 0.05)(B, S, D)),
            "wqkv": _w((lambda *s: torch.randn(*s, dtype=torch.bfloat16) * 1.6)(D, 3 * D)),
            "wo": _w((lambda *s: torch.randn(*s, dtype=torch.bfloat16) * 0.1)(D, D)),
        }
    _B_in = _B_setup()

    return {
        "A_x": _A_in["x"],
        "A_wqkv": _A_in["wqkv"],
        "A_wo": _A_in["wo"],
        "B_x": _B_in["x"],
        "B_wqkv": _B_in["wqkv"],
        "B_wo": _B_in["wo"],
    }

def reference(A_x, A_wqkv, A_wo, B_x, B_wqkv, B_wo):
    def _A_ref(x, wqkv, wo):
        B, S, D = x.shape
        H = 32; Dh = D // H; W = 256
        qkv = (lambda x, *d: x.permute(*d))((x @ wqkv).reshape(B, S, 3, H, Dh), 2, 0, 3, 1, 4)
        q, k, v = qkv[0], qkv[1], qkv[2]
        scores = torch.einsum("bhqd,bhkd->bhqk", q, k) * (Dh ** -0.5)
        idx = torch.arange(S)
        mask = (idx[:, None] - idx[None, :] >= 0) & (idx[:, None] - idx[None, :] < W)
        scores = torch.where(mask[None, None], scores, -1e9)
        p = _softmax(scores, axis=-1)
        o = torch.einsum("bhqk,bhkd->bhqd", p, v)
        return (lambda x, *d: x.permute(*d))(o, 0, 2, 1, 3).reshape(B, S, D) @ wo

    def _B_ref(x, wqkv, wo):
        B, S, D = x.shape
        H = 32; Dh = D // H
        qkv = (lambda x, *d: x.permute(*d))((x @ wqkv).reshape(B, S, 3, H, Dh), 2, 0, 3, 1, 4)
        q, k, v = qkv[0], qkv[1], qkv[2]
        phi = lambda t: torch.where(t > 0, t + 1, torch.exp(t)) + 1e-6
        pq, pk = phi(q), phi(k)
        kv = torch.einsum("bhsd,bhse->bhde", pk, v)
        z = torch.einsum("bhsd->bhd", pk)
        num = torch.einsum("bhqd,bhde->bhqe", pq, kv)
        den = torch.einsum("bhqd,bhd->bhq", pq, z)[..., None] + 1e-6
        o = num / den
        return (lambda x, *d: x.permute(*d))(o, 0, 2, 1, 3).reshape(B, S, D) @ wo

    x = A_x
    x = x + _A_ref(_layernorm(x, axis=-1), A_wqkv, A_wo)
    x = x + _B_ref(_layernorm(x, axis=-1), B_wqkv, B_wo)
    x = x + _A_ref(_layernorm(x, axis=-1), A_wqkv, A_wo)
    x = x + _B_ref(_layernorm(x, axis=-1), B_wqkv, B_wo)
    return x
```

```cpp
#include <hip/hip_runtime.h>
#include <hip/hip_bf16.h>
typedef unsigned short u16;
__device__ __forceinline__ float bf2f(u16 b){ return __uint_as_float((unsigned)b<<16); }
__device__ __forceinline__ u16 f2bf(float f){ unsigned u=__float_as_uint(f); u += 0x7FFFu + ((u>>16)&1u); return (u16)(u>>16);}
#define S 2048
#define D 512
__global__ void k_ln(const u16* x, u16* y){
  int row = blockIdx.x; int lane = threadIdx.x;
  float v[8]; float s=0;
  for(int j=0;j<8;j++){ v[j]=bf2f(x[row*D+lane*8+j]); s+=v[j]; }
  for(int m=32;m>=1;m>>=1) s+=__shfl_xor(s,m);
  float mean=s/D; float q=0;
  for(int j=0;j<8;j++){ float d=v[j]-mean; q+=d*d; }
  for(int m=32;m>=1;m>>=1) q+=__shfl_xor(q,m);
  float r = 1.0f/sqrtf(q/D+1e-5f);
  for(int j=0;j<8;j++) y[row*D+lane*8+j]=f2bf((v[j]-mean)*r);
}
__global__ void k_gemm(const u16* A, const u16* B, u16* C, const u16* R, int N){
  __shared__ float sa[16][513];
  int r0 = blockIdx.y*16; int c = blockIdx.x*256+threadIdx.x;
  for(int i=threadIdx.x;i<16*512;i+=256) sa[i/512][i%512]=bf2f(A[(r0+i/512)*512+i%512]);
  __syncthreads();
  float acc[16]; for(int i=0;i<16;i++) acc[i]=0;
  for(int k=0;k<512;k++){ float b=bf2f(B[k*N+c]);
    #pragma unroll
    for(int i=0;i<16;i++) acc[i]+=sa[i][k]*b; }
  #pragma unroll
  for(int i=0;i<16;i++){ float v=acc[i]; if(R){ v = bf2f(f2bf(v)) + bf2f(R[(r0+i)*N+c]); } C[(r0+i)*N+c]=f2bf(v);}
}
__global__ void k_attn(const u16* qkv, u16* o){
  int t = blockIdx.x*256+threadIdx.x; int h = t>>11; int q = t&2047;
  float qv[16]; for(int d=0;d<16;d++) qv[d]=bf2f(qkv[q*1536+h*16+d]);
  int k0 = q-255; if(k0<0)k0=0;
  float m=-1e30f;
  for(int k=k0;k<=q;k++){ float s=0; for(int d=0;d<16;d++) s+=qv[d]*bf2f(qkv[k*1536+512+h*16+d]); s*=0.25f; m=fmaxf(m,s);}
  float l=0; float acc[16]; for(int d=0;d<16;d++)acc[d]=0;
  for(int k=k0;k<=q;k++){ float s=0; for(int d=0;d<16;d++) s+=qv[d]*bf2f(qkv[k*1536+512+h*16+d]); s*=0.25f; float p=__expf(s-m); l+=p; p=bf2f(f2bf(p));
    for(int d=0;d<16;d++) acc[d]+=p*bf2f(qkv[k*1536+1024+h*16+d]); }
  for(int d=0;d<16;d++) o[q*512+h*16+d]=f2bf(acc[d]/l);
}
__device__ __forceinline__ float phi(float t){ float r = t>0? bf2f(f2bf(t+1.0f)) : bf2f(f2bf(__expf(t))); return bf2f(f2bf(r+1e-6f)); }
__global__ void k_kv(const u16* qkv, float* kv){
  int h=blockIdx.x; int t=threadIdx.x; if(t>=272) return;
  float acc=0;
  if(t<256){ int d=t>>4,e=t&15; for(int s=0;s<S;s++) acc+=phi(bf2f(qkv[s*1536+512+h*16+d]))*bf2f(qkv[s*1536+1024+h*16+e]); }
  else { int d=t-256; for(int s=0;s<S;s++) acc+=phi(bf2f(qkv[s*1536+512+h*16+d])); }
  kv[h*272+t]=bf2f(f2bf(acc));
}
__global__ void k_lin(const u16* qkv, const float* kv, u16* o){
  int t = blockIdx.x*256+threadIdx.x; int h = t>>11; int q = t&2047;
  float pq[16]; for(int d=0;d<16;d++) pq[d]=phi(bf2f(qkv[q*1536+h*16+d]));
  float den=0; for(int d=0;d<16;d++) den+=pq[d]*kv[h*272+256+d];
  den = bf2f(f2bf(bf2f(f2bf(den))+1e-6f));
  for(int e=0;e<16;e++){ float n=0; for(int d=0;d<16;d++) n+=pq[d]*kv[h*272+d*16+e]; n=bf2f(f2bf(n)); o[q*512+h*16+e]=f2bf(n/den);}
}
extern "C" void kernel_launch(void* const* d_in, const int* in_sizes, int n_in, void* d_out, int out_size, void* d_ws, size_t ws_size, hipStream_t stream){
  const u16* x=(const u16*)d_in[0]; const u16* Aw=(const u16*)d_in[1]; const u16* Ao=(const u16*)d_in[2];
  const u16* Bw=(const u16*)d_in[4]; const u16* Bo=(const u16*)d_in[5];
  char* ws=(char*)d_ws; u16* ln=(u16*)ws; u16* qkv=(u16*)(ws+(2<<20)); u16* o=(u16*)(ws+(8<<20)); u16* x1=(u16*)(ws+(10<<20)); u16* x2=(u16*)(ws+(12<<20)); float* kv=(float*)(ws+(14<<20));
  const u16* cur=x;
  for(int l=0;l<4;l++){
    u16* nxt = l==3? (u16*)d_out : ((l&1)? x2:x1);
    k_ln<<<S,64,0,stream>>>(cur,ln);
    k_gemm<<<dim3(6,128),256,0,stream>>>(ln,(l&1)?Bw:Aw,qkv,nullptr,1536);
    if(l&1){ k_kv<<<32,320,0,stream>>>(qkv,kv); k_lin<<<256,256,0,stream>>>(qkv,kv,o);} else k_attn<<<256,256,0,stream>>>(qkv,o);
    k_gemm<<<dim3(2,128),256,0,stream>>>(o,(l&1)?Bo:Ao,nxt,cur,512);
    cur=nxt;
  }
}
```

```cpp
#include <hip/hip_runtime.h>
#include <hip/hip_bf16.h>
#include <stdint.h>

#define DI __device__ __forceinline__
#define LAS __attribute__((address_space(3)))
#define GAS __attribute__((address_space(1)))
typedef unsigned short u16;
typedef unsigned v4u __attribute__((ext_vector_type(4)));
typedef unsigned v2u __attribute__((ext_vector_type(2)));
typedef float f32x16 __attribute__((ext_vector_type(16)));
typedef float f32x4 __attribute__((ext_vector_type(4)));
typedef float f32x2 __attribute__((ext_vector_type(2)));
typedef short bf16x8 __attribute__((ext_vector_type(8)));
typedef short s16x4 __attribute__((ext_vector_type(4)));
typedef __bf16 bf2 __attribute__((ext_vector_type(2)));
typedef GAS unsigned gu32;
typedef LAS char* lptr;
#define RLX_AGENT __ATOMIC_RELAXED, __HIP_MEMORY_SCOPE_AGENT
#define RLX_WG __ATOMIC_RELAXED, __HIP_MEMORY_SCOPE_WORKGROUP
#define MFMA(a, b, c) __builtin_amdgcn_mfma_f32_32x32x16_bf16(__builtin_bit_cast(bf16x8, (a)), __builtin_bit_cast(bf16x8, (b)), (c), 0, 0, 0)
#define SB() __builtin_amdgcn_sched_barrier(0)
#define DRAIN() asm volatile("s_waitcnt vmcnt(0)" ::: "memory")
#define LGKM0() asm volatile("s_waitcnt lgkmcnt(0)" ::: "memory")
#define BAR() asm volatile("s_waitcnt lgkmcnt(0)\n\ts_barrier" ::: "memory")
constexpr int SC1 = 16;

constexpr int FL = 256;
constexpr int MB2 = 2 << 20;
constexpr int WS_FX = 4096;
constexpr int WS_FO = WS_FX + 3 * 1024 * FL;
constexpr int WS_FH = WS_FO + 4 * 1024 * FL;
constexpr int WS_FP = WS_FH + 2 * 1024 * FL;
constexpr int WS_XS = WS_FP + 2 * 256 * FL;
constexpr int PSTR = 2304;
constexpr int WS_P = WS_XS + 3 * 1024 * 256;
constexpr int WS_X = 6 << 20;
constexpr int WS_O = WS_X + 3 * MB2;
constexpr int WS_KV = WS_O + 4 * MB2;
constexpr int WS_END = WS_KV + 4 * MB2;
static_assert(WS_P + 2 * 256 * PSTR <= WS_X, "ws map");

constexpr int L_WQ = 0, L_WK = 32768, L_WV = 65536, L_WO = 98304, L_K = 131072, L_V = 73728, L_MISC = 155648;
constexpr int L_QF = L_MISC, L_ONES = L_MISC + 4096, L_ST = L_MISC + 5120, L_CS = L_MISC + 7168, L_CTR = L_MISC + 7680;
constexpr int L_LN = L_WO;
constexpr int L_PW = L_K, L_PR = L_K + 4 * PSTR;
constexpr int LDS_BYTES = 163840;
constexpr int NQ0 = 18;
enum { C_W = 0, C_CS = 1, C_ATT = 2, C_WO = 3, C_OP = 4 };

DI unsigned pk2(float lo, float hi) { f32x2 v = {lo, hi}; return __builtin_bit_cast(unsigned, __builtin_convertvector(v, bf2)); }
DI float bflo(unsigned u) { return __uint_as_float(u << 16); }
DI float bfhi(unsigned u) { return __uint_as_float(u & 0xffff0000u); }
DI float rbf(float x) { return bflo(pk2(x, 0.f)); }
DI float dot2(unsigned a, unsigned b, float c) { return __builtin_amdgcn_fdot2_f32_bf16(__builtin_bit_cast(bf2, a), __builtin_bit_cast(bf2, b), c, false); }
DI float half_sum(float v) { auto r = __builtin_amdgcn_permlane32_swap(__float_as_uint(v), __float_as_uint(v), false, false); return __uint_as_float(r[0]) + __uint_as_float(r[1]); }
DI float half_max(float v) { auto r = __builtin_amdgcn_permlane32_swap(__float_as_uint(v), __float_as_uint(v), false, false); return __builtin_fmaxf(__uint_as_float(r[0]), __uint_as_float(r[1])); }
DI v4u pack8(const f32x16& a, int o) { v4u r = {pk2(a[o], a[o + 1]), pk2(a[o + 2], a[o + 3]), pk2(a[o + 4], a[o + 5]), pk2(a[o + 6], a[o + 7])}; return r; }
DI v4u lds_r128(lptr p) { return *(const LAS v4u*)p; }
DI f32x4 lds_r128f(lptr p) { return *(const LAS f32x4*)p; }
DI void lds_w128(lptr p, v4u v) { *(LAS v4u*)p = v; }
DI v4u wfrag(lptr p0, lptr p1, int off) {
  const s16x4 a = __builtin_amdgcn_ds_read_tr16_b64_v4i16((LAS s16x4*)(p0 + off));
  const s16x4 b = __builtin_amdgcn_ds_read_tr16_b64_v4i16((LAS s16x4*)(p1 + off));
  const v2u x = __builtin_bit_cast(v2u, a), y = __builtin_bit_cast(v2u, b);
  v4u r = {x[0], x[1], y[0], y[1]};
  return r;
}
DI void glds16(const void* base, unsigned voff, unsigned lds_dst) {
  unsigned keep;
  asm volatile("s_mov_b32 %0, m0\n\ts_mov_b32 m0, %3\n\ts_nop 0\n\tglobal_load_lds_dwordx4 %1, %2\n\ts_mov_b32 m0, %0" : "=&s"(keep) : "v"(voff), "s"(base), "s"(lds_dst) : "memory");
}
DI void dma_strip(const u16* wcol, int ldw, unsigned lds_base, int n0, int n1, int lane) {
  const unsigned voff = ((lane >> 2) * ldw + (lane & 3) * 8) * 2;
  const u16* p = wcol + (size_t)(16 * n0) * ldw;
#pragma unroll 1
  for (int n = n0; n < n1; ++n) { glds16(p, voff, lds_base + n * 1024); p += 16 * ldw; }
}
DI void ctr_arrive(lptr L, int c, int lane) { asm volatile("" ::: "memory"); if (lane == 0) (void)__hip_atomic_fetch_add((LAS unsigned*)(L + L_CTR + 4 * c), 1u, RLX_WG); }
DI void ctr_wait(lptr L, int c, unsigned target) {
  unsigned spins = 0;
  while (__builtin_amdgcn_readfirstlane(__hip_atomic_load((LAS unsigned*)(L + L_CTR + 4 * c), RLX_WG)) < target && ++spins < (1u << 22)) __builtin_amdgcn_s_sleep(1);
  asm volatile("" ::: "memory");
}
DI void poll(unsigned char* ws, int line_off, int slot, bool active, unsigned tag, unsigned zero) {
  gu32* p = (gu32*)(ws + line_off) + slot;
  for (unsigned spins = 0; spins < (1u << 14); ++spins) {
    unsigned v = tag;
    if (active) v = __hip_atomic_fetch_add(p, zero, RLX_AGENT);
    if (__all(v == tag)) break;
  }
  __builtin_amdgcn_fence(__ATOMIC_ACQUIRE, "wavefront");
  asm volatile("" ::: "memory");
}
DI void publish(unsigned char* ws, int word_off, bool active, unsigned tag) {
  DRAIN();
  if (active) (void)__hip_atomic_exchange((gu32*)(ws + word_off), tag, RLX_AGENT);
}
DI v4u ld_sc1(__amdgpu_buffer_rsrc_t rs, int voff, int soff) { return __builtin_amdgcn_raw_buffer_load_b128(rs, voff, soff, SC1); }
DI void st_sc1(__amdgpu_buffer_rsrc_t rs, v4u v, int voff, int soff) { __builtin_amdgcn_raw_buffer_store_b128(v, rs, voff, soff, SC1); }

DI void row_stats(float s1, float s2, float& mu, float& rs) {
  const float m = s1 * (1.0f / 512.0f);
  const float var = __builtin_fmaxf(__builtin_fmaf(-m, m, s2 * (1.0f / 512.0f)), 0.f);
  mu = rbf(m);
  rs = __builtin_amdgcn_rcpf(rbf(__builtin_amdgcn_sqrtf(rbf(rbf(var) + 1e-5f))));
}
DI v4u ln_frag(v4u x, float mu, float rs) {
  v4u r;
#pragma unroll
  for (int i = 0; i < 4; ++i) { const unsigned d = pk2(bflo(x[i]) - mu, bfhi(x[i]) - mu); r[i] = pk2(bflo(d) * rs, bfhi(d) * rs); }
  return r;
}
DI void scale_tok(f32x16& a, lptr cs, float mu, float rs) {
#pragma unroll
  for (int q = 0; q < 4; ++q) { const f32x4 c = lds_r128f(cs + q * 32);
#pragma unroll
    for (int i = 0; i < 4; ++i) a[4 * q + i] = __builtin_fmaf(-mu, c[i], a[4 * q + i]) * rs; }
}
DI void scale_feat(f32x16& a, lptr st, float cs) {
#pragma unroll
  for (int q = 0; q < 4; ++q) { const f32x4 u = lds_r128f(st + q * 64), v = lds_r128f(st + q * 64 + 16);
    a[4 * q + 0] = __builtin_fmaf(-u[0], cs, a[4 * q + 0]) * u[1]; a[4 * q + 1] = __builtin_fmaf(-u[2], cs, a[4 * q + 1]) * u[3];
    a[4 * q + 2] = __builtin_fmaf(-v[0], cs, a[4 * q + 2]) * v[1]; a[4 * q + 3] = __builtin_fmaf(-v[2], cs, a[4 * q + 3]) * v[3]; }
}
DI float phi1(float t) { return (t > 0.f ? t + 1.0f : __builtin_amdgcn_exp2f(t * 1.4426950408889634f)) + 1e-6f; }
DI v4u phi_pack8(const f32x16& a, int o) {
  v4u r;
#pragma unroll
  for (int i = 0; i < 4; ++i) { const unsigned t = pk2(a[o + 2 * i], a[o + 2 * i + 1]); r[i] = pk2(phi1(bflo(t)), phi1(bfhi(t))); }
  return r;
}
DI f32x16 zero16() { f32x16 z;
#pragma unroll
  for (int i = 0; i < 16; ++i) z[i] = 0.f;
  return z; }

__global__ void __launch_bounds__(512, 2) mega(const u16* __restrict__ x_in, const u16* __restrict__ Aw, const u16* __restrict__ Ao,
                                               const u16* __restrict__ Bw, const u16* __restrict__ Bo, u16* __restrict__ out, unsigned char* ws) {
  extern __shared__ __attribute__((aligned(16))) char smem[];
  const lptr L = (lptr)smem;
  const unsigned L0 = (unsigned)(uintptr_t)L;
  int b = blockIdx.x, lane = threadIdx.x & 63, w = __builtin_amdgcn_readfirstlane(threadIdx.x >> 6);
  const __amdgpu_buffer_rsrc_t rs = __builtin_amdgcn_make_buffer_rsrc(ws, 0, WS_END, 0x00020000);
  unsigned zero = 0; asm volatile("" : "+v"(zero));
  unsigned epoch = __hip_atomic_load((gu32*)ws, RLX_AGENT);

  f32x16 xres = zero16();
  float mu = 0.f, rstd = 1.f;

  unsigned tag = 0;

  for (int pr = 0; pr < 2; ++pr) {
#pragma unroll
    for (int isB = 0; isB < 2; ++isB) {
      const int l = 2 * pr + isB;
      asm volatile("" : "+v"(lane), "+s"(b), "+s"(w));
      const int g = 2 * (b & 7) + (b >> 7), j = (b >> 3) & 15;
      const int t = w & 3, hh = w >> 2, r = lane & 31, h = lane >> 5;
      const bool isQ = w < 4;
      const int gt = g * 4 + t, gj = g * 16 + j;
      const int trl = ((lane & 15) >> 2) * 64 + ((lane >> 4) & 1) * 32 + (lane & 3) * 8;
      const int l16 = lane * 16;
      const bool first = (isB == 0) && (pr == 0);
      v4u qf;
      if (isQ) {
        v4u xf[32];
        v2u st[8];
        const lptr p0 = L + trl + (first ? h * 512 : h * 256), p1 = p0 + (first ? 256 : 512);
        if (first) {
          const u16* xrow = x_in + (size_t)(gt * 32 + r) * 512 + 8 * h;
#pragma unroll
          for (int s = 0; s < NQ0; ++s) xf[s] = *(const GAS v4u*)(xrow + 16 * s);
          if (w == 0) { const v4u o = {0x3f803f80u, 0x3f803f80u, 0x3f803f80u, 0x3f803f80u}; lds_w128(L + L_ONES + l16, o); }
          if (w == 1 && lane < 40) { const v4u z = {0u, 0u, 0u, 0u}; lds_w128(L + L_CS + l16, z); }
          float s1 = 0.f, s2 = 0.f;
#pragma unroll
          for (int s = 0; s < NQ0; ++s)
#pragma unroll
            for (int i = 0; i < 4; ++i) { s1 = dot2(xf[s][i], 0x3f803f80u, s1); s2 = dot2(xf[s][i], xf[s][i], s2); }
          s1 = half_sum(s1); s2 = half_sum(s2);
          if (h == 0) { f32x2 v = {s1, s2}; *(LAS f32x2*)(L + L_ST + w * 256 + r * 8) = v; }
          BAR();
          const f32x2 v = *(const LAS f32x2*)(L + L_ST + (w + 4) * 256 + r * 8);
          float m0, r0; row_stats(s1 + v[0], s2 + v[1], m0, r0);
#pragma unroll
          for (int s = 0; s < NQ0; ++s) xf[s] = ln_frag(xf[s], m0, r0);
          BAR();
#pragma unroll
          for (int s = NQ0; s < 32; ++s) xf[s] = lds_r128(L + L_LN + (t * 14 + s - NQ0) * 1024 + l16);
          if (h == 0) { f32x2 v = {0.f, 1.f}; *(LAS f32x2*)(L + L_ST + w * 256 + r * 8) = v; }
          LGKM0();
          ctr_arrive(L, C_OP, lane);
          asm volatile("" : "+v"(epoch));
          tag = __builtin_amdgcn_readfirstlane(epoch) + 1u;
#pragma unroll
          for (int i = 0; i < 8; ++i) { st[i][0] = 0u; st[i][1] = 0u; }
        } else {
          DRAIN(); ctr_arrive(L, C_W, lane); ctr_wait(L, C_W, 4u * l);
          if (w < 3) {
            const v4u ones = {0x3f803f80u, 0x3f803f80u, 0x3f803f80u, 0x3f803f80u};
            f32x16 c = zero16();
            const lptr q0 = p0 + w * 32768, q1 = p1 + w * 32768;
#pragma unroll
            for (int s = 0; s < 32; ++s) c = MFMA(ones, wfrag(q0, q1, s * 1024), c);
            if (h == 0) *(LAS float*)(L + L_CS + (w * 32 + r) * 4) = c[0];
            LGKM0(); ctr_arrive(L, C_CS, lane);
          }
          poll(ws, WS_FX + (((l - 1) * 64 + gt) * 16 + j) * FL, lane, lane < 16, tag, zero);
          const int xb = WS_X + (l - 1) * MB2 + gt * 32768;
#pragma unroll
          for (int s = 0; s < 32; ++s) xf[s] = ld_sc1(rs, l16 + (s & 3) * 1024, xb + (s >> 2) * 4096);
          const int sb = WS_XS + ((l - 1) * 64 + gt) * 4096;
#pragma unroll
          for (int i = 0; i < 8; ++i) st[i] = __builtin_amdgcn_raw_buffer_load_b64(rs, h * 256 + r * 8 + i * 512, sb, SC1);
          SB();
        }
        f32x16 aK = zero16(), aV = zero16();
        { const lptr v0 = p0 + L_WV, v1 = p1 + L_WV;
#pragma unroll
          for (int s = 0; s < 32; ++s) {
            const v4u wk = wfrag(p0, p1, L_WK + s * 1024), wv = wfrag(v0, v1, s * 1024);
            aK = isB ? MFMA(xf[s], wk, aK) : MFMA(wk, xf[s], aK);
            aV = MFMA(xf[s], wv, aV);
          } }
        if (!first) {
          float s1 = 0.f, s2 = 0.f;
#pragma unroll
          for (int i = 0; i < 8; ++i) { s1 += __uint_as_float(st[i][0]); s2 += __uint_as_float(st[i][1]); }
          row_stats(half_sum(s1), half_sum(s2), mu, rstd);
          if (h == 0) { f32x2 v = {mu, rstd}; *(LAS f32x2*)(L + L_ST + w * 256 + r * 8) = v; }
          ctr_wait(L, C_CS, 3u * l);
        }
        const lptr stp = L + L_ST + w * 256 + h * 32;
        scale_feat(aV, stp, *(const LAS float*)(L + L_CS + (64 + r) * 4));
        const v4u vf0 = pack8(aV, 0), vf1 = pack8(aV, 8);
        if (!isB) {
          scale_tok(aK, L + L_CS + 128 + h * 16, mu, rstd);
          const v4u k0 = pack8(aK, 0), k1 = pack8(aK, 8);
          if (g < 15) {
            const int kb = WS_KV + ((pr * 256 + gj) * 4 + t) * 4096;
            st_sc1(rs, k0, l16, kb); st_sc1(rs, k1, l16 + 1024, kb); st_sc1(rs, vf0, l16 + 2048, kb); st_sc1(rs, vf1, l16 + 3072, kb);
          }
          ctr_arrive(L, C_ATT, lane); ctr_wait(L, C_ATT, 12u * pr + 4u);
          lds_w128(L + L_K + (8 + t) * 1024 + l16, k0); lds_w128(L + L_K + (20 + t) * 1024 + l16, k1);
          const lptr vd = L + L_V + ((r >> 4) * 12 + 8 + t) * 1024 + h * 256 + (r & 15) * 16;
          lds_w128(vd, vf0); lds_w128(vd + 512, vf1);
        } else {
          scale_feat(aK, stp, *(const LAS float*)(L + L_CS + (32 + r) * 4));
          const v4u k0 = phi_pack8(aK, 0), k1 = phi_pack8(aK, 8);
          f32x16 d = MFMA(k0, vf0, zero16()); d = MFMA(k1, vf1, d);
          float z = 0.f;
#pragma unroll
          for (int i = 0; i < 4; ++i) { z = dot2(k0[i], 0x3f803f80u, z); z = dot2(k1[i], 0x3f803f80u, z); }
          z = half_sum(z);
          f32x4 u0, u1;
#pragma unroll
          for (int i = 0; i < 4; ++i) { u0[i] = (r < 16) ? d[i] : d[8 + i]; u1[i] = (r < 16) ? d[4 + i] : d[12 + i]; }
          const lptr pw = L + L_PW + t * PSTR;
          *(LAS f32x4*)(pw + l16) = u0; *(LAS f32x4*)(pw + 1024 + l16) = u1;
          if (h == 0) *(LAS float*)(pw + 2048 + r * 4) = z;
          BAR();
        }
        f32x16 aQ = zero16();
#pragma unroll
        for (int s = 0; s < 32; ++s) aQ = MFMA(wfrag(p0, p1, L_WQ + s * 1024), xf[s], aQ);
        if (!isB) publish(ws, WS_FH + ((pr * 256 + gj + 16 * (lane + 1)) * 4 + t) * FL + lane * 4, lane < 2 && g + lane < 15, tag);
        scale_tok(aQ, L + L_CS + h * 16, mu, rstd);
        if (!isB) { qf = pack8(aQ, 0); lds_w128(L + L_QF + t * 1024 + l16, pack8(aQ, 8)); }
        else { qf = phi_pack8(aQ, 0); lds_w128(L + L_QF + t * 1024 + l16, phi_pack8(aQ, 8)); }
        BAR();
      } else {
        if (first) {
          v4u xf[32 - NQ0];
          const u16* xrow = x_in + (size_t)(gt * 32 + r) * 512 + 8 * h;
#pragma unroll
          for (int s = NQ0; s < 32; ++s) xf[s - NQ0] = *(const GAS v4u*)(xrow + 16 * s);
          SB();
          dma_strip(Aw + 512 + 32 * j, 1536, L0 + L_WK, 8 * t, 8 * t + 8, lane);
          dma_strip(Aw + 1024 + 32 * j, 1536, L0 + L_WV, 8 * t, 8 * t + 8, lane);
          dma_strip(Aw + 32 * j, 1536, L0 + L_WQ, 8 * t, 8 * t + 8, lane);
          const u16* xr = x_in + (size_t)(gt * 32 + r) * 512 + 32 * j + 4 * h;
#pragma unroll
          for (int q = 0; q < 4; ++q) { const v2u v = *(const GAS v2u*)(xr + 8 * q);
            xres[4 * q] = bflo(v[0]); xres[4 * q + 1] = bfhi(v[0]); xres[4 * q + 2] = bflo(v[1]); xres[4 * q + 3] = bfhi(v[1]); }
          float s1 = 0.f, s2 = 0.f;
#pragma unroll
          for (int s = 0; s < 32 - NQ0; ++s)
#pragma unroll
            for (int i = 0; i < 4; ++i) { s1 = dot2(xf[s][i], 0x3f803f80u, s1); s2 = dot2(xf[s][i], xf[s][i], s2); }
          s1 = half_sum(s1); s2 = half_sum(s2);
          if (h == 0) { f32x2 v = {s1, s2}; *(LAS f32x2*)(L + L_ST + w * 256 + r * 8) = v; }
          BAR();
          const f32x2 v = *(const LAS f32x2*)(L + L_ST + t * 256 + r * 8);
          float m0, r0; row_stats(v[0] + s1, v[1] + s2, m0, r0);
#pragma unroll
          for (int s = 0; s < 32 - NQ0; ++s) lds_w128(L + L_LN + (t * 14 + s) * 1024 + l16, ln_frag(xf[s], m0, r0));
          DRAIN();
          BAR();
          asm volatile("" : "+v"(epoch));
          tag = __builtin_amdgcn_readfirstlane(epoch) + 1u;
        }
        ctr_wait(L, C_OP, 4u * (l + 1));
        dma_strip((isB ? Bo : Ao) + 32 * j, 512, L0 + L_WO, 8 * t, 8 * t + 8, lane);
        if (!isB) {
          v4u kv[8];
          poll(ws, WS_FH + ((pr * 256 + gj) * 4 + t) * FL, lane, lane < 2 && lane < g, tag, zero);
          const v4u z4 = {0u, 0u, 0u, 0u};
#pragma unroll
          for (int q = 0; q < 2; ++q) {
            const int kb = WS_KV + ((pr * 256 + gj - 16 * (q + 1)) * 4 + t) * 4096;
            if (q < g) {
#pragma unroll
              for (int i = 0; i < 4; ++i) kv[4 * q + i] = ld_sc1(rs, l16 + i * 1024, kb);
            } else {
#pragma unroll
              for (int i = 0; i < 4; ++i) kv[4 * q + i] = z4;
            }
          }
          ctr_wait(L, C_ATT, 12u * pr + 4u);
#pragma unroll
          for (int q = 0; q < 2; ++q) {
            const int ti = 4 - 4 * q + t;
            lds_w128(L + L_K + ti * 1024 + l16, kv[4 * q]); lds_w128(L + L_K + (12 + ti) * 1024 + l16, kv[4 * q + 1]);
            const lptr vd = L + L_V + ((r >> 4) * 12 + ti) * 1024 + h * 256 + (r & 15) * 16;
            lds_w128(vd, kv[4 * q + 2]); lds_w128(vd + 512, kv[4 * q + 3]);
          }
          DRAIN();
          BAR();
        } else {
          BAR();
          if (t == 0) {
            f32x4 u0 = lds_r128f(L + L_PW + l16), u1 = lds_r128f(L + L_PW + 1024 + l16);
            float z = *(const LAS float*)(L + L_PW + 2048 + r * 4);
#pragma unroll
            for (int q = 1; q < 4; ++q) { u0 += lds_r128f(L + L_PW + q * PSTR + l16); u1 += lds_r128f(L + L_PW + q * PSTR + 1024 + l16); z += *(const LAS float*)(L + L_PW + q * PSTR + 2048 + r * 4); }
            const int pb = WS_P + ((pr * 16 + j) * 16 + g) * PSTR;
            st_sc1(rs, __builtin_bit_cast(v4u, u0), l16, pb); st_sc1(rs, __builtin_bit_cast(v4u, u1), l16 + 1024, pb);
            if (h == 0) __builtin_amdgcn_raw_buffer_store_b32(__float_as_uint(z), rs, 2048 + r * 4, pb, SC1);
            publish(ws, WS_FP + (pr * 256 + lane * 16 + j) * FL + g * 4, lane < 16, tag);
          }
          poll(ws, WS_FP + (pr * 256 + gj) * FL, 4 * t + lane, lane < 4, tag, zero);
          v4u a[8]; unsigned zz[4];
#pragma unroll
          for (int q = 0; q < 4; ++q) { const int pb = WS_P + ((pr * 16 + j) * 16 + 4 * t + q) * PSTR;
            a[2 * q] = ld_sc1(rs, l16, pb); a[2 * q + 1] = ld_sc1(rs, l16 + 1024, pb); zz[q] = __builtin_amdgcn_raw_buffer_load_b32(rs, 2048 + r * 4, pb, SC1); }
          SB();
          f32x4 u0 = __builtin_bit_cast(f32x4, a[0]), u1 = __builtin_bit_cast(f32x4, a[1]); float z = __uint_as_float(zz[0]);
#pragma unroll
          for (int q = 1; q < 4; ++q) { u0 += __builtin_bit_cast(f32x4, a[2 * q]); u1 += __builtin_bit_cast(f32x4, a[2 * q + 1]); z += __uint_as_float(zz[q]); }
          const lptr pw = L + L_PR + t * PSTR;
          *(LAS f32x4*)(pw + l16) = u0; *(LAS f32x4*)(pw + 1024 + l16) = u1;
          if (h == 0) *(LAS float*)(pw + 2048 + r * 4) = z;
          DRAIN();
          BAR();
        }
        qf = lds_r128(L + L_QF + t * 1024 + l16);
      }

      f32x16 o;
      if (!isB) {
        const lptr kp = L + L_K + (hh * 12 + t) * 1024 + l16;
        const int nv = 8 - 4 * g - t;
        f32x16 s[9];
        const f32x16 z16 = zero16();
#pragma unroll
        for (int n = 0; n < 9; ++n) {
          if (n >= nv) s[n] = MFMA(lds_r128(kp + n * 1024), qf, z16);
          else {
#pragma unroll
            for (int i = 0; i < 16; ++i) s[n][i] = -3.0e38f;
          }
        }
#pragma unroll
        for (int i = 0; i < 16; ++i) { const int key = (i & 3) + 8 * (i >> 2) + 4 * h; s[0][i] = (key <= r) ? s[8][i] : s[0][i]; }
        float m = s[0][0];
#pragma unroll
        for (int n = 0; n < 8; ++n)
#pragma unroll
          for (int i = 0; i < 16; i += 2) m = __builtin_fmaxf(__builtin_fmaxf(m, s[n][i]), s[n][i + 1]);
        m = half_max(m);
        constexpr float C = 0.25f * 1.4426950408889634f;
        const float nm = -m * C;
        lptr vp = (r < 16 ? L + L_V + (hh * 12 + t) * 1024 : L + L_ONES) + h * 256 + (r & 15) * 16;
        const int vs = r < 16 ? 1024 : 0;
        o = z16;
#pragma unroll
        for (int n = 0; n < 8; ++n) {
#pragma unroll
          for (int i = 0; i < 16; ++i) s[n][i] = __builtin_amdgcn_exp2f(__builtin_fmaf(s[n][i], C, nm));
          if (n == 0) {
            f32x16 pf, pd;
#pragma unroll
            for (int i = 0; i < 16; ++i) { const int key = (i & 3) + 8 * (i >> 2) + 4 * h; pd[i] = (key <= r) ? s[0][i] : 0.f; pf[i] = (key <= r) ? 0.f : s[0][i]; }
            o = MFMA(lds_r128(vp), pack8(pf, 0), o); o = MFMA(lds_r128(vp + 512), pack8(pf, 8), o);
            const lptr vq = vp + 8 * vs;
            o = MFMA(lds_r128(vq), pack8(pd, 0), o); o = MFMA(lds_r128(vq + 512), pack8(pd, 8), o);
          } else {
            const lptr vq = vp + n * vs;
            o = MFMA(lds_r128(vq), pack8(s[n], 0), o); o = MFMA(lds_r128(vq + 512), pack8(s[n], 8), o);
          }
        }
        ctr_arrive(L, C_ATT, lane);
      } else {
        const lptr a0 = L + L_PR + (r < 16 ? (hh * 16 + r + 32 * h) * 16 : 2048 + (hh * 16 + 4 * h) * 4);
        const lptr a1 = a0 + (r < 16 ? 1024 : 32);
        f32x4 u0 = lds_r128f(a0), u1 = lds_r128f(a1);
#pragma unroll
        for (int q = 1; q < 4; ++q) { u0 += lds_r128f(a0 + q * PSTR); u1 += lds_r128f(a1 + q * PSTR); }
        const v4u af = {pk2(u0[0], u0[1]), pk2(u0[2], u0[3]), pk2(u1[0], u1[1]), pk2(u1[2], u1[3])};
        o = MFMA(af, qf, zero16());
        o[8] += 1e-6f;
      }
      { const float ri = __builtin_amdgcn_rcpf(o[8]);
#pragma unroll
        for (int i = 0; i < 8; ++i) o[i] *= ri;
        st_sc1(rs, pack8(o, 0), l16, WS_O + l * MB2 + gt * 32768 + (2 * j + hh) * 1024);
        publish(ws, WS_FO + ((l * 64 + gt) * 16 + lane) * FL + (2 * j + hh) * 4, lane < 16, tag);
      }

      if (isQ) {
        if (l < 3) {
          const u16* W = (isB ? Aw : Bw) + 32 * j;
          dma_strip(W + 512, 1536, L0 + L_WK, 8 * t, 8 * t + 8, lane);
          dma_strip(W, 1536, L0 + L_WQ, 8 * t, 8 * t + 8, lane);
          if (!isB) ctr_wait(L, C_ATT, 12u * pr + 12u);
          dma_strip(W + 1024, 1536, L0 + L_WV, 8 * t, 8 * t + 8, lane);
        }
      } else {
        poll(ws, WS_FO + ((l * 64 + gt) * 16 + j) * FL, lane, lane < 32, tag, zero);
        const int ob = WS_O + l * MB2 + gt * 32768;
        v4u of[32];
#pragma unroll
        for (int s = 0; s < 32; ++s) of[s] = ld_sc1(rs, l16 + (s & 3) * 1024, ob + (s >> 2) * 4096);
        SB();
        const lptr p0 = L + L_WO + trl + h * 256, p1 = p0 + 512;
        f32x16 y = zero16();
#pragma unroll
        for (int s = 0; s < 32; ++s) y = MFMA(wfrag(p0, p1, s * 1024), of[s], y);
        ctr_arrive(L, C_OP, lane);
        v4u x0, x1; float s1 = 0.f, s2 = 0.f;
#pragma unroll
        for (int i = 0; i < 8; ++i) {
          const unsigned yb = pk2(y[2 * i], y[2 * i + 1]);
          const unsigned xb = pk2(xres[2 * i] + bflo(yb), xres[2 * i + 1] + bfhi(yb));
          xres[2 * i] = bflo(xb); xres[2 * i + 1] = bfhi(xb);
          s1 = dot2(xb, 0x3f803f80u, s1); s2 = dot2(xb, xb, s2);
          if (i < 4) x0[i] = xb; else x1[i - 4] = xb;
        }
        if (l < 3) {
          const int xb = WS_X + l * MB2 + gt * 32768 + j * 2048;
          st_sc1(rs, x0, l16, xb); st_sc1(rs, x1, l16 + 1024, xb);
          s1 = half_sum(s1); s2 = half_sum(s2);
          if (h == 0) { v2u v = {__float_as_uint(s1), __float_as_uint(s2)}; __builtin_amdgcn_raw_buffer_store_b64(v, rs, r * 8, WS_XS + ((l * 64 + gt) * 16 + j) * 256, SC1); }
          publish(ws, WS_FX + ((l * 64 + gt) * 16 + lane) * FL + j * 4, lane < 16, tag);
        } else {
          u16* op = out + (size_t)(gt * 32 + r) * 512 + 32 * j + 4 * h;
          v2u a = {x0[0], x0[1]}, c = {x0[2], x0[3]}, d = {x1[0], x1[1]}, e = {x1[2], x1[3]};
          *(GAS v2u*)(op) = a; *(GAS v2u*)(op + 8) = c; *(GAS v2u*)(op + 16) = d; *(GAS v2u*)(op + 24) = e;
          if (b == 0 && w == 4 && lane == 0) __hip_atomic_store((gu32*)ws, tag, RLX_AGENT);
        }
      }
    }
  }
}

extern "C" void kernel_launch(void* const* d_in, const int* in_sizes, int n_in, void* d_out, int out_size, void* d_ws, size_t ws_size,
                              hipStream_t stream) {
  (void)in_sizes; (void)n_in; (void)out_size; (void)ws_size;
  (void)hipFuncSetAttribute((const void*)mega, hipFuncAttributeMaxDynamicSharedMemorySize, LDS_BYTES);
  mega<<<dim3(256), dim3(512), LDS_BYTES, stream>>>((const u16*)d_in[0], (const u16*)d_in[1], (const u16*)d_in[2], (const u16*)d_in[4],
                                                    (const u16*)d_in[5], (u16*)d_out, (unsigned char*)d_ws);
}
```

```cpp
#include <hip/hip_runtime.h>
#include <hip/hip_bf16.h>
#include <stdint.h>

#define DI __device__ __forceinline__
#define LAS __attribute__((address_space(3)))
#define GAS __attribute__((address_space(1)))
typedef unsigned short u16;
typedef unsigned v4u __attribute__((ext_vector_type(4)));
typedef unsigned v2u __attribute__((ext_vector_type(2)));
typedef float f32x16 __attribute__((ext_vector_type(16)));
typedef float f32x4 __attribute__((ext_vector_type(4)));
typedef float f32x2 __attribute__((ext_vector_type(2)));
typedef short bf16x8 __attribute__((ext_vector_type(8)));
typedef short s16x4 __attribute__((ext_vector_type(4)));
typedef __bf16 bf2 __attribute__((ext_vector_type(2)));
typedef GAS unsigned gu32;
typedef LAS char* lptr;
#define RLX_AGENT __ATOMIC_RELAXED, __HIP_MEMORY_SCOPE_AGENT
#define RLX_WG __ATOMIC_RELAXED, __HIP_MEMORY_SCOPE_WORKGROUP
#define MFMA(a, b, c) __builtin_amdgcn_mfma_f32_32x32x16_bf16(__builtin_bit_cast(bf16x8, (a)), __builtin_bit_cast(bf16x8, (b)), (c), 0, 0, 0)
#define SB() __builtin_amdgcn_sched_barrier(0)
#define PIN(x) asm volatile("" : "+v"(x))
#define DRAIN() asm volatile("s_waitcnt vmcnt(0)" ::: "memory")
#define LGKM0() asm volatile("s_waitcnt lgkmcnt(0)" ::: "memory")
#define BAR() asm volatile("s_waitcnt lgkmcnt(0)\n\ts_barrier" ::: "memory")
constexpr int SC1 = 16;
constexpr unsigned ONE2 = 0x3f803f80u;
#define STAMP(i) do {} while (0)

#define XMAP 1
#define MAP_G(b) (4 * (((b) & 7) >> 1) + ((b) >> 6))
#define MAP_J(b) (8 * ((b) & 1) + (((b) >> 3) & 7))
constexpr int FL = 256;
constexpr int MB2 = 2 << 20;
constexpr int WS_FX = 4096;
constexpr int WS_FO = WS_FX + 4 * 256 * FL;
constexpr int WS_FH = WS_FO + 4 * 256 * FL;
constexpr int WS_FP = WS_FH + 2 * 256 * FL;
constexpr int WS_FR = WS_FP + 2 * 256 * FL;
constexpr int WS_XS = WS_FR + 256 * FL;
constexpr int PSTR = 2304;
constexpr int WS_P = WS_XS + 4 * 1024 * 256;
constexpr int WS_X = 6 << 20;
constexpr int WS_O = WS_X + 4 * MB2;
constexpr int WS_KV = WS_O + 4 * MB2;
constexpr int WS_WT = WS_KV + 4 * MB2;
constexpr int WS_END = WS_WT + 2 * MB2;
constexpr int T_WB = 0, T_WA = 96, T_OA = 192, T_OB = 224;
static_assert(WS_P + 2 * 256 * PSTR <= WS_X, "ws map");

constexpr int L_WQ = 0, L_WK = 32768, L_WV = 65536, L_WO = 98304, L_K = 131072, L_V = L_WO + 8192, L_MISC = 155648;
constexpr int L_QF = L_MISC, L_MSK = L_MISC + 4096, L_ONES = L_MISC + 6144;
constexpr int L_RS = L_ONES + 256;
constexpr int L_WX = L_MISC + 7168, L_MU = L_WX + 768, L_CTR = L_MU + 192;
constexpr int L_NM = L_QF;
constexpr int L_LN = L_K;
constexpr int L_PW = L_K, L_PR = L_K + 4 * PSTR;
constexpr int LDS_BYTES = 163840;
enum { C_W = 0, C_CS = 1, C_CA = 2, C_KV = 3, C_OP = 4, C_P = 5, C_PX = 6, C_PO = 7, C_PH = 8, C_PR = 9, C_R = 10, C_WO = 11, C_XI = 12, C_ST = 13, C_QD = 14 };

template <int I> struct IC { static constexpr int value = I; };
template <int N, int I = 0, class F> DI void static_for(F&& f) { if constexpr (I < N) { f(IC<I>{}); static_for<N, I + 1>(f); } }
DI unsigned pk2(float lo, float hi) { f32x2 v = {lo, hi}; return __builtin_bit_cast(unsigned, __builtin_convertvector(v, bf2)); }
DI float bflo(unsigned u) { return __uint_as_float(u << 16); }
DI float bfhi(unsigned u) { return __uint_as_float(u & 0xffff0000u); }
DI float rbf(float x) { return bflo(pk2(x, 0.f)); }
DI float dot2(unsigned a, unsigned b, float c) { return __builtin_amdgcn_fdot2_f32_bf16(__builtin_bit_cast(bf2, a), __builtin_bit_cast(bf2, b), c, false); }
DI float half_sum(float v) { auto r = __builtin_amdgcn_permlane32_swap(__float_as_uint(v), __float_as_uint(v), false, false); return __uint_as_float(r[0]) + __uint_as_float(r[1]); }
DI float half_max(float v) { auto r = __builtin_amdgcn_permlane32_swap(__float_as_uint(v), __float_as_uint(v), false, false); return __builtin_fmaxf(__uint_as_float(r[0]), __uint_as_float(r[1])); }
template <int C> DI float dpp_add(float v) { return v + __uint_as_float(__builtin_amdgcn_update_dpp(0u, __float_as_uint(v), C, 0xf, 0xf, false)); }
DI float sum32(float v) {
  v = dpp_add<0x128>(v); v = dpp_add<0x124>(v); v = dpp_add<0x122>(v); v = dpp_add<0x121>(v);
  auto r = __builtin_amdgcn_permlane16_swap(__float_as_uint(v), __float_as_uint(v), false, false);
  return __uint_as_float(r[0]) + __uint_as_float(r[1]);
}
DI float max3(float a, float b, float c) { return __builtin_fmaxf(__builtin_fmaxf(a, b), c); }
DI v4u pack8(const f32x16& a, int o) { v4u r = {pk2(a[o], a[o + 1]), pk2(a[o + 2], a[o + 3]), pk2(a[o + 4], a[o + 5]), pk2(a[o + 6], a[o + 7])}; return r; }
DI v4u lds_r128(lptr p) { return *(const LAS v4u*)p; }
DI f32x4 lds_r128f(lptr p) { return *(const LAS f32x4*)p; }
DI void lds_w128(lptr p, v4u v) { *(LAS v4u*)p = v; }
DI f32x16 splat16(float x) { f32x16 z;
#pragma unroll
  for (int i = 0; i < 16; ++i) z[i] = x;
  return z; }
DI v4u wfrag(lptr p, int off) {
  const s16x4 a = __builtin_amdgcn_ds_read_tr16_b64_v4i16((LAS s16x4*)(p + off));
  const s16x4 b = __builtin_amdgcn_ds_read_tr16_b64_v4i16((LAS s16x4*)(p + off + 512));
  const v2u x = __builtin_bit_cast(v2u, a), y = __builtin_bit_cast(v2u, b);
  v4u r = {x[0], x[1], y[0], y[1]};
  return r;
}
DI void glds16(const void* base, unsigned voff, unsigned lds_dst) {
  unsigned keep;
  asm volatile("s_mov_b32 %0, m0\n\ts_mov_b32 m0, %3\n\ts_nop 0\n\tglobal_load_lds_dwordx4 %1, %2\n\ts_mov_b32 m0, %0" : "=&s"(keep) : "v"(voff), "s"(base), "s"(lds_dst) : "memory");
}
DI void dma_qkv(const u16* Wj, unsigned voff, unsigned L0, int n) { glds16(Wj + (size_t)(n & 31) * (16 * 1536) + (n >> 5) * 512, voff, L0 + n * 1024); }
template <int PACE> DI void dma_tiled(const unsigned char* wt, int src, unsigned dst, int n0, int n1, int l16) {
#pragma unroll 1
  for (int n = n0; n < n1; ++n) { glds16(wt + (src + n) * 1024, l16, dst + n * 1024); if (PACE) __builtin_amdgcn_s_sleep(PACE); }
}
template <int N> DI void nap() { __builtin_amdgcn_s_sleep(N); }
DI void ctr_arrive(lptr L, int c, int lane) { asm volatile("" ::: "memory"); if (lane == 0) (void)__hip_atomic_fetch_add((LAS unsigned*)(L + L_CTR + 4 * c), 1u, RLX_WG); }
DI void ctr_wait(lptr L, int c, unsigned target) {
  unsigned spins = 0;
  while (__builtin_amdgcn_readfirstlane(__hip_atomic_load((LAS unsigned*)(L + L_CTR + 4 * c), RLX_WG)) < target && ++spins < (1u << 22)) __builtin_amdgcn_s_sleep(1);
  asm volatile("" ::: "memory");
}
DI void poll(unsigned char* ws, int line_off, int slot, bool active, unsigned tag, unsigned zero) {
  gu32* p = (gu32*)(ws + line_off) + slot;
  for (unsigned spins = 0; spins < (1u << 14); ++spins) {
    unsigned v = tag;
    if (active) v = __hip_atomic_fetch_add(p, zero, RLX_AGENT);
    if (__all(v == tag)) break;
  }
  __builtin_amdgcn_fence(__ATOMIC_ACQUIRE, "wavefront");
  asm volatile("" ::: "memory");
}
DI void publish(unsigned char* ws, int word_off, bool active, unsigned tag) {
  DRAIN();
  if (active) (void)__hip_atomic_exchange((gu32*)(ws + word_off), tag, RLX_AGENT);
}
DI void publish_wg(lptr L, int c, unsigned target, int lane, unsigned char* ws, int word_off, bool active, unsigned tag) {
  DRAIN();
  unsigned old = 0;
  if (lane == 0) old = __hip_atomic_fetch_add((LAS unsigned*)(L + L_CTR + 4 * c), 1u, RLX_WG);
  if (__builtin_amdgcn_readfirstlane(old) + 1u == target && active) (void)__hip_atomic_exchange((gu32*)(ws + word_off), tag, RLX_AGENT);
}
DI v4u ld_sc1(__amdgpu_buffer_rsrc_t rs, int voff, int soff) { return __builtin_amdgcn_raw_buffer_load_b128(rs, voff, soff, SC1); }
DI void st_sc1(__amdgpu_buffer_rsrc_t rs, v4u v, int voff, int soff) { __builtin_amdgcn_raw_buffer_store_b128(v, rs, voff + soff, 0, SC1); }

DI void row_stats(float s1, float s2, float& mu, float& rs) {
  const float m = s1 * (1.0f / 512.0f);
  const float var = __builtin_fmaxf(__builtin_fmaf(-m, m, s2 * (1.0f / 512.0f)), 0.f);
  mu = rbf(m);
  rs = __builtin_amdgcn_rcpf(rbf(__builtin_amdgcn_sqrtf(rbf(rbf(var) + 1e-5f))));
}
DI v4u ln_frag(v4u x, float mu, float rs) {
  v4u r;
#pragma unroll
  for (int i = 0; i < 4; ++i) { const unsigned d = pk2(bflo(x[i]) - mu, bfhi(x[i]) - mu); r[i] = pk2(bflo(d) * rs, bfhi(d) * rs); }
  return r;
}
DI void scale_tok(f32x16& a, float rs) {
#pragma unroll
  for (int i = 0; i < 16; ++i) a[i] *= rs;
}
DI void scale_feat(f32x16& a, const f32x4 (&u)[4]) {
#pragma unroll
  for (int i = 0; i < 16; ++i) a[i] *= u[i >> 2][i & 3];
}
DI float phi1(float t) { return (__builtin_fmaxf(t, 0.f) + __builtin_amdgcn_exp2f(__builtin_fminf(t, 0.f) * 1.4426950408889634f)) + 1e-6f; }
DI v4u phi_pack8(const f32x16& a, int o) {
  v4u r;
#pragma unroll
  for (int i = 0; i < 4; ++i) { const unsigned t = pk2(a[o + 2 * i], a[o + 2 * i + 1]); r[i] = pk2(phi1(bflo(t)), phi1(bfhi(t))); }
  return r;
}
#define NEARLY 4
#define PACE2 2
#define NB1 16
#define NB2 15
#define PACE1 1
#define NA1 16
#define NA0 8
#define PFX 12
constexpr int PF = PFX;
DI v4u ld_frag(__amdgpu_buffer_rsrc_t rs, int l16, int base, int s) { return ld_sc1(rs, l16 + (s & 3) * 1024, base + (s >> 2) * 4096); }
DI f32x16 gemm_out(lptr L, lptr p, __amdgpu_buffer_rsrc_t rs, int l16, int base, unsigned wo_target) {
  constexpr int D = 5, P = 16;
  v4u wr[D + 1], xr[P + 1];
#pragma unroll
  for (int s = 0; s < P; ++s) xr[s] = ld_frag(rs, l16, base, s);
  SB();
  ctr_wait(L, C_WO, wo_target);
#pragma unroll
  for (int s = 0; s < D; ++s) wr[s] = wfrag(p, s * 1024);
  SB();
  f32x16 a = splat16(0.f);
#pragma unroll
  for (int s = 0; s < 32; ++s) {
    if (s + P < 32) xr[(s + P) % (P + 1)] = ld_frag(rs, l16, base, s + P);
    if (s + D < 32) wr[(s + D) % (D + 1)] = wfrag(p, (s + D) * 1024);
    a = MFMA(wr[s % (D + 1)], xr[s % (P + 1)], a);
    SB();
  }
  return a;
}
DI float colsum(lptr p) {
  constexpr int D = 5;
  v4u wr[D + 1];
  const v4u ones = {ONE2, ONE2, ONE2, ONE2};
#pragma unroll
  for (int s = 0; s < D; ++s) wr[s] = wfrag(p, s * 1024);
  SB();
  f32x16 a = splat16(0.f);
#pragma unroll
  for (int s = 0; s < 32; ++s) {
    if (s + D < 32) wr[(s + D) % (D + 1)] = wfrag(p, (s + D) * 1024);
    a = MFMA(ones, wr[s % (D + 1)], a);
    SB();
  }
  return a[0];
}
DI float x16_sum(float v) { auto r = __builtin_amdgcn_permlane16_swap(__float_as_uint(v), __float_as_uint(v), false, false); return __uint_as_float(r[0]) + __uint_as_float(r[1]); }
DI v4u split3(float c) { const float hi = rbf(c), mid = rbf(c - hi), lo = (c - hi) - mid; return v4u{pk2(hi, mid), pk2(lo, 0.f), 0u, 0u}; }
template <bool ISB> DI void gemm_kv(v4u (&xf)[32], lptr L, lptr pq, lptr pv, f32x16& aK, f32x16& aV, __amdgpu_buffer_rsrc_t rs, int lane, int base,
                                    v4u& xe, v4u& eq, float& rstd, f32x4 (&rs16)[4], unsigned cs_target, unsigned st_target, int t) {
  constexpr int D = 3;
  const int l16 = lane * 16, r = lane & 31, h = lane >> 5;
  v4u wk[D + 1], wv[D + 1];
#pragma unroll
  for (int s = 0; s < PF; ++s) xf[s] = ld_frag(rs, l16, base, s);
  SB();
  ctr_wait(L, C_CS, cs_target);
  const lptr rp = L + L_RS + (t & 1) * 128 + (t >> 1) * 512, np = L + L_NM + t * 1024, cs = L + L_WX + (ISB ? 384 : 0) + r * 4;
  v4u ek = split3(*(const LAS float*)(cs + 128)), ev = split3(*(const LAS float*)(cs + 256));
  eq = split3(*(const LAS float*)cs);
  PIN(ek); PIN(ev); PIN(eq);
#pragma unroll
  for (int s = 0; s < D; ++s) { wk[s] = wfrag(pq, L_WK + s * 1024); wv[s] = wfrag(pv, s * 1024); }
  SB();
  aK = splat16(0.f); aV = splat16(0.f);
  unsigned cv = 0, nm = 0;
  static_for<32>([&](auto SI) __attribute__((always_inline)) {
    constexpr int s = decltype(SI)::value;
    if constexpr (s + D < 32) wk[(s + D) % (D + 1)] = wfrag(pq, L_WK + (s + D) * 1024);
    if constexpr (s == 24) cv = __hip_atomic_load((LAS unsigned*)(L + L_CTR + 4 * C_ST), RLX_WG);
    if constexpr (s == 28) {
      if (__builtin_amdgcn_readfirstlane(cv) < st_target) ctr_wait(L, C_ST, st_target);
      asm volatile("" ::: "memory");
      nm = *(const LAS unsigned*)(np + r * 4); rstd = *(const LAS float*)(rp + r * 4);
    }
    if constexpr (s == 29 || s == 30) rs16[s - 29] = lds_r128f(rp + h * 16 + (s - 29) * 32);
    if constexpr (s == 31) { nm = h ? 0u : nm; xe = v4u{nm, nm & 0xffffu, 0u, 0u}; aK = ISB ? MFMA(xe, ek, aK) : MFMA(ek, xe, aK); aV = MFMA(xe, ev, aV); }
    aK = ISB ? MFMA(xf[s], wk[s % (D + 1)], aK) : MFMA(wk[s % (D + 1)], xf[s], aK);
    SB();
    if constexpr (s + D < 32) wv[(s + D) % (D + 1)] = wfrag(pv, (s + D) * 1024);
    if constexpr (s + PF < 32) xf[s + PF] = ld_frag(rs, l16, base, s + PF);
    if constexpr (ISB && s + PF == 32) ctr_arrive(L, C_XI, lane);
    if constexpr (s == 29 || s == 30) rs16[s - 27] = lds_r128f(rp + h * 16 + (s - 27) * 32);
    aV = MFMA(xf[s], wv[s % (D + 1)], aV);
    SB();
  });
}
DI f32x16 gemm_q(const v4u (&xf)[32], lptr p, v4u xe, v4u eq) {
  constexpr int D = 5;
  v4u wr[D + 1];
#pragma unroll
  for (int s = 0; s < D; ++s) wr[s] = wfrag(p, s * 1024);
  f32x16 a = MFMA(eq, xe, splat16(0.f));
  SB();
#pragma unroll
  for (int s = 0; s < 32; ++s) {
    if (s + D < 32) wr[(s + D) % (D + 1)] = wfrag(p, (s + D) * 1024);
    a = MFMA(wr[s % (D + 1)], xf[s], a);
    SB();
  }
  return a;
}

constexpr unsigned long long diag_mask(int i) { return ((unsigned long long)(0xffffffffu << ((i & 3) + 8 * (i >> 2) + 4)) << 32) | (0xffffffffu << ((i & 3) + 8 * (i >> 2))); }
template <bool EDGE>
DI f32x16 attention(lptr kp, lptr vp, int vs, lptr mp, v4u qf, int nv) {
  v4u kf[9];
  kf[8] = lds_r128(kp + 8 * 1024);
#pragma unroll
  for (int n = 0; n < 8; ++n) kf[n] = lds_r128(kp + n * 1024);
  v4u va = lds_r128(vp), vb = lds_r128(vp + 512);
  SB();
  f32x16 s[9];
  float m0, m1, m2, m3;
#define SC(n) s[n] = MFMA(kf[n], qf, splat16(EDGE ? ((n) < nv ? -3.0e38f : 0.f) : 0.f))
#define MX(n) do { m0 = max3(m0, s[n][0], s[n][1]); m1 = max3(m1, s[n][2], s[n][3]); m2 = max3(m2, s[n][4], s[n][5]); m3 = max3(m3, s[n][6], s[n][7]); \
    m0 = max3(m0, s[n][8], s[n][9]); m1 = max3(m1, s[n][10], s[n][11]); m2 = max3(m2, s[n][12], s[n][13]); m3 = max3(m3, s[n][14], s[n][15]); } while (0)
#define MG(i) s[0][i] = __builtin_amdgcn_inverse_ballot_w64(diag_mask(i)) ? s[8][i] : s[0][i]
  s[8] = MFMA(kf[8], qf, splat16(0.f)); SC(0); SC(1); SB();
  SC(2); MG(0); MG(1); MG(2); MG(3); MG(4); MG(5); MG(6); MG(7); SB();
  SC(3); MG(8); MG(9); MG(10); MG(11); MG(12); MG(13); MG(14); MG(15); SB();
  SC(4); m0 = max3(s[1][0], s[1][1], s[1][2]); m1 = max3(s[1][3], s[1][4], s[1][5]); m2 = max3(s[1][6], s[1][7], s[1][8]); m3 = max3(s[1][9], s[1][10], s[1][11]);
  m0 = max3(m0, s[1][12], s[1][13]); m1 = max3(m1, s[1][14], s[1][15]); SB();
  SC(5); MX(0); SB();
  SC(6); MX(2); SB();
  SC(7); MX(3); SB();
  const v4u mlo = lds_r128(mp), mhi = lds_r128(mp + 1024);
  const v4u da = lds_r128(vp + 8 * vs), db = lds_r128(vp + 8 * vs + 512);
  MX(4); MX(5); MX(6); MX(7);
#undef SC
#undef MX
#undef MG
  constexpr float C = 0.25f * 1.4426950408889634f;
  const float nm = -C * half_max(__builtin_fmaxf(max3(m0, m1, m2), m3));
  f32x2 C2 = {C, C}, nm2 = {nm, nm};
  f32x16 o = splat16(0.f);
#pragma unroll
  for (int n = 0; n < 8; ++n) {
    v4u na, nb;
    if (n < 7) { vp += vs; na = lds_r128(vp); nb = lds_r128(vp + 512); }
    f32x2 e[8];
#pragma unroll
    for (int i = 0; i < 8; ++i) { const f32x2 v = {s[n][2 * i], s[n][2 * i + 1]}; e[i] = __builtin_elementwise_fma(v, C2, nm2); }
#pragma unroll
    for (int i = 0; i < 8; ++i) PIN(e[i]);
    v4u p0, p1;
#pragma unroll
    for (int i = 0; i < 4; ++i) p0[i] = pk2(__builtin_amdgcn_exp2f(e[i][0]), __builtin_amdgcn_exp2f(e[i][1]));
#pragma unroll
    for (int i = 0; i < 4; ++i) p1[i] = pk2(__builtin_amdgcn_exp2f(e[4 + i][0]), __builtin_amdgcn_exp2f(e[4 + i][1]));
    if (n == 0) {
      const v4u d0 = p0 & mlo, d1 = p1 & mhi;
      o = MFMA(da, d0, o); o = MFMA(va, p0 ^ d0, o); o = MFMA(db, d1, o); o = MFMA(vb, p1 ^ d1, o);
    } else { o = MFMA(va, p0, o); o = MFMA(vb, p1, o); }
    if (n < 7) { va = na; vb = nb; }
    SB();
  }
  return o;
}

__global__ void __launch_bounds__(512, 2) mega(const u16* __restrict__ x_in, const u16* __restrict__ Aw, const u16* __restrict__ Ao,
                                               const u16* __restrict__ Bw, const u16* __restrict__ Bo, u16* __restrict__ out, unsigned char* ws) {
  extern __shared__ __attribute__((aligned(16))) char smem[];
  const lptr L = (lptr)smem;
  const unsigned L0 = (unsigned)(uintptr_t)L;
  int b = blockIdx.x, lane = threadIdx.x & 63, w = __builtin_amdgcn_readfirstlane(threadIdx.x >> 6);
  const __amdgpu_buffer_rsrc_t rs = __builtin_amdgcn_make_buffer_rsrc(ws, 0, WS_END, 0x00020000);
  STAMP(63);
  unsigned zero = 0; PIN(zero);
  unsigned tag;
  f32x16 xres = splat16(0.f);

  {
    const int g = MAP_G(b), j = MAP_J(b), t = w & 3, r = lane & 31, h = lane >> 5;
    unsigned epoch = __hip_atomic_load((gu32*)ws, RLX_AGENT);
    if (w < 4) {
      const unsigned voq = ((lane >> 2) * 1536 + (lane & 3) * 8) * 2;
      if (w == 0) { const v4u z = {0u, 0u, 0u, 0u}; lds_w128(L + L_WX + lane * 16, z); }
      BAR();
#pragma unroll 1
      for (int n = 0; n < 16; ++n) { if (n == NEARLY) ctr_wait(L, C_PX, 4u); dma_qkv(Aw + 32 * j, voq, L0, 32 + 16 * t + n); }
      DRAIN(); ctr_arrive(L, C_W, lane); ctr_wait(L, C_W, 8u);
    } else {
      const u16* xrow = x_in + (size_t)(g * 128 + 8 * j + 2 * t + h) * 512 + 8 * r;
      const v4u xa = *(const GAS v4u*)xrow, xb = *(const GAS v4u*)(xrow + 256);
      const u16* xr = x_in + (size_t)(g * 128 + t * 32 + r) * 512 + 32 * j + 4 * h;
      v2u xv[4];
#pragma unroll
      for (int q = 0; q < 4; ++q) xv[q] = *(const GAS v2u*)(xr + 8 * q);
      BAR();
      { const unsigned fill = (lane & 16) ? 0x3f800000u : ONE2; const v4u o = {fill, fill, fill, fill}; if (w == 4) lds_w128(L + L_ONES + lane * 16, o); }
      if (h == 0) *(LAS unsigned*)(L + L_NM + t * 1024 + r * 4) = 0u;
      if (w >= 6) { v4u mk;
#pragma unroll
        for (int i = 0; i < 4; ++i) { const int key = ((2 * i) & 3) + 8 * (i >> 1) + 16 * (w - 6) + 4 * h; mk[i] = (key + 1 <= r) ? 0xffffffffu : ((key <= r) ? 0xffffu : 0u); }
        lds_w128(L + L_MSK + (w - 6) * 1024 + lane * 16, mk); }
      float s1 = 0.f, s2 = 0.f;
#pragma unroll
      for (int i = 0; i < 4; ++i) { s1 = dot2(xa[i], ONE2, s1); s2 = dot2(xa[i], xa[i], s2); s1 = dot2(xb[i], ONE2, s1); s2 = dot2(xb[i], xb[i], s2); }
      float m0, r0; row_stats(sum32(s1), sum32(s2), m0, r0);
      const v4u na = ln_frag(xa, m0, r0), nb = ln_frag(xb, m0, r0);
      { const lptr d = L + L_LN + (r >> 1) * 256 + (2 * t + h) * 16 + (r & 1) * 8;
        v2u u = {na[0], na[1]}; *(LAS v2u*)d = u; u[0] = na[2]; u[1] = na[3]; *(LAS v2u*)(d + 128) = u;
        u[0] = nb[0]; u[1] = nb[1]; *(LAS v2u*)(d + 4096) = u; u[0] = nb[2]; u[1] = nb[3]; *(LAS v2u*)(d + 4096 + 128) = u; }
      STAMP(56);
      ctr_arrive(L, C_ST, lane); ctr_wait(L, C_ST, 4u);
      const int vo = (lane >> 4) * 1024 + (((lane >> 3) & 1) * 32 + 8 * (j & 3) + (lane & 7)) * 16;
      const int xb0 = WS_X + (g * 4 + (j >> 2)) * 32768 + t * 8192;
      st_sc1(rs, lds_r128(L + L_LN + t * 2048 + lane * 16), vo, xb0);
      st_sc1(rs, lds_r128(L + L_LN + t * 2048 + 1024 + lane * 16), vo, xb0 + 4096);
      PIN(epoch);
      publish_wg(L, C_PX, 4u, lane, ws, WS_FX + (g * 16 + lane) * FL + j * 4, lane < 16, epoch + 1u);
      STAMP(57);
      { const unsigned voq = ((lane >> 2) * 1536 + (lane & 3) * 8) * 2;
#pragma unroll 1
        for (int n = 0; n < 8; ++n) dma_qkv(Aw + 32 * j, voq, L0, 8 * t + n);
        v4u rt[4];
#pragma unroll
        for (int i = 0; i < 4; ++i) {
          const bool po = (i >= 2) && (t & 1);
          const u16* src = po ? (t == 1 ? Ao : Bo) + (32 * g + 16 * (i & 1)) * 512
                              : (t < 2 ? Bw : Aw) + (32 * g + 16 * (i & 1)) * 1536 + (2 * (t & 1) + (i >> 1)) * 512;
          rt[i] = *(const GAS v4u*)(src + 32 * j + (lane >> 2) * (po ? 512 : 1536) + (lane & 3) * 8);
        }
        asm volatile("s_waitcnt vmcnt(4)" ::: "memory"); ctr_arrive(L, C_W, lane);
#pragma unroll
        for (int i = 0; i < 4; ++i) {
          const bool po = (i >= 2) && (t & 1);
          const int piece = (po ? (t == 1 ? T_OA : T_OB) : (t < 2 ? T_WB : T_WA) + 32 * (2 * (t & 1) + (i >> 1))) + 2 * g + (i & 1);
          st_sc1(rs, rt[i], lane * 16, WS_WT + (j * 256 + piece) * 1024);
        }
        publish_wg(L, C_PR, 4u, lane, ws, WS_FR + (lane * 16 + j) * FL + g * 4, lane < 16, epoch + 1u);
        if (t == 3) {
          poll(ws, WS_FR + (g * 16 + j) * FL, lane, lane < 16, __builtin_amdgcn_readfirstlane(epoch) + 1u, zero);
          __builtin_amdgcn_fence(__ATOMIC_ACQUIRE, "agent");
          DRAIN(); ctr_arrive(L, C_R, lane);
        }
        STAMP(58); }
#pragma unroll
      for (int q = 0; q < 4; ++q) { xres[4 * q] = bflo(xv[q][0]); xres[4 * q + 1] = bfhi(xv[q][0]); xres[4 * q + 2] = bflo(xv[q][1]); xres[4 * q + 3] = bfhi(xv[q][1]); }
    }
    PIN(epoch);
    tag = __builtin_amdgcn_readfirstlane(epoch) + 1u;
  }

  for (int pr = 0; pr < 2; ++pr) {
#pragma unroll
    for (int isB = 0; isB < 2; ++isB) {
      const int l = 2 * pr + isB;
      asm volatile("" : "+v"(lane), "+s"(b), "+s"(w));
      const int g = MAP_G(b), j = MAP_J(b);
      const int t = w & 3, hh = w >> 2, r = lane & 31, h = lane >> 5;
      const bool isQ = w < 4;
      const int gt = g * 4 + t, gj = g * 16 + j;
      const int trl = ((lane & 15) >> 2) * 64 + ((lane >> 4) & 1) * 32 + (lane & 3) * 8 + h * 256;
      const int l16 = lane * 16;
      const unsigned char* wt = ws + WS_WT + j * (256 * 1024);
      v4u qf;
      STAMP(l * 14 + 0);
      if (isQ) {
        const lptr pq = L + trl;
        lptr pv = pq + L_WV; PIN(pv);
        STAMP(l * 14 + 1);
        poll(ws, WS_FX + (l * 256 + gj) * FL, lane, lane < 16, tag, zero);
        STAMP(l * 14 + 2);
        const int xb = WS_X + l * MB2 + gt * 32768;
        f32x16 aK, aV; float rstd; f32x4 rs16[4]; v4u xf[32], xe, eq;
        if (isB) gemm_kv<true>(xf, L, pq, pv, aK, aV, rs, lane, xb, xe, eq, rstd, rs16, 3u, 4u * l + 4u, t);
        else gemm_kv<false>(xf, L, pq, pv, aK, aV, rs, lane, xb, xe, eq, rstd, rs16, 0u, 4u * l + 4u, t);
        STAMP(l * 14 + 12);
        if (!isB) {
          scale_tok(aK, rstd);
          const v4u k0 = pack8(aK, 0), k1 = pack8(aK, 8);
          const int kb = WS_KV + ((pr * 256 + gj) * 4 + t) * 4096;
          if (g < 15) { st_sc1(rs, k0, l16, kb); st_sc1(rs, k1, l16 + 1024, kb); }
          scale_feat(aV, rs16);
          const v4u vf0 = pack8(aV, 0), vf1 = pack8(aV, 8);
          if (g < 15) { st_sc1(rs, vf0, l16 + 2048, kb); st_sc1(rs, vf1, l16 + 3072, kb); }
          lds_w128(L + L_K + (8 + t) * 1024 + l16, k0); lds_w128(L + L_K + (20 + t) * 1024 + l16, k1);
          const lptr vd = L + L_V + ((r >> 4) * 12 + 8 + t) * 1024 + h * 256 + (r & 15) * 16;
          lds_w128(vd, vf0); lds_w128(vd + 512, vf1);
          STAMP(l * 14 + 4);
          f32x16 aQ = gemm_q(xf, pq, xe, eq);
          publish_wg(L, C_PH, 4u * pr + 4u, lane, ws, WS_FH + (pr * 256 + gj + 16 * (lane + 1)) * FL + lane * 4, lane < 2 && g + lane < 15, tag);
          STAMP(l * 14 + 5);
          scale_tok(aQ, rstd);
          qf = pack8(aQ, 0); lds_w128(L + L_QF + t * 1024 + l16, pack8(aQ, 8));
          if (l == 0) {
            if (t < 3) { const float c = colsum(pq + t * 32768); if (h == 0) *(LAS float*)(L + L_WX + (t * 32 + r) * 4) = c; }
            ctr_arrive(L, C_CA, lane); ctr_wait(L, C_CA, 4u); ctr_wait(L, C_R, 1u);
          } else ctr_wait(L, C_PH, 4u * pr + 4u);
          dma_tiled<PACE1>(wt, T_WB, L0, 24 * t, 24 * t + (l == 0 ? NA0 : NA1), l16);
          STAMP(l * 14 + 3);
        } else {
          scale_feat(aK, rs16); scale_feat(aV, rs16);
          const v4u k0 = phi_pack8(aK, 0), vf0 = pack8(aV, 0);
          f32x16 d = MFMA(k0, vf0, splat16(0.f));
          const v4u k1 = phi_pack8(aK, 8), vf1 = pack8(aV, 8);
          d = MFMA(k1, vf1, d);
          float z = 0.f;
#pragma unroll
          for (int i = 0; i < 4; ++i) { z = dot2(k0[i], ONE2, z); z = dot2(k1[i], ONE2, z); }
          z = half_sum(z);
          const lptr pw = L + L_PW + t * PSTR;
          if (h == 0) *(LAS float*)(pw + 2048 + r * 4) = z;
          if (r < 16) { *(LAS f32x4*)(pw + l16) = f32x4{d[0], d[1], d[2], d[3]}; *(LAS f32x4*)(pw + 1024 + l16) = f32x4{d[4], d[5], d[6], d[7]}; asm volatile("" ::: "memory"); }
          else { *(LAS f32x4*)(pw + l16) = f32x4{d[8], d[9], d[10], d[11]}; *(LAS f32x4*)(pw + 1024 + l16) = f32x4{d[12], d[13], d[14], d[15]}; }
          ctr_arrive(L, C_KV, lane);
          STAMP(l * 14 + 4);
          f32x16 aQ = gemm_q(xf, pq, xe, eq);
          if (pr == 0) ctr_arrive(L, C_QD, lane);
          scale_tok(aQ, rstd);
          qf = phi_pack8(aQ, 0); lds_w128(L + L_QF + t * 1024 + l16, phi_pack8(aQ, 8));
          STAMP(l * 14 + 5);
          if (pr == 0) {
            ctr_wait(L, C_QD, 4u);
            dma_tiled<PACE2>(wt, T_WA, L0, 24 * t, 24 * t + NB1, l16);
          }
        }
        BAR();
      } else {
        if (l > 0) {
          poll(ws, WS_FX + (l * 256 + gj) * FL, lane, lane < 16, tag, zero);
          f32x4 st[4];
#pragma unroll
          for (int i = 0; i < 4; ++i) st[i] = __builtin_bit_cast(f32x4, ld_sc1(rs, l16 + i * 1024, WS_XS + (l * 64 + gt) * 4096));
          const f32x4 a = (st[0] + st[1]) + (st[2] + st[3]);
          float m0, r0, m1, r1;
          row_stats(half_sum(x16_sum(a[0])), half_sum(x16_sum(a[1])), m0, r0);
          row_stats(half_sum(x16_sum(a[2])), half_sum(x16_sum(a[3])), m1, r1);
          if (lane < 16) { *(LAS v2u*)(L + L_NM + t * 1024 + lane * 8) = v2u{pk2(-m0, -m0), pk2(-m1, -m1)};
            *(LAS f32x2*)(L + L_RS + (t & 1) * 128 + (t >> 1) * 512 + lane * 8) = f32x2{r0, r1}; }
          ctr_arrive(L, C_ST, lane);
        }
        STAMP(l * 14 + 13);
        if (isB) {
          ctr_wait(L, C_OP, 4u * l);
          ctr_wait(L, C_XI, 4u * pr + 4u);
          dma_tiled<0>(wt, T_OB, L0 + L_WO, 8 * t, 8 * t + 8, l16);
        }
        STAMP(l * 14 + 1);
        if (!isB) {
          v4u kv[8];
          poll(ws, WS_FH + (pr * 256 + gj) * FL, lane, lane < 2 && lane < g, tag, zero);
          STAMP(l * 14 + 2);
          const v4u z4 = {0u, 0u, 0u, 0u};
#pragma unroll
          for (int q = 0; q < 2; ++q) {
            const int kb = WS_KV + ((pr * 256 + gj - 16 * (q + 1)) * 4 + t) * 4096;
            if (q < g) {
#pragma unroll
              for (int i = 0; i < 4; ++i) kv[4 * q + i] = ld_sc1(rs, l16 + i * 1024, kb);
            } else {
#pragma unroll
              for (int i = 0; i < 4; ++i) kv[4 * q + i] = z4;
            }
          }
          ctr_wait(L, C_OP, 4u * l);
#pragma unroll
          for (int q = 0; q < 2; ++q) {
            const int ti = 4 - 4 * q + t;
            lds_w128(L + L_K + ti * 1024 + l16, kv[4 * q]); lds_w128(L + L_K + (12 + ti) * 1024 + l16, kv[4 * q + 1]);
            const lptr vd = L + L_V + ((r >> 4) * 12 + ti) * 1024 + h * 256 + (r & 15) * 16;
            lds_w128(vd, kv[4 * q + 2]); lds_w128(vd + 512, kv[4 * q + 3]);
          }
          STAMP(l * 14 + 5);
          BAR();
        } else {
          if (t == 0) {
            ctr_wait(L, C_KV, 4u * pr + 4u);
            STAMP(l * 14 + 4);
            f32x4 u0 = lds_r128f(L + L_PW + l16), u1 = lds_r128f(L + L_PW + 1024 + l16);
            float z = *(const LAS float*)(L + L_PW + 2048 + r * 4);
#pragma unroll
            for (int q = 1; q < 4; ++q) { u0 += lds_r128f(L + L_PW + q * PSTR + l16); u1 += lds_r128f(L + L_PW + q * PSTR + 1024 + l16); z += *(const LAS float*)(L + L_PW + q * PSTR + 2048 + r * 4); }
            const int pb = WS_P + ((pr * 16 + j) * 16 + g) * PSTR;
            st_sc1(rs, __builtin_bit_cast(v4u, u0), l16, pb); st_sc1(rs, __builtin_bit_cast(v4u, u1), l16 + 1024, pb);
            if (h == 0) __builtin_amdgcn_raw_buffer_store_b32(__float_as_uint(z), rs, 2048 + r * 4, pb, SC1);
            publish(ws, WS_FP + (pr * 256 + lane * 16 + j) * FL + g * 4, lane < 16, tag);
            STAMP(l * 14 + 3);
          }
          poll(ws, WS_FP + (pr * 256 + gj) * FL, 4 * t + lane, lane < 4, tag, zero);
          STAMP(l * 14 + 2);
          v4u a[8]; unsigned zz[4];
#pragma unroll
          for (int q = 0; q < 4; ++q) { const int pb = WS_P + ((pr * 16 + j) * 16 + 4 * t + q) * PSTR;
            a[2 * q] = ld_sc1(rs, l16, pb); a[2 * q + 1] = ld_sc1(rs, l16 + 1024, pb); zz[q] = __builtin_amdgcn_raw_buffer_load_b32(rs, 2048 + r * 4, pb, SC1); }
          SB();
          f32x4 u0 = __builtin_bit_cast(f32x4, a[0]), u1 = __builtin_bit_cast(f32x4, a[1]); float z = __uint_as_float(zz[0]);
#pragma unroll
          for (int q = 1; q < 4; ++q) { u0 += __builtin_bit_cast(f32x4, a[2 * q]); u1 += __builtin_bit_cast(f32x4, a[2 * q + 1]); z += __uint_as_float(zz[q]); }
          const lptr pw = L + L_PR + t * PSTR;
          *(LAS f32x4*)(pw + l16) = u0; *(LAS f32x4*)(pw + 1024 + l16) = u1;
          if (h == 0) *(LAS float*)(pw + 2048 + r * 4) = z;
          DRAIN();
          STAMP(l * 14 + 5);
          BAR();
        }
        qf = lds_r128(L + L_QF + t * 1024 + l16);
      }

      STAMP(l * 14 + 6);
      f32x16 o;
      if (!isB) {

        const lptr kp = L + L_K + (hh * 12 + t) * 1024 + l16;
        const lptr vp = (r < 16 ? L + L_V + (hh * 12 + t) * 1024 + h * 256 : L + L_ONES) + (r & 15) * 16;
        const int vs = r < 16 ? 1024 : 0;
        if (g >= 2) o = attention<false>(kp, vp, vs, L + L_MSK + l16, qf, 0);
        else o = attention<true>(kp, vp, vs, L + L_MSK + l16, qf, 8 - 4 * g - t);
      } else {
        const lptr a0 = L + L_PR + (r < 16 ? (hh * 16 + r + 32 * h) * 16 : 2048 + (hh * 16 + 4 * h) * 4);
        const lptr a1 = a0 + (r < 16 ? 1024 : 32);
        f32x4 u0 = lds_r128f(a0), u1 = lds_r128f(a1);
#pragma unroll
        for (int q = 1; q < 4; ++q) { u0 += lds_r128f(a0 + q * PSTR); u1 += lds_r128f(a1 + q * PSTR); }
        const v4u af = {pk2(u0[0], u0[1]), pk2(u0[2], u0[3]), pk2(u1[0], u1[1]), pk2(u1[2], u1[3])};
        o = MFMA(af, qf, splat16(0.f));
        o[8] += 1e-6f;
      }
      { const float ri = __builtin_amdgcn_rcpf(o[8]);
#pragma unroll
        for (int i = 0; i < 8; ++i) o[i] *= ri;
        asm volatile("" :: "v"(o[0]));
        STAMP(l * 14 + 7);
        st_sc1(rs, pack8(o, 0), l16, WS_O + l * MB2 + gt * 32768 + (2 * j + hh) * 1024);
        publish_wg(L, C_PO, 8u * l + 8u, lane, ws, WS_FO + (l * 256 + g * 16 + lane) * FL + j * 4, lane < 16, tag);
      }
      STAMP(l * 14 + 8);

      if (isQ) {
        if (!isB) {
          dma_tiled<0>(wt, T_WB, L0, 24 * t + (l == 0 ? NA0 : NA1), 24 * t + 24, l16);
          ctr_wait(L, C_PO, 8u * l + 8u);
          dma_tiled<0>(wt, T_OA, L0 + L_WO, 8 * t, 8 * t + 8, l16);
          DRAIN(); ctr_arrive(L, C_WO, lane);
        } else if (pr == 0) dma_tiled<PACE2>(wt, T_WA, L0, 24 * t + NB1, 24 * t + 24, l16);
        STAMP(l * 14 + 9);
        if (l < 3) { DRAIN(); ctr_arrive(L, C_W, lane); ctr_wait(L, C_W, 4u * l + 12u); }
        if (l == 0 && t < 3) {
          const float c = colsum(L + trl + t * 32768);
          if (h == 0) *(LAS float*)(L + L_WX + 384 + (t * 32 + r) * 4) = c;
          ctr_arrive(L, C_CS, lane);
        }
      } else {
        poll(ws, WS_FO + (l * 256 + gj) * FL, lane, lane < 16, tag, zero);
        STAMP(l * 14 + 9);
        lptr po = L + L_WO + trl; PIN(po);
        const f32x16 y = gemm_out(L, po, rs, l16, WS_O + l * MB2 + gt * 32768, 4u * pr + 4u);
        ctr_arrive(L, C_OP, lane);
        asm volatile("" :: "v"(y[0]));
        STAMP(l * 14 + 10);
        v4u x0, x1; float s1 = 0.f, s2 = 0.f;
#pragma unroll
        for (int i = 0; i < 8; ++i) {
          const unsigned yb = pk2(y[2 * i], y[2 * i + 1]);
          const unsigned xb = pk2(xres[2 * i] + bflo(yb), xres[2 * i + 1] + bfhi(yb));
          xres[2 * i] = bflo(xb); xres[2 * i + 1] = bfhi(xb);
          s1 = dot2(xb, ONE2, s1); s2 = dot2(xb, xb, s2);
          if (i < 4) x0[i] = xb; else x1[i - 4] = xb;
        }
        if (l < 3) {
          const int xb = WS_X + (l + 1) * MB2 + gt * 32768 + j * 2048;
          st_sc1(rs, x0, l16, xb); st_sc1(rs, x1, l16 + 1024, xb);
          s1 = half_sum(s1); s2 = half_sum(s2);
          if (h == 0) { v2u v = {__float_as_uint(s1), __float_as_uint(s2)}; __builtin_amdgcn_raw_buffer_store_b64(v, rs, r * 8, WS_XS + (((l + 1) * 64 + gt) * 16 + j) * 256, SC1); }
          publish_wg(L, C_PX, 4u * l + 8u, lane, ws, WS_FX + ((l + 1) * 256 + g * 16 + lane) * FL + j * 4, lane < 16, tag);
        } else {
          u16* op = out + (size_t)(gt * 32 + r) * 512 + 32 * j + 4 * h;
          v2u a = {x0[0], x0[1]}, c = {x0[2], x0[3]}, d = {x1[0], x1[1]}, e = {x1[2], x1[3]};
          *(GAS v2u*)(op) = a; *(GAS v2u*)(op + 8) = c; *(GAS v2u*)(op + 16) = d; *(GAS v2u*)(op + 24) = e;
          if (b == 0 && w == 4 && lane == 0) __hip_atomic_store((gu32*)ws, tag, RLX_AGENT);
        }
      }
      STAMP(l * 14 + 11);
    }
  }
}

extern "C" void kernel_launch(void* const* d_in, const int* in_sizes, int n_in, void* d_out, int out_size, void* d_ws, size_t ws_size,
                              hipStream_t stream) {
  (void)in_sizes; (void)n_in; (void)out_size; (void)ws_size;
  (void)hipFuncSetAttribute((const void*)mega, hipFuncAttributeMaxDynamicSharedMemorySize, LDS_BYTES);
  mega<<<dim3(256), dim3(512), LDS_BYTES, stream>>>((const u16*)d_in[0], (const u16*)d_in[1], (const u16*)d_in[2], (const u16*)d_in[4],
                                                    (const u16*)d_in[5], (u16*)d_out, (unsigned char*)d_ws);
}
```

```cpp
#include <hip/hip_runtime.h>
#include <hip/hip_bf16.h>
#include <stdint.h>

#define DI __device__ __forceinline__
#define LAS __attribute__((address_space(3)))
#define GAS __attribute__((address_space(1)))
typedef unsigned short u16;
typedef unsigned v4u __attribute__((ext_vector_type(4)));
typedef unsigned v2u __attribute__((ext_vector_type(2)));
typedef float f32x16 __attribute__((ext_vector_type(16)));
typedef float f32x4 __attribute__((ext_vector_type(4)));
typedef float f32x2 __attribute__((ext_vector_type(2)));
typedef short bf16x8 __attribute__((ext_vector_type(8)));
typedef short s16x4 __attribute__((ext_vector_type(4)));
typedef __bf16 bf2 __attribute__((ext_vector_type(2)));
typedef GAS unsigned gu32;
typedef LAS char* lptr;
#define RLX_AGENT __ATOMIC_RELAXED, __HIP_MEMORY_SCOPE_AGENT
#define RLX_WG __ATOMIC_RELAXED, __HIP_MEMORY_SCOPE_WORKGROUP
#define MFMA(a, b, c) __builtin_amdgcn_mfma_f32_32x32x16_bf16(__builtin_bit_cast(bf16x8, (a)), __builtin_bit_cast(bf16x8, (b)), (c), 0, 0, 0)
#define SB() __builtin_amdgcn_sched_barrier(0)
#define PIN(x) asm volatile("" : "+v"(x))
#define DRAIN() asm volatile("s_waitcnt vmcnt(0)" ::: "memory")
#define LGKM0() asm volatile("s_waitcnt lgkmcnt(0)" ::: "memory")
#define BAR() asm volatile("s_waitcnt lgkmcnt(0)\n\ts_barrier" ::: "memory")
constexpr int SC1 = 16;
constexpr unsigned ONE2 = 0x3f803f80u;
#define STAMP(i) do {} while (0)

#define XMAP 1
#define MAP_G(b) (4 * (((b) & 7) >> 1) + ((b) >> 6))
#define MAP_J(b) (8 * ((b) & 1) + (((b) >> 3) & 7))
constexpr int FL = 256;
constexpr int MB2 = 2 << 20;
constexpr int WS_FX = 4096;
constexpr int WS_FO = WS_FX + 4 * 256 * FL;
constexpr int WS_FH = WS_FO + 4 * 256 * FL;
constexpr int WS_FP = WS_FH + 2 * 256 * FL;
constexpr int WS_FR = WS_FP + 2 * 256 * FL;
constexpr int WS_XS = WS_FR + 256 * FL;
constexpr int PSTR = 2304;
constexpr int WS_P = WS_XS + 4 * 1024 * 256;
constexpr int WS_X = 6 << 20;
constexpr int WS_O = WS_X + 4 * MB2;
constexpr int WS_KV = WS_O + 4 * MB2;
constexpr int WS_WT = WS_KV + 4 * MB2;
constexpr int WS_END = WS_WT + 2 * MB2;
constexpr int T_WB = 0, T_WA = 96, T_OA = 192, T_OB = 224;
static_assert(WS_P + 2 * 256 * PSTR <= WS_X, "ws map");

constexpr int L_WQ = 0, L_WK = 32768, L_WV = 65536, L_WO = 98304, L_K = 131072, L_V = L_WO + 8192, L_MISC = 155648;
constexpr int L_QF = L_MISC, L_MSK = L_MISC + 4096, L_ONES = L_MISC + 6144;
constexpr int L_RS = L_ONES + 256;
constexpr int L_WX = L_MISC + 7168, L_MU = L_WX + 768, L_CTR = L_MU + 192;
constexpr int L_NM = L_QF;
constexpr int L_LN = L_K;
constexpr int L_PW = L_K, L_PR = L_K + 4 * PSTR;
constexpr int LDS_BYTES = 163840;
enum { C_W = 0, C_CS = 1, C_CA = 2, C_KV = 3, C_OP = 4, C_P = 5, C_PX = 6, C_PO = 7, C_PH = 8, C_PR = 9, C_R = 10, C_WO = 11, C_XI = 12, C_ST = 13, C_QD = 14 };

template <int I> struct IC { static constexpr int value = I; };
template <int N, int I = 0, class F> DI void static_for(F&& f) { if constexpr (I < N) { f(IC<I>{}); static_for<N, I + 1>(f); } }
DI unsigned pk2(float lo, float hi) { f32x2 v = {lo, hi}; return __builtin_bit_cast(unsigned, __builtin_convertvector(v, bf2)); }
DI float bflo(unsigned u) { return __uint_as_float(u << 16); }
DI float bfhi(unsigned u) { return __uint_as_float(u & 0xffff0000u); }
DI float rbf(float x) { return bflo(pk2(x, 0.f)); }
DI float dot2(unsigned a, unsigned b, float c) { return __builtin_amdgcn_fdot2_f32_bf16(__builtin_bit_cast(bf2, a), __builtin_bit_cast(bf2, b), c, false); }
DI float half_sum(float v) { auto r = __builtin_amdgcn_permlane32_swap(__float_as_uint(v), __float_as_uint(v), false, false); return __uint_as_float(r[0]) + __uint_as_float(r[1]); }
DI float half_max(float v) { auto r = __builtin_amdgcn_permlane32_swap(__float_as_uint(v), __float_as_uint(v), false, false); return __builtin_fmaxf(__uint_as_float(r[0]), __uint_as_float(r[1])); }
template <int C> DI float dpp_add(float v) { return v + __uint_as_float(__builtin_amdgcn_update_dpp(0u, __float_as_uint(v), C, 0xf, 0xf, false)); }
DI float sum32(float v) {
  v = dpp_add<0x128>(v); v = dpp_add<0x124>(v); v = dpp_add<0x122>(v); v = dpp_add<0x121>(v);
  auto r = __builtin_amdgcn_permlane16_swap(__float_as_uint(v), __float_as_uint(v), false, false);
  return __uint_as_float(r[0]) + __uint_as_float(r[1]);
}
DI float max3(float a, float b, float c) { return __builtin_fmaxf(__builtin_fmaxf(a, b), c); }
DI v4u pack8(const f32x16& a, int o) { v4u r = {pk2(a[o], a[o + 1]), pk2(a[o + 2], a[o + 3]), pk2(a[o + 4], a[o + 5]), pk2(a[o + 6], a[o + 7])}; return r; }
DI v4u lds_r128(lptr p) { return *(const LAS v4u*)p; }
DI f32x4 lds_r128f(lptr p) { return *(const LAS f32x4*)p; }
DI void lds_w128(lptr p, v4u v) { *(LAS v4u*)p = v; }
DI f32x16 splat16(float x) { f32x16 z;
#pragma unroll
  for (int i = 0; i < 16; ++i) z[i] = x;
  return z; }
DI v4u wfrag(lptr p, int off) {
  const s16x4 a = __builtin_amdgcn_ds_read_tr16_b64_v4i16((LAS s16x4*)(p + off));
  const s16x4 b = __builtin_amdgcn_ds_read_tr16_b64_v4i16((LAS s16x4*)(p + off + 512));
  const v2u x = __builtin_bit_cast(v2u, a), y = __builtin_bit_cast(v2u, b);
  v4u r = {x[0], x[1], y[0], y[1]};
  return r;
}
DI void glds16(const void* base, unsigned voff, unsigned lds_dst) {
  unsigned keep;
  asm volatile("s_mov_b32 %0, m0\n\ts_mov_b32 m0, %3\n\ts_nop 0\n\tglobal_load_lds_dwordx4 %1, %2\n\ts_mov_b32 m0, %0" : "=&s"(keep) : "v"(voff), "s"(base), "s"(lds_dst) : "memory");
}
DI void dma_qkv(const u16* Wj, unsigned voff, unsigned L0, int n) { glds16(Wj + (size_t)(n & 31) * (16 * 1536) + (n >> 5) * 512, voff, L0 + n * 1024); }
template <int PACE> DI void dma_tiled(const unsigned char* wt, int src, unsigned dst, int n0, int n1, int l16) {
#pragma unroll 1
  for (int n = n0; n < n1; ++n) { glds16(wt + (src + n) * 1024, l16, dst + n * 1024); if (PACE) __builtin_amdgcn_s_sleep(PACE); }
}
template <int N> DI void nap() { __builtin_amdgcn_s_sleep(N); }
DI void ctr_arrive(lptr L, int c, int lane) { asm volatile("" ::: "memory"); if (lane == 0) (void)__hip_atomic_fetch_add((LAS unsigned*)(L + L_CTR + 4 * c), 1u, RLX_WG); }
DI void ctr_wait(lptr L, int c, unsigned target) {
  unsigned spins = 0;
  while (__builtin_amdgcn_readfirstlane(__hip_atomic_load((LAS unsigned*)(L + L_CTR + 4 * c), RLX_WG)) < target && ++spins < (1u << 22)) __builtin_amdgcn_s_sleep(1);
  asm volatile("" ::: "memory");
}
DI void poll(unsigned char* ws, int line_off, int slot, bool active, unsigned tag, unsigned zero) {
  gu32* p = (gu32*)(ws + line_off) + slot;
  for (unsigned spins = 0; spins < (1u << 14); ++spins) {
    unsigned v = tag;
    if (active) v = __hip_atomic_fetch_add(p, zero, RLX_AGENT);
    if (__all(v == tag)) break;
  }
  __builtin_amdgcn_fence(__ATOMIC_ACQUIRE, "wavefront");
  asm volatile("" ::: "memory");
}
DI void publish(unsigned char* ws, int word_off, bool active, unsigned tag) {
  DRAIN();
  if (active) (void)__hip_atomic_exchange((gu32*)(ws + word_off), tag, RLX_AGENT);
}
DI void publish_wg(lptr L, int c, unsigned target, int lane, unsigned char* ws, int word_off, bool active, unsigned tag) {
  DRAIN();
  unsigned old = 0;
  if (lane == 0) old = __hip_atomic_fetch_add((LAS unsigned*)(L + L_CTR + 4 * c), 1u, RLX_WG);
  if (__builtin_amdgcn_readfirstlane(old) + 1u == target && active) (void)__hip_atomic_exchange((gu32*)(ws + word_off), tag, RLX_AGENT);
}
DI v4u ld_sc1(__amdgpu_buffer_rsrc_t rs, int voff, int soff) { return __builtin_amdgcn_raw_buffer_load_b128(rs, voff, soff, SC1); }
DI void st_sc1(__amdgpu_buffer_rsrc_t rs, v4u v, int voff, int soff) { __builtin_amdgcn_raw_buffer_store_b128(v, rs, voff + soff, 0, SC1); }

DI void row_stats(float s1, float s2, float& mu, float& rs) {
  const float m = s1 * (1.0f / 512.0f);
  const float var = __builtin_fmaxf(__builtin_fmaf(-m, m, s2 * (1.0f / 512.0f)), 0.f);
  mu = rbf(m);
  rs = __builtin_amdgcn_rcpf(rbf(__builtin_amdgcn_sqrtf(rbf(rbf(var) + 1e-5f))));
}
DI v4u ln_frag(v4u x, float mu, float rs) {
  v4u r;
#pragma unroll
  for (int i = 0; i < 4; ++i) { const unsigned d = pk2(bflo(x[i]) - mu, bfhi(x[i]) - mu); r[i] = pk2(bflo(d) * rs, bfhi(d) * rs); }
  return r;
}
DI void scale_tok(f32x16& a, float rs) {
#pragma unroll
  for (int i = 0; i < 16; ++i) a[i] *= rs;
}
DI void scale_feat(f32x16& a, const f32x4 (&u)[4]) {
#pragma unroll
  for (int i = 0; i < 16; ++i) a[i] *= u[i >> 2][i & 3];
}
DI float phi1(float t) { return (__builtin_fmaxf(t, 0.f) + __builtin_amdgcn_exp2f(__builtin_fminf(t, 0.f) * 1.4426950408889634f)) + 1e-6f; }
DI v4u phi_pack8(const f32x16& a, int o) {
  v4u r;
#pragma unroll
  for (int i = 0; i < 4; ++i) { const unsigned t = pk2(a[o + 2 * i], a[o + 2 * i + 1]); r[i] = pk2(phi1(bflo(t)), phi1(bfhi(t))); }
  return r;
}
#define WARMUP 2
#define NEARLY 4
#define PACE2 2
#define NB1 16
#define NB2 15
#define PACE1 1
#define NA1 16
#define NA0 8
#define PFX 12
constexpr int PF = PFX;
DI v4u ld_frag(__amdgpu_buffer_rsrc_t rs, int l16, int base, int s) { return ld_sc1(rs, l16 + (s & 3) * 1024, base + (s >> 2) * 4096); }
DI f32x16 gemm_out(lptr L, lptr p, __amdgpu_buffer_rsrc_t rs, int l16, int base, unsigned wo_target) {
  constexpr int D = 5, P = 16;
  v4u wr[D + 1], xr[P + 1];
#pragma unroll
  for (int s = 0; s < P; ++s) xr[s] = ld_frag(rs, l16, base, s);
  SB();
  ctr_wait(L, C_WO, wo_target);
#pragma unroll
  for (int s = 0; s < D; ++s) wr[s] = wfrag(p, s * 1024);
  SB();
  f32x16 a = splat16(0.f);
#pragma unroll
  for (int s = 0; s < 32; ++s) {
    if (s + P < 32) xr[(s + P) % (P + 1)] = ld_frag(rs, l16, base, s + P);
    if (s + D < 32) wr[(s + D) % (D + 1)] = wfrag(p, (s + D) * 1024);
    a = MFMA(wr[s % (D + 1)], xr[s % (P + 1)], a);
    SB();
  }
  return a;
}
DI float colsum(lptr p) {
  constexpr int D = 5;
  v4u wr[D + 1];
  const v4u ones = {ONE2, ONE2, ONE2, ONE2};
#pragma unroll
  for (int s = 0; s < D; ++s) wr[s] = wfrag(p, s * 1024);
  SB();
  f32x16 a = splat16(0.f);
#pragma unroll
  for (int s = 0; s < 32; ++s) {
    if (s + D < 32) wr[(s + D) % (D + 1)] = wfrag(p, (s + D) * 1024);
    a = MFMA(ones, wr[s % (D + 1)], a);
    SB();
  }
  return a[0];
}
DI float x16_sum(float v) { auto r = __builtin_amdgcn_permlane16_swap(__float_as_uint(v), __float_as_uint(v), false, false); return __uint_as_float(r[0]) + __uint_as_float(r[1]); }
DI v4u split3(float c) { const float hi = rbf(c), mid = rbf(c - hi), lo = (c - hi) - mid; return v4u{pk2(hi, mid), pk2(lo, 0.f), 0u, 0u}; }
template <bool ISB> DI void gemm_kv(v4u (&xf)[32], lptr L, lptr pq, lptr pv, f32x16& aK, f32x16& aV, __amdgpu_buffer_rsrc_t rs, int lane, int base,
                                    v4u& xe, v4u& eq, float& rstd, f32x4 (&rs16)[4], unsigned cs_target, unsigned st_target, int t) {
  constexpr int D = 3;
  const int l16 = lane * 16, r = lane & 31, h = lane >> 5;
  v4u wk[D + 1], wv[D + 1];
#pragma unroll
  for (int s = 0; s < PF; ++s) xf[s] = ld_frag(rs, l16, base, s);
  SB();
  ctr_wait(L, C_CS, cs_target);
  const lptr rp = L + L_RS + (t & 1) * 128 + (t >> 1) * 512, np = L + L_NM + t * 1024, cs = L + L_WX + (ISB ? 384 : 0) + r * 4;
  v4u ek = split3(*(const LAS float*)(cs + 128)), ev = split3(*(const LAS float*)(cs + 256));
  eq = split3(*(const LAS float*)cs);
  PIN(ek); PIN(ev); PIN(eq);
#pragma unroll
  for (int s = 0; s < D; ++s) { wk[s] = wfrag(pq, L_WK + s * 1024); wv[s] = wfrag(pv, s * 1024); }
  SB();
  aK = splat16(0.f); aV = splat16(0.f);
  unsigned cv = 0, nm = 0;
  static_for<32>([&](auto SI) __attribute__((always_inline)) {
    constexpr int s = decltype(SI)::value;
    if constexpr (s + D < 32) wk[(s + D) % (D + 1)] = wfrag(pq, L_WK + (s + D) * 1024);
    if constexpr (s == 24) cv = __hip_atomic_load((LAS unsigned*)(L + L_CTR + 4 * C_ST), RLX_WG);
    if constexpr (s == 28) {
      if (__builtin_amdgcn_readfirstlane(cv) < st_target) ctr_wait(L, C_ST, st_target);
      asm volatile("" ::: "memory");
      nm = *(const LAS unsigned*)(np + r * 4); rstd = *(const LAS float*)(rp + r * 4);
    }
    if constexpr (s == 29 || s == 30) rs16[s - 29] = lds_r128f(rp + h * 16 + (s - 29) * 32);
    if constexpr (s == 31) { nm = h ? 0u : nm; xe = v4u{nm, nm & 0xffffu, 0u, 0u}; aK = ISB ? MFMA(xe, ek, aK) : MFMA(ek, xe, aK); aV = MFMA(xe, ev, aV); }
    aK = ISB ? MFMA(xf[s], wk[s % (D + 1)], aK) : MFMA(wk[s % (D + 1)], xf[s], aK);
    SB();
    if constexpr (s + D < 32) wv[(s + D) % (D + 1)] = wfrag(pv, (s + D) * 1024);
    if constexpr (s + PF < 32) xf[s + PF] = ld_frag(rs, l16, base, s + PF);
    if constexpr (ISB && s + PF == 32) ctr_arrive(L, C_XI, lane);
    if constexpr (s == 29 || s == 30) rs16[s - 27] = lds_r128f(rp + h * 16 + (s - 27) * 32);
    aV = MFMA(xf[s], wv[s % (D + 1)], aV);
    SB();
  });
}
DI f32x16 gemm_q(const v4u (&xf)[32], lptr p, v4u xe, v4u eq) {
  constexpr int D = 5;
  v4u wr[D + 1];
#pragma unroll
  for (int s = 0; s < D; ++s) wr[s] = wfrag(p, s * 1024);
  f32x16 a = MFMA(eq, xe, splat16(0.f));
  SB();
#pragma unroll
  for (int s = 0; s < 32; ++s) {
    if (s + D < 32) wr[(s + D) % (D + 1)] = wfrag(p, (s + D) * 1024);
    a = MFMA(wr[s % (D + 1)], xf[s], a);
    SB();
  }
  return a;
}

constexpr unsigned long long diag_mask(int i) { return ((unsigned long long)(0xffffffffu << ((i & 3) + 8 * (i >> 2) + 4)) << 32) | (0xffffffffu << ((i & 3) + 8 * (i >> 2))); }
template <bool EDGE>
DI f32x16 attention(lptr kp, lptr vp, int vs, lptr mp, v4u qf, int nv) {
  v4u kf[9];
  kf[8] = lds_r128(kp + 8 * 1024);
#pragma unroll
  for (int n = 0; n < 8; ++n) kf[n] = lds_r128(kp + n * 1024);
  v4u va = lds_r128(vp), vb = lds_r128(vp + 512);
  SB();
  f32x16 s[9];
  float m0, m1, m2, m3;
#define SC(n) s[n] = MFMA(kf[n], qf, splat16(EDGE ? ((n) < nv ? -3.0e38f : 0.f) : 0.f))
#define MX(n) do { m0 = max3(m0, s[n][0], s[n][1]); m1 = max3(m1, s[n][2], s[n][3]); m2 = max3(m2, s[n][4], s[n][5]); m3 = max3(m3, s[n][6], s[n][7]); \
    m0 = max3(m0, s[n][8], s[n][9]); m1 = max3(m1, s[n][10], s[n][11]); m2 = max3(m2, s[n][12], s[n][13]); m3 = max3(m3, s[n][14], s[n][15]); } while (0)
#define MG(i) s[0][i] = __builtin_amdgcn_inverse_ballot_w64(diag_mask(i)) ? s[8][i] : s[0][i]
  s[8] = MFMA(kf[8], qf, splat16(0.f)); SC(0); SC(1); SB();
  SC(2); MG(0); MG(1); MG(2); MG(3); MG(4); MG(5); MG(6); MG(7); SB();
  SC(3); MG(8); MG(9); MG(10); MG(11); MG(12); MG(13); MG(14); MG(15); SB();
  SC(4); m0 = max3(s[1][0], s[1][1], s[1][2]); m1 = max3(s[1][3], s[1][4], s[1][5]); m2 = max3(s[1][6], s[1][7], s[1][8]); m3 = max3(s[1][9], s[1][10], s[1][11]);
  m0 = max3(m0, s[1][12], s[1][13]); m1 = max3(m1, s[1][14], s[1][15]); SB();
  SC(5); MX(0); SB();
  SC(6); MX(2); SB();
  SC(7); MX(3); SB();
  const v4u mlo = lds_r128(mp), mhi = lds_r128(mp + 1024);
  const v4u da = lds_r128(vp + 8 * vs), db = lds_r128(vp + 8 * vs + 512);
  MX(4); MX(5); MX(6); MX(7);
#undef SC
#undef MX
#undef MG
  constexpr float C = 0.25f * 1.4426950408889634f;
  const float nm = -C * half_max(__builtin_fmaxf(max3(m0, m1, m2), m3));
  f32x2 C2 = {C, C}, nm2 = {nm, nm};
  f32x16 o = splat16(0.f);
#pragma unroll
  for (int n = 0; n < 8; ++n) {
    v4u na, nb;
    if (n < 7) { vp += vs; na = lds_r128(vp); nb = lds_r128(vp + 512); }
    f32x2 e[8];
#pragma unroll
    for (int i = 0; i < 8; ++i) { const f32x2 v = {s[n][2 * i], s[n][2 * i + 1]}; e[i] = __builtin_elementwise_fma(v, C2, nm2); }
#pragma unroll
    for (int i = 0; i < 8; ++i) PIN(e[i]);
    v4u p0, p1;
#pragma unroll
    for (int i = 0; i < 4; ++i) p0[i] = pk2(__builtin_amdgcn_exp2f(e[i][0]), __builtin_amdgcn_exp2f(e[i][1]));
#pragma unroll
    for (int i = 0; i < 4; ++i) p1[i] = pk2(__builtin_amdgcn_exp2f(e[4 + i][0]), __builtin_amdgcn_exp2f(e[4 + i][1]));
    if (n == 0) {
      const v4u d0 = p0 & mlo, d1 = p1 & mhi;
      o = MFMA(da, d0, o); o = MFMA(va, p0 ^ d0, o); o = MFMA(db, d1, o); o = MFMA(vb, p1 ^ d1, o);
    } else { o = MFMA(va, p0, o); o = MFMA(vb, p1, o); }
    if (n < 7) { va = na; vb = nb; }
    SB();
  }
  return o;
}

__global__ void __launch_bounds__(512, 2) mega(const u16* __restrict__ x_in, const u16* __restrict__ Aw, const u16* __restrict__ Ao,
                                               const u16* __restrict__ Bw, const u16* __restrict__ Bo, u16* __restrict__ out, unsigned char* ws) {
  extern __shared__ __attribute__((aligned(16))) char smem[];
  const lptr L = (lptr)smem;
  const unsigned L0 = (unsigned)(uintptr_t)L;
  int b = blockIdx.x, lane = threadIdx.x & 63, w = __builtin_amdgcn_readfirstlane(threadIdx.x >> 6);
  const __amdgpu_buffer_rsrc_t rs = __builtin_amdgcn_make_buffer_rsrc(ws, 0, WS_END, 0x00020000);
  STAMP(63);
  unsigned zero = 0; PIN(zero);
  unsigned tag;
  f32x16 xres = splat16(0.f);

  {
    const int g = MAP_G(b), j = MAP_J(b), t = w & 3, r = lane & 31, h = lane >> 5;
    unsigned epoch = __hip_atomic_load((gu32*)ws, RLX_AGENT);
    unsigned keep0 = 0, keep1 = 0;
    if (w < 4) {
      const unsigned voq = ((lane >> 2) * 1536 + (lane & 3) * 8) * 2;
      if (w == 2 && b < 240) {
        unsigned long long pc; asm volatile("s_getpc_b64 %0" : "=s"(pc));
        const char* p = (const char*)(pc & ~255ull) + (b >> 3) * 1024 + lane * 16;
        unsigned d; asm volatile("global_load_dword %0, %1, off" : "=v"(d) : "v"(p) : "memory"); keep0 = d;
      }
      if (w == 0) { const v4u z = {0u, 0u, 0u, 0u}; lds_w128(L + L_WX + lane * 16, z); }
      BAR();
#pragma unroll 1
      for (int n = 0; n < 16; ++n) { if (n == NEARLY) ctr_wait(L, C_PX, 4u); dma_qkv(Aw + 32 * j, voq, L0, 32 + 16 * t + n); }
      DRAIN(); ctr_arrive(L, C_W, lane); ctr_wait(L, C_W, 8u);
      asm volatile("" :: "v"(keep0), "v"(keep1));
    } else {
      const u16* xrow = x_in + (size_t)(g * 128 + 8 * j + 2 * t + h) * 512 + 8 * r;
      const v4u xa = *(const GAS v4u*)xrow, xb = *(const GAS v4u*)(xrow + 256);
      const u16* xr = x_in + (size_t)(g * 128 + t * 32 + r) * 512 + 32 * j + 4 * h;
      v2u xv[4];
#pragma unroll
      for (int q = 0; q < 4; ++q) xv[q] = *(const GAS v2u*)(xr + 8 * q);
      BAR();
      { const unsigned fill = (lane & 16) ? 0x3f800000u : ONE2; const v4u o = {fill, fill, fill, fill}; if (w == 4) lds_w128(L + L_ONES + lane * 16, o); }
      if (h == 0) *(LAS unsigned*)(L + L_NM + t * 1024 + r * 4) = 0u;
      if (w >= 6) { v4u mk;
#pragma unroll
        for (int i = 0; i < 4; ++i) { const int key = ((2 * i) & 3) + 8 * (i >> 1) + 16 * (w - 6) + 4 * h; mk[i] = (key + 1 <= r) ? 0xffffffffu : ((key <= r) ? 0xffffu : 0u); }
        lds_w128(L + L_MSK + (w - 6) * 1024 + lane * 16, mk); }
      float s1 = 0.f, s2 = 0.f;
#pragma unroll
      for (int i = 0; i < 4; ++i) { s1 = dot2(xa[i], ONE2, s1); s2 = dot2(xa[i], xa[i], s2); s1 = dot2(xb[i], ONE2, s1); s2 = dot2(xb[i], xb[i], s2); }
      float m0, r0; row_stats(sum32(s1), sum32(s2), m0, r0);
      const v4u na = ln_frag(xa, m0, r0), nb = ln_frag(xb, m0, r0);
      { const lptr d = L + L_LN + (r >> 1) * 256 + (2 * t + h) * 16 + (r & 1) * 8;
        v2u u = {na[0], na[1]}; *(LAS v2u*)d = u; u[0] = na[2]; u[1] = na[3]; *(LAS v2u*)(d + 128) = u;
        u[0] = nb[0]; u[1] = nb[1]; *(LAS v2u*)(d + 4096) = u; u[0] = nb[2]; u[1] = nb[3]; *(LAS v2u*)(d + 4096 + 128) = u; }
      STAMP(56);
      ctr_arrive(L, C_ST, lane); ctr_wait(L, C_ST, 4u);
      const int vo = (lane >> 4) * 1024 + (((lane >> 3) & 1) * 32 + 8 * (j & 3) + (lane & 7)) * 16;
      const int xb0 = WS_X + (g * 4 + (j >> 2)) * 32768 + t * 8192;
      st_sc1(rs, lds_r128(L + L_LN + t * 2048 + lane * 16), vo, xb0);
      st_sc1(rs, lds_r128(L + L_LN + t * 2048 + 1024 + lane * 16), vo, xb0 + 4096);
      PIN(epoch);
      publish_wg(L, C_PX, 4u, lane, ws, WS_FX + (g * 16 + lane) * FL + j * 4, lane < 16, epoch + 1u);
      STAMP(57);
      { const unsigned voq = ((lane >> 2) * 1536 + (lane & 3) * 8) * 2;
#pragma unroll 1
        for (int n = 0; n < 8; ++n) dma_qkv(Aw + 32 * j, voq, L0, 8 * t + n);
        v4u rt[4];
#pragma unroll
        for (int i = 0; i < 4; ++i) {
          const bool po = (i >= 2) && (t & 1);
          const u16* src = po ? (t == 1 ? Ao : Bo) + (32 * g + 16 * (i & 1)) * 512
                              : (t < 2 ? Bw : Aw) + (32 * g + 16 * (i & 1)) * 1536 + (2 * (t & 1) + (i >> 1)) * 512;
          rt[i] = *(const GAS v4u*)(src + 32 * j + (lane >> 2) * (po ? 512 : 1536) + (lane & 3) * 8);
        }
        asm volatile("s_waitcnt vmcnt(4)" ::: "memory"); ctr_arrive(L, C_W, lane);
#pragma unroll
        for (int i = 0; i < 4; ++i) {
          const bool po = (i >= 2) && (t & 1);
          const int piece = (po ? (t == 1 ? T_OA : T_OB) : (t < 2 ? T_WB : T_WA) + 32 * (2 * (t & 1) + (i >> 1))) + 2 * g + (i & 1);
          st_sc1(rs, rt[i], lane * 16, WS_WT + (j * 256 + piece) * 1024);
        }
        publish_wg(L, C_PR, 4u, lane, ws, WS_FR + (lane * 16 + j) * FL + g * 4, lane < 16, epoch + 1u);
        if (t == 3) {
          poll(ws, WS_FR + (g * 16 + j) * FL, lane, lane < 16, __builtin_amdgcn_readfirstlane(epoch) + 1u, zero);
          __builtin_amdgcn_fence(__ATOMIC_ACQUIRE, "agent");
          DRAIN(); ctr_arrive(L, C_R, lane);
        }
        STAMP(58); }
#pragma unroll
      for (int q = 0; q < 4; ++q) { xres[4 * q] = bflo(xv[q][0]); xres[4 * q + 1] = bfhi(xv[q][0]); xres[4 * q + 2] = bflo(xv[q][1]); xres[4 * q + 3] = bfhi(xv[q][1]); }
    }
    PIN(epoch);
    tag = __builtin_amdgcn_readfirstlane(epoch) + 1u;
  }

  for (int pr = 0; pr < 2; ++pr) {
#pragma unroll
    for (int isB = 0; isB < 2; ++isB) {
      const int l = 2 * pr + isB;
      asm volatile("" : "+v"(lane), "+s"(b), "+s"(w));
      const int g = MAP_G(b), j = MAP_J(b);
      const int t = w & 3, hh = w >> 2, r = lane & 31, h = lane >> 5;
      const bool isQ = w < 4;
      const int gt = g * 4 + t, gj = g * 16 + j;
      const int trl = ((lane & 15) >> 2) * 64 + ((lane >> 4) & 1) * 32 + (lane & 3) * 8 + h * 256;
      const int l16 = lane * 16;
      const unsigned char* wt = ws + WS_WT + j * (256 * 1024);
      v4u qf;
      STAMP(l * 14 + 0);
      if (isQ) {
        const lptr pq = L + trl;
        lptr pv = pq + L_WV; PIN(pv);
        STAMP(l * 14 + 1);
        poll(ws, WS_FX + (l * 256 + gj) * FL, lane, lane < 16, tag, zero);
        STAMP(l * 14 + 2);
        const int xb = WS_X + l * MB2 + gt * 32768;
        f32x16 aK, aV; float rstd; f32x4 rs16[4]; v4u xf[32], xe, eq;
        if (isB) gemm_kv<true>(xf, L, pq, pv, aK, aV, rs, lane, xb, xe, eq, rstd, rs16, 3u, 4u * l + 4u, t);
        else gemm_kv<false>(xf, L, pq, pv, aK, aV, rs, lane, xb, xe, eq, rstd, rs16, 0u, 4u * l + 4u, t);
        STAMP(l * 14 + 12);
        if (!isB) {
          scale_tok(aK, rstd);
          const v4u k0 = pack8(aK, 0), k1 = pack8(aK, 8);
          const int kb = WS_KV + ((pr * 256 + gj) * 4 + t) * 4096;
          if (g < 15) { st_sc1(rs, k0, l16, kb); st_sc1(rs, k1, l16 + 1024, kb); }
          scale_feat(aV, rs16);
          const v4u vf0 = pack8(aV, 0), vf1 = pack8(aV, 8);
          if (g < 15) { st_sc1(rs, vf0, l16 + 2048, kb); st_sc1(rs, vf1, l16 + 3072, kb); }
          lds_w128(L + L_K + (8 + t) * 1024 + l16, k0); lds_w128(L + L_K + (20 + t) * 1024 + l16, k1);
          const lptr vd = L + L_V + ((r >> 4) * 12 + 8 + t) * 1024 + h * 256 + (r & 15) * 16;
          lds_w128(vd, vf0); lds_w128(vd + 512, vf1);
          STAMP(l * 14 + 4);
          f32x16 aQ = gemm_q(xf, pq, xe, eq);
          publish_wg(L, C_PH, 4u * pr + 4u, lane, ws, WS_FH + (pr * 256 + gj + 16 * (lane + 1)) * FL + lane * 4, lane < 2 && g + lane < 15, tag);
          STAMP(l * 14 + 5);
          scale_tok(aQ, rstd);
          qf = pack8(aQ, 0); lds_w128(L + L_QF + t * 1024 + l16, pack8(aQ, 8));
          if (l == 0) {
            if (t < 3) { const float c = colsum(pq + t * 32768); if (h == 0) *(LAS float*)(L + L_WX + (t * 32 + r) * 4) = c; }
            ctr_arrive(L, C_CA, lane); ctr_wait(L, C_CA, 4u); ctr_wait(L, C_R, 1u);
          } else ctr_wait(L, C_PH, 4u * pr + 4u);
          dma_tiled<PACE1>(wt, T_WB, L0, 24 * t, 24 * t + (l == 0 ? NA0 : NA1), l16);
          STAMP(l * 14 + 3);
        } else {
          scale_feat(aK, rs16); scale_feat(aV, rs16);
          const v4u k0 = phi_pack8(aK, 0), vf0 = pack8(aV, 0);
          f32x16 d = MFMA(k0, vf0, splat16(0.f));
          const v4u k1 = phi_pack8(aK, 8), vf1 = pack8(aV, 8);
          d = MFMA(k1, vf1, d);
          float z = 0.f;
#pragma unroll
          for (int i = 0; i < 4; ++i) { z = dot2(k0[i], ONE2, z); z = dot2(k1[i], ONE2, z); }
          z = half_sum(z);
          const lptr pw = L + L_PW + t * PSTR;
          if (h == 0) *(LAS float*)(pw + 2048 + r * 4) = z;
          if (r < 16) { *(LAS f32x4*)(pw + l16) = f32x4{d[0], d[1], d[2], d[3]}; *(LAS f32x4*)(pw + 1024 + l16) = f32x4{d[4], d[5], d[6], d[7]}; asm volatile("" ::: "memory"); }
          else { *(LAS f32x4*)(pw + l16) = f32x4{d[8], d[9], d[10], d[11]}; *(LAS f32x4*)(pw + 1024 + l16) = f32x4{d[12], d[13], d[14], d[15]}; }
          ctr_arrive(L, C_KV, lane);
          STAMP(l * 14 + 4);
          f32x16 aQ = gemm_q(xf, pq, xe, eq);
          if (pr == 0) ctr_arrive(L, C_QD, lane);
          scale_tok(aQ, rstd);
          qf = phi_pack8(aQ, 0); lds_w128(L + L_QF + t * 1024 + l16, phi_pack8(aQ, 8));
          STAMP(l * 14 + 5);
          if (pr == 0) {
            ctr_wait(L, C_QD, 4u);
            dma_tiled<PACE2>(wt, T_WA, L0, 24 * t, 24 * t + NB1, l16);
          }
        }
        BAR();
      } else {
        if (l > 0) {
          poll(ws, WS_FX + (l * 256 + gj) * FL, lane, lane < 16, tag, zero);
          f32x4 st[4];
#pragma unroll
          for (int i = 0; i < 4; ++i) st[i] = __builtin_bit_cast(f32x4, ld_sc1(rs, l16 + i * 1024, WS_XS + (l * 64 + gt) * 4096));
          const f32x4 a = (st[0] + st[1]) + (st[2] + st[3]);
          float m0, r0, m1, r1;
          row_stats(half_sum(x16_sum(a[0])), half_sum(x16_sum(a[1])), m0, r0);
          row_stats(half_sum(x16_sum(a[2])), half_sum(x16_sum(a[3])), m1, r1);
          if (lane < 16) { *(LAS v2u*)(L + L_NM + t * 1024 + lane * 8) = v2u{pk2(-m0, -m0), pk2(-m1, -m1)};
            *(LAS f32x2*)(L + L_RS + (t & 1) * 128 + (t >> 1) * 512 + lane * 8) = f32x2{r0, r1}; }
          ctr_arrive(L, C_ST, lane);
        }
        STAMP(l * 14 + 13);
        if (isB) {
          ctr_wait(L, C_OP, 4u * l);
          ctr_wait(L, C_XI, 4u * pr + 4u);
          dma_tiled<0>(wt, T_OB, L0 + L_WO, 8 * t, 8 * t + 8, l16);
        }
        STAMP(l * 14 + 1);
        if (!isB) {
          v4u kv[8];
          poll(ws, WS_FH + (pr * 256 + gj) * FL, lane, lane < 2 && lane < g, tag, zero);
          STAMP(l * 14 + 2);
          const v4u z4 = {0u, 0u, 0u, 0u};
#pragma unroll
          for (int q = 0; q < 2; ++q) {
            const int kb = WS_KV + ((pr * 256 + gj - 16 * (q + 1)) * 4 + t) * 4096;
            if (q < g) {
#pragma unroll
              for (int i = 0; i < 4; ++i) kv[4 * q + i] = ld_sc1(rs, l16 + i * 1024, kb);
            } else {
#pragma unroll
              for (int i = 0; i < 4; ++i) kv[4 * q + i] = z4;
            }
          }
          ctr_wait(L, C_OP, 4u * l);
#pragma unroll
          for (int q = 0; q < 2; ++q) {
            const int ti = 4 - 4 * q + t;
            lds_w128(L + L_K + ti * 1024 + l16, kv[4 * q]); lds_w128(L + L_K + (12 + ti) * 1024 + l16, kv[4 * q + 1]);
            const lptr vd = L + L_V + ((r >> 4) * 12 + ti) * 1024 + h * 256 + (r & 15) * 16;
            lds_w128(vd, kv[4 * q + 2]); lds_w128(vd + 512, kv[4 * q + 3]);
          }
          STAMP(l * 14 + 5);
          BAR();
        } else {
          if (t == 0) {
            ctr_wait(L, C_KV, 4u * pr + 4u);
            STAMP(l * 14 + 4);
            f32x4 u0 = lds_r128f(L + L_PW + l16), u1 = lds_r128f(L + L_PW + 1024 + l16);
            float z = *(const LAS float*)(L + L_PW + 2048 + r * 4);
#pragma unroll
            for (int q = 1; q < 4; ++q) { u0 += lds_r128f(L + L_PW + q * PSTR + l16); u1 += lds_r128f(L + L_PW + q * PSTR + 1024 + l16); z += *(const LAS float*)(L + L_PW + q * PSTR + 2048 + r * 4); }
            const int pb = WS_P + ((pr * 16 + j) * 16 + g) * PSTR;
            st_sc1(rs, __builtin_bit_cast(v4u, u0), l16, pb); st_sc1(rs, __builtin_bit_cast(v4u, u1), l16 + 1024, pb);
            if (h == 0) __builtin_amdgcn_raw_buffer_store_b32(__float_as_uint(z), rs, 2048 + r * 4, pb, SC1);
            publish(ws, WS_FP + (pr * 256 + lane * 16 + j) * FL + g * 4, lane < 16, tag);
            STAMP(l * 14 + 3);
          }
          poll(ws, WS_FP + (pr * 256 + gj) * FL, 4 * t + lane, lane < 4, tag, zero);
          STAMP(l * 14 + 2);
          v4u a[8]; unsigned zz[4];
#pragma unroll
          for (int q = 0; q < 4; ++q) { const int pb = WS_P + ((pr * 16 + j) * 16 + 4 * t + q) * PSTR;
            a[2 * q] = ld_sc1(rs, l16, pb); a[2 * q + 1] = ld_sc1(rs, l16 + 1024, pb); zz[q] = __builtin_amdgcn_raw_buffer_load_b32(rs, 2048 + r * 4, pb, SC1); }
          SB();
          f32x4 u0 = __builtin_bit_cast(f32x4, a[0]), u1 = __builtin_bit_cast(f32x4, a[1]); float z = __uint_as_float(zz[0]);
#pragma unroll
          for (int q = 1; q < 4; ++q) { u0 += __builtin_bit_cast(f32x4, a[2 * q]); u1 += __builtin_bit_cast(f32x4, a[2 * q + 1]); z += __uint_as_float(zz[q]); }
          const lptr pw = L + L_PR + t * PSTR;
          *(LAS f32x4*)(pw + l16) = u0; *(LAS f32x4*)(pw + 1024 + l16) = u1;
          if (h == 0) *(LAS float*)(pw + 2048 + r * 4) = z;
          DRAIN();
          STAMP(l * 14 + 5);
          BAR();
        }
        qf = lds_r128(L + L_QF + t * 1024 + l16);
      }

      STAMP(l * 14 + 6);
      f32x16 o;
      if (!isB) {

        const lptr kp = L + L_K + (hh * 12 + t) * 1024 + l16;
        const lptr vp = (r < 16 ? L + L_V + (hh * 12 + t) * 1024 + h * 256 : L + L_ONES) + (r & 15) * 16;
        const int vs = r < 16 ? 1024 : 0;
        if (g >= 2) o = attention<false>(kp, vp, vs, L + L_MSK + l16, qf, 0);
        else o = attention<true>(kp, vp, vs, L + L_MSK + l16, qf, 8 - 4 * g - t);
      } else {
        const lptr a0 = L + L_PR + (r < 16 ? (hh * 16 + r + 32 * h) * 16 : 2048 + (hh * 16 + 4 * h) * 4);
        const lptr a1 = a0 + (r < 16 ? 1024 : 32);
        f32x4 u0 = lds_r128f(a0), u1 = lds_r128f(a1);
#pragma unroll
        for (int q = 1; q < 4; ++q) { u0 += lds_r128f(a0 + q * PSTR); u1 += lds_r128f(a1 + q * PSTR); }
        const v4u af = {pk2(u0[0], u0[1]), pk2(u0[2], u0[3]), pk2(u1[0], u1[1]), pk2(u1[2], u1[3])};
        o = MFMA(af, qf, splat16(0.f));
        o[8] += 1e-6f;
      }
      { const float ri = __builtin_amdgcn_rcpf(o[8]);
#pragma unroll
        for (int i = 0; i < 8; ++i) o[i] *= ri;
        asm volatile("" :: "v"(o[0]));
        STAMP(l * 14 + 7);
        st_sc1(rs, pack8(o, 0), l16, WS_O + l * MB2 + gt * 32768 + (2 * j + hh) * 1024);
        publish_wg(L, C_PO, 8u * l + 8u, lane, ws, WS_FO + (l * 256 + g * 16 + lane) * FL + j * 4, lane < 16, tag);
      }
      STAMP(l * 14 + 8);

      if (isQ) {
        if (!isB) {
          dma_tiled<0>(wt, T_WB, L0, 24 * t + (l == 0 ? NA0 : NA1), 24 * t + 24, l16);
          ctr_wait(L, C_PO, 8u * l + 8u);
          dma_tiled<0>(wt, T_OA, L0 + L_WO, 8 * t, 8 * t + 8, l16);
          DRAIN(); ctr_arrive(L, C_WO, lane);
        } else if (pr == 0) dma_tiled<PACE2>(wt, T_WA, L0, 24 * t + NB1, 24 * t + 24, l16);
        STAMP(l * 14 + 9);
        if (l < 3) { DRAIN(); ctr_arrive(L, C_W, lane); ctr_wait(L, C_W, 4u * l + 12u); }
        if (l == 0 && t < 3) {
          const float c = colsum(L + trl + t * 32768);
          if (h == 0) *(LAS float*)(L + L_WX + 384 + (t * 32 + r) * 4) = c;
          ctr_arrive(L, C_CS, lane);
        }
      } else {
        poll(ws, WS_FO + (l * 256 + gj) * FL, lane, lane < 16, tag, zero);
        STAMP(l * 14 + 9);
        lptr po = L + L_WO + trl; PIN(po);
        const f32x16 y = gemm_out(L, po, rs, l16, WS_O + l * MB2 + gt * 32768, 4u * pr + 4u);
        ctr_arrive(L, C_OP, lane);
        asm volatile("" :: "v"(y[0]));
        STAMP(l * 14 + 10);
        v4u x0, x1; float s1 = 0.f, s2 = 0.f;
#pragma unroll
        for (int i = 0; i < 8; ++i) {
          const unsigned yb = pk2(y[2 * i], y[2 * i + 1]);
          const unsigned xb = pk2(xres[2 * i] + bflo(yb), xres[2 * i + 1] + bfhi(yb));
          xres[2 * i] = bflo(xb); xres[2 * i + 1] = bfhi(xb);
          s1 = dot2(xb, ONE2, s1); s2 = dot2(xb, xb, s2);
          if (i < 4) x0[i] = xb; else x1[i - 4] = xb;
        }
        if (l < 3) {
          const int xb = WS_X + (l + 1) * MB2 + gt * 32768 + j * 2048;
          st_sc1(rs, x0, l16, xb); st_sc1(rs, x1, l16 + 1024, xb);
          s1 = half_sum(s1); s2 = half_sum(s2);
          if (h == 0) { v2u v = {__float_as_uint(s1), __float_as_uint(s2)}; __builtin_amdgcn_raw_buffer_store_b64(v, rs, r * 8, WS_XS + (((l + 1) * 64 + gt) * 16 + j) * 256, SC1); }
          publish_wg(L, C_PX, 4u * l + 8u, lane, ws, WS_FX + ((l + 1) * 256 + g * 16 + lane) * FL + j * 4, lane < 16, tag);
        } else {
          u16* op = out + (size_t)(gt * 32 + r) * 512 + 32 * j + 4 * h;
          v2u a = {x0[0], x0[1]}, c = {x0[2], x0[3]}, d = {x1[0], x1[1]}, e = {x1[2], x1[3]};
          *(GAS v2u*)(op) = a; *(GAS v2u*)(op + 8) = c; *(GAS v2u*)(op + 16) = d; *(GAS v2u*)(op + 24) = e;
          if (b == 0 && w == 4 && lane == 0) __hip_atomic_store((gu32*)ws, tag, RLX_AGENT);
        }
      }
      STAMP(l * 14 + 11);
    }
  }
}

extern "C" void kernel_launch(void* const* d_in, const int* in_sizes, int n_in, void* d_out, int out_size, void* d_ws, size_t ws_size,
                              hipStream_t stream) {
  (void)in_sizes; (void)n_in; (void)out_size; (void)ws_size;
  (void)hipFuncSetAttribute((const void*)mega, hipFuncAttributeMaxDynamicSharedMemorySize, LDS_BYTES);
  mega<<<dim3(256), dim3(512), LDS_BYTES, stream>>>((const u16*)d_in[0], (const u16*)d_in[1], (const u16*)d_in[2], (const u16*)d_in[4],
                                                    (const u16*)d_in[5], (u16*)d_out, (unsigned char*)d_ws);
}
```

```cpp
#include <hip/hip_runtime.h>
#include <hip/hip_bf16.h>
#include <stdint.h>

#define DI __device__ __forceinline__
#define LAS __attribute__((address_space(3)))
#define GAS __attribute__((address_space(1)))
typedef unsigned short u16;
typedef unsigned v4u __attribute__((ext_vector_type(4)));
typedef unsigned v2u __attribute__((ext_vector_type(2)));
typedef float f32x16 __attribute__((ext_vector_type(16)));
typedef float f32x4 __attribute__((ext_vector_type(4)));
typedef float f32x2 __attribute__((ext_vector_type(2)));
typedef short bf16x8 __attribute__((ext_vector_type(8)));
typedef short s16x4 __attribute__((ext_vector_type(4)));
typedef __bf16 bf2 __attribute__((ext_vector_type(2)));
typedef GAS unsigned gu32;
typedef LAS char* lptr;
#define RLX_AGENT __ATOMIC_RELAXED, __HIP_MEMORY_SCOPE_AGENT
#define RLX_WG __ATOMIC_RELAXED, __HIP_MEMORY_SCOPE_WORKGROUP
#define MFMA(a, b, c) __builtin_amdgcn_mfma_f32_32x32x16_bf16(__builtin_bit_cast(bf16x8, (a)), __builtin_bit_cast(bf16x8, (b)), (c), 0, 0, 0)
#define SB() __builtin_amdgcn_sched_barrier(0)
#define PIN(x) asm volatile("" : "+v"(x))
#define DRAIN() asm volatile("s_waitcnt vmcnt(0)" ::: "memory")
#define LGKM0() asm volatile("s_waitcnt lgkmcnt(0)" ::: "memory")
#define BAR() asm volatile("s_waitcnt lgkmcnt(0)\n\ts_barrier" ::: "memory")
constexpr int SC1 = 16;
constexpr unsigned ONE2 = 0x3f803f80u;
#define STAMP(i) do {} while (0)

#define XMAP 1
#define MAP_G(b) (4 * (((b) & 7) >> 1) + ((b) >> 6))
#define MAP_J(b) (8 * ((b) & 1) + (((b) >> 3) & 7))
constexpr int FL = 256;
constexpr int MB2 = 2 << 20;
constexpr int WS_FX = 4096;
constexpr int WS_FO = WS_FX + 4 * 256 * FL;
constexpr int WS_FH = WS_FO + 4 * 256 * FL;
constexpr int WS_FP = WS_FH + 2 * 256 * FL;
constexpr int WS_FR = WS_FP + 2 * 256 * FL;
constexpr int WS_XS = WS_FR + 256 * FL;
constexpr int PSTR = 2304;
constexpr int WS_P = WS_XS + 4 * 1024 * 256;
constexpr int WS_X = 6 << 20;
constexpr int WS_O = WS_X + 4 * MB2;
constexpr int WS_KV = WS_O + 4 * MB2;
constexpr int WS_WT = WS_KV + 4 * MB2;
constexpr int WS_END = WS_WT + 2 * MB2;
constexpr int T_WB = 0, T_WA = 96, T_OA = 192, T_OB = 224;
static_assert(WS_P + 2 * 256 * PSTR <= WS_X, "ws map");

constexpr int L_WQ = 0, L_WK = 32768, L_WV = 65536, L_WO = 98304, L_K = 131072, L_V = L_WO + 8192, L_MISC = 155648;
constexpr int L_QF = L_MISC, L_MSK = L_MISC + 4096, L_ONES = L_MISC + 6144;
constexpr int L_RS = L_ONES + 256;
constexpr int L_WX = L_MISC + 7168, L_MU = L_WX + 768, L_CTR = L_MU + 192;
constexpr int L_NM = L_QF;
constexpr int L_LN = L_K;
constexpr int L_PW = L_K, L_PR = L_K + 4 * PSTR;
constexpr int LDS_BYTES = 163840;
enum { C_W = 0, C_CS = 1, C_CA = 2, C_KV = 3, C_OP = 4, C_P = 5, C_PX = 6, C_PO = 7, C_PH = 8, C_PR = 9, C_R = 10, C_WO = 11, C_XI = 12, C_ST = 13, C_QD = 14 };

template <int I> struct IC { static constexpr int value = I; };
template <int N, int I = 0, class F> DI void static_for(F&& f) { if constexpr (I < N) { f(IC<I>{}); static_for<N, I + 1>(f); } }
DI unsigned pk2(float lo, float hi) { f32x2 v = {lo, hi}; return __builtin_bit_cast(unsigned, __builtin_convertvector(v, bf2)); }
DI float bflo(unsigned u) { return __uint_as_float(u << 16); }
DI float bfhi(unsigned u) { return __uint_as_float(u & 0xffff0000u); }
DI float rbf(float x) { return bflo(pk2(x, 0.f)); }
DI float dot2(unsigned a, unsigned b, float c) { return __builtin_amdgcn_fdot2_f32_bf16(__builtin_bit_cast(bf2, a), __builtin_bit_cast(bf2, b), c, false); }
DI float half_sum(float v) { auto r = __builtin_amdgcn_permlane32_swap(__float_as_uint(v), __float_as_uint(v), false, false); return __uint_as_float(r[0]) + __uint_as_float(r[1]); }
DI float half_max(float v) { auto r = __builtin_amdgcn_permlane32_swap(__float_as_uint(v), __float_as_uint(v), false, false); return __builtin_fmaxf(__uint_as_float(r[0]), __uint_as_float(r[1])); }
template <int C> DI float dpp_add(float v) { return v + __uint_as_float(__builtin_amdgcn_update_dpp(0u, __float_as_uint(v), C, 0xf, 0xf, false)); }
DI float sum32(float v) {
  v = dpp_add<0x128>(v); v = dpp_add<0x124>(v); v = dpp_add<0x122>(v); v = dpp_add<0x121>(v);
  auto r = __builtin_amdgcn_permlane16_swap(__float_as_uint(v), __float_as_uint(v), false, false);
  return __uint_as_float(r[0]) + __uint_as_float(r[1]);
}
DI float max3(float a, float b, float c) { return __builtin_fmaxf(__builtin_fmaxf(a, b), c); }
DI v4u pack8(const f32x16& a, int o) { v4u r = {pk2(a[o], a[o + 1]), pk2(a[o + 2], a[o + 3]), pk2(a[o + 4], a[o + 5]), pk2(a[o + 6], a[o + 7])}; return r; }
DI v4u lds_r128(lptr p) { return *(const LAS v4u*)p; }
DI f32x4 lds_r128f(lptr p) { return *(const LAS f32x4*)p; }
DI void lds_w128(lptr p, v4u v) { *(LAS v4u*)p = v; }
DI f32x16 splat16(float x) { f32x16 z;
#pragma unroll
  for (int i = 0; i < 16; ++i) z[i] = x;
  return z; }
DI v4u wfrag(lptr p, int off) {
  const s16x4 a = __builtin_amdgcn_ds_read_tr16_b64_v4i16((LAS s16x4*)(p + off));
  const s16x4 b = __builtin_amdgcn_ds_read_tr16_b64_v4i16((LAS s16x4*)(p + off + 512));
  const v2u x = __builtin_bit_cast(v2u, a), y = __builtin_bit_cast(v2u, b);
  v4u r = {x[0], x[1], y[0], y[1]};
  return r;
}
DI void glds16(const void* base, unsigned voff, unsigned lds_dst) {
  unsigned keep;
  asm volatile("s_mov_b32 %0, m0\n\ts_mov_b32 m0, %3\n\ts_nop 0\n\tglobal_load_lds_dwordx4 %1, %2\n\ts_mov_b32 m0, %0" : "=&s"(keep) : "v"(voff), "s"(base), "s"(lds_dst) : "memory");
}
DI void dma_qkv(const u16* Wj, unsigned voff, unsigned L0, int n) { glds16(Wj + (size_t)(n & 31) * (16 * 1536) + (n >> 5) * 512, voff, L0 + n * 1024); }
template <int PACE> DI void dma_tiled(const unsigned char* wt, int src, unsigned dst, int n0, int n1, int l16) {
#pragma unroll 1
  for (int n = n0; n < n1; ++n) { glds16(wt + (src + n) * 1024, l16, dst + n * 1024); if (PACE) __builtin_amdgcn_s_sleep(PACE); }
}
template <int N> DI void nap() { __builtin_amdgcn_s_sleep(N); }
DI void ctr_arrive(lptr L, int c, int lane) { asm volatile("" ::: "memory"); if (lane == 0) (void)__hip_atomic_fetch_add((LAS unsigned*)(L + L_CTR + 4 * c), 1u, RLX_WG); }
DI void ctr_wait(lptr L, int c, unsigned target) {
  unsigned spins = 0;
  while (__builtin_amdgcn_readfirstlane(__hip_atomic_load((LAS unsigned*)(L + L_CTR + 4 * c), RLX_WG)) < target && ++spins < (1u << 22)) __builtin_amdgcn_s_sleep(1);
  asm volatile("" ::: "memory");
}
DI void poll(unsigned char* ws, int line_off, int slot, bool active, unsigned tag, unsigned zero) {
  gu32* p = (gu32*)(ws + line_off) + slot;
  for (unsigned spins = 0; spins < (1u << 14); ++spins) {
    unsigned v = tag;
    if (active) v = __hip_atomic_fetch_add(p, zero, RLX_AGENT);
    if (__all(v == tag)) break;
  }
  __builtin_amdgcn_fence(__ATOMIC_ACQUIRE, "wavefront");
  asm volatile("" ::: "memory");
}
DI void publish(unsigned char* ws, int word_off, bool active, unsigned tag) {
  DRAIN();
  if (active) (void)__hip_atomic_exchange((gu32*)(ws + word_off), tag, RLX_AGENT);
}
DI void publish_wg(lptr L, int c, unsigned target, int lane, unsigned char* ws, int word_off, bool active, unsigned tag) {
  DRAIN();
  unsigned old = 0;
  if (lane == 0) old = __hip_atomic_fetch_add((LAS unsigned*)(L + L_CTR + 4 * c), 1u, RLX_WG);
  if (__builtin_amdgcn_readfirstlane(old) + 1u == target && active) (void)__hip_atomic_exchange((gu32*)(ws + word_off), tag, RLX_AGENT);
}
DI v4u ld_sc1(__amdgpu_buffer_rsrc_t rs, int voff, int soff) { return __builtin_amdgcn_raw_buffer_load_b128(rs, voff, soff, SC1); }
DI void st_sc1(__amdgpu_buffer_rsrc_t rs, v4u v, int voff, int soff) { __builtin_amdgcn_raw_buffer_store_b128(v, rs, voff + soff, 0, SC1); }

DI void row_stats(float s1, float s2, float& mu, float& rs) {
  const float m = s1 * (1.0f / 512.0f);
  const float var = __builtin_fmaxf(__builtin_fmaf(-m, m, s2 * (1.0f / 512.0f)), 0.f);
  mu = rbf(m);
  rs = __builtin_amdgcn_rcpf(rbf(__builtin_amdgcn_sqrtf(rbf(rbf(var) + 1e-5f))));
}
DI v4u ln_frag(v4u x, float mu, float rs) {
  v4u r;
#pragma unroll
  for (int i = 0; i < 4; ++i) { const unsigned d = pk2(bflo(x[i]) - mu, bfhi(x[i]) - mu); r[i] = pk2(bflo(d) * rs, bfhi(d) * rs); }
  return r;
}
DI void scale_tok(f32x16& a, float rs) {
#pragma unroll
  for (int i = 0; i < 16; ++i) a[i] *= rs;
}
DI void scale_feat(f32x16& a, const f32x4 (&u)[4]) {
#pragma unroll
  for (int i = 0; i < 16; ++i) a[i] *= u[i >> 2][i & 3];
}
DI float phi1(float t) { return (__builtin_fmaxf(t, 0.f) + __builtin_amdgcn_exp2f(__builtin_fminf(t, 0.f) * 1.4426950408889634f)) + 1e-6f; }
DI v4u phi_pack8(const f32x16& a, int o) {
  v4u r;
#pragma unroll
  for (int i = 0; i < 4; ++i) { const unsigned t = pk2(a[o + 2 * i], a[o + 2 * i + 1]); r[i] = pk2(phi1(bflo(t)), phi1(bfhi(t))); }
  return r;
}
#define NEARLY 4
#define PACE2 2
#define NB1 16
#define NB2 15
#define PACE1 1
#define NA1 16
#define NA0 8
#define PFX 12
constexpr int PF = PFX;
DI v4u ld_frag(__amdgpu_buffer_rsrc_t rs, int l16, int base, int s) { return ld_sc1(rs, l16 + (s & 3) * 1024, base + (s >> 2) * 4096); }
DI f32x16 gemm_out(lptr L, lptr p, __amdgpu_buffer_rsrc_t rs, int l16, int base, unsigned wo_target) {
  constexpr int D = 5, P = 16;
  v4u wr[D + 1], xr[P + 1];
#pragma unroll
  for (int s = 0; s < P; ++s) xr[s] = ld_frag(rs, l16, base, s);
  SB();
  ctr_wait(L, C_WO, wo_target);
#pragma unroll
  for (int s = 0; s < D; ++s) wr[s] = wfrag(p, s * 1024);
  SB();
  f32x16 a = splat16(0.f);
#pragma unroll
  for (int s = 0; s < 32; ++s) {
    if (s + P < 32) xr[(s + P) % (P + 1)] = ld_frag(rs, l16, base, s + P);
    if (s + D < 32) wr[(s + D) % (D + 1)] = wfrag(p, (s + D) * 1024);
    a = MFMA(wr[s % (D + 1)], xr[s % (P + 1)], a);
    SB();
  }
  return a;
}
DI float colsum(lptr p) {
  constexpr int D = 5;
  v4u wr[D + 1];
  const v4u ones = {ONE2, ONE2, ONE2, ONE2};
#pragma unroll
  for (int s = 0; s < D; ++s) wr[s] = wfrag(p, s * 1024);
  SB();
  f32x16 a = splat16(0.f);
#pragma unroll
  for (int s = 0; s < 32; ++s) {
    if (s + D < 32) wr[(s + D) % (D + 1)] = wfrag(p, (s + D) * 1024);
    a = MFMA(ones, wr[s % (D + 1)], a);
    SB();
  }
  return a[0];
}
DI float x16_sum(float v) { auto r = __builtin_amdgcn_permlane16_swap(__float_as_uint(v), __float_as_uint(v), false, false); return __uint_as_float(r[0]) + __uint_as_float(r[1]); }
DI v4u split3(float c) { const float hi = rbf(c), mid = rbf(c - hi), lo = (c - hi) - mid; return v4u{pk2(hi, mid), pk2(lo, 0.f), 0u, 0u}; }
template <bool ISB> DI void gemm_kv(v4u (&xf)[32], lptr L, lptr pq, lptr pv, f32x16& aK, f32x16& aV, __amdgpu_buffer_rsrc_t rs, int lane, int base,
                                    v4u& xe, v4u& eq, float& rstd, f32x4 (&rs16)[4], unsigned cs_target, unsigned st_target, int t) {
  constexpr int D = 3;
  const int l16 = lane * 16, r = lane & 31, h = lane >> 5;
  v4u wk[D + 1], wv[D + 1];
#pragma unroll
  for (int s = 0; s < PF; ++s) xf[s] = ld_frag(rs, l16, base, s);
  SB();
  ctr_wait(L, C_CS, cs_target);
  const lptr rp = L + L_RS + (t & 1) * 128 + (t >> 1) * 512, np = L + L_NM + t * 1024, cs = L + L_WX + (ISB ? 384 : 0) + r * 4;
  v4u ek = split3(*(const LAS float*)(cs + 128)), ev = split3(*(const LAS float*)(cs + 256));
  eq = split3(*(const LAS float*)cs);
  PIN(ek); PIN(ev); PIN(eq);
#pragma unroll
  for (int s = 0; s < D; ++s) { wk[s] = wfrag(pq, L_WK + s * 1024); wv[s] = wfrag(pv, s * 1024); }
  SB();
  aK = splat16(0.f); aV = splat16(0.f);
  unsigned cv = 0, nm = 0;
  static_for<32>([&](auto SI) __attribute__((always_inline)) {
    constexpr int s = decltype(SI)::value;
    if constexpr (s + D < 32) wk[(s + D) % (D + 1)] = wfrag(pq, L_WK + (s + D) * 1024);
    if constexpr (s == 24) cv = __hip_atomic_load((LAS unsigned*)(L + L_CTR + 4 * C_ST), RLX_WG);
    if constexpr (s == 28) {
      if (__builtin_amdgcn_readfirstlane(cv) < st_target) ctr_wait(L, C_ST, st_target);
      asm volatile("" ::: "memory");
      nm = *(const LAS unsigned*)(np + r * 4); rstd = *(const LAS float*)(rp + r * 4);
    }
    if constexpr (s == 29 || s == 30) rs16[s - 29] = lds_r128f(rp + h * 16 + (s - 29) * 32);
    if constexpr (s == 31) { nm = h ? 0u : nm; xe = v4u{nm, nm & 0xffffu, 0u, 0u}; aK = ISB ? MFMA(xe, ek, aK) : MFMA(ek, xe, aK); aV = MFMA(xe, ev, aV); }
    aK = ISB ? MFMA(xf[s], wk[s % (D + 1)], aK) : MFMA(wk[s % (D + 1)], xf[s], aK);
    SB();
    if constexpr (s + D < 32) wv[(s + D) % (D + 1)] = wfrag(pv, (s + D) * 1024);
    if constexpr (s + PF < 32) xf[s + PF] = ld_frag(rs, l16, base, s + PF);
    if constexpr (ISB && s + PF == 32) ctr_arrive(L, C_XI, lane);
    if constexpr (s == 29 || s == 30) rs16[s - 27] = lds_r128f(rp + h * 16 + (s - 27) * 32);
    aV = MFMA(xf[s], wv[s % (D + 1)], aV);
    SB();
  });
}
DI f32x16 gemm_q(const v4u (&xf)[32], lptr p, v4u xe, v4u eq) {
  constexpr int D = 5;
  v4u wr[D + 1];
#pragma unroll
  for (int s = 0; s < D; ++s) wr[s] = wfrag(p, s * 1024);
  f32x16 a = MFMA(eq, xe, splat16(0.f));
  SB();
#pragma unroll
  for (int s = 0; s < 32; ++s) {
    if (s + D < 32) wr[(s + D) % (D + 1)] = wfrag(p, (s + D) * 1024);
    a = MFMA(wr[s % (D + 1)], xf[s], a);
    SB();
  }
  return a;
}

constexpr unsigned long long diag_mask(int i) { return ((unsigned long long)(0xffffffffu << ((i & 3) + 8 * (i >> 2) + 4)) << 32) | (0xffffffffu << ((i & 3) + 8 * (i >> 2))); }
template <bool EDGE>
DI f32x16 attention(lptr kp, lptr vp, int vs, lptr mp, v4u qf, int nv) {
  v4u kf[9];
  kf[8] = lds_r128(kp + 8 * 1024);
#pragma unroll
  for (int n = 0; n < 8; ++n) kf[n] = lds_r128(kp + n * 1024);
  v4u va = lds_r128(vp), vb = lds_r128(vp + 512);
  SB();
  f32x16 s[9];
  float m0, m1, m2, m3;
#define SC(n) s[n] = MFMA(kf[n], qf, splat16(EDGE ? ((n) < nv ? -3.0e38f : 0.f) : 0.f))
#define MX(n) do { m0 = max3(m0, s[n][0], s[n][1]); m1 = max3(m1, s[n][2], s[n][3]); m2 = max3(m2, s[n][4], s[n][5]); m3 = max3(m3, s[n][6], s[n][7]); \
    m0 = max3(m0, s[n][8], s[n][9]); m1 = max3(m1, s[n][10], s[n][11]); m2 = max3(m2, s[n][12], s[n][13]); m3 = max3(m3, s[n][14], s[n][15]); } while (0)
#define MG(i) s[0][i] = __builtin_amdgcn_inverse_ballot_w64(diag_mask(i)) ? s[8][i] : s[0][i]
  s[8] = MFMA(kf[8], qf, splat16(0.f)); SC(0); SC(1); SB();
  SC(2); MG(0); MG(1); MG(2); MG(3); MG(4); MG(5); MG(6); MG(7); SB();
  SC(3); MG(8); MG(9); MG(10); MG(11); MG(12); MG(13); MG(14); MG(15); SB();
  SC(4); m0 = max3(s[1][0], s[1][1], s[1][2]); m1 = max3(s[1][3], s[1][4], s[1][5]); m2 = max3(s[1][6], s[1][7], s[1][8]); m3 = max3(s[1][9], s[1][10], s[1][11]);
  m0 = max3(m0, s[1][12], s[1][13]); m1 = max3(m1, s[1][14], s[1][15]); SB();
  SC(5); MX(0); SB();
  SC(6); MX(2); SB();
  SC(7); MX(3); SB();
  const v4u mlo = lds_r128(mp), mhi = lds_r128(mp + 1024);
  const v4u da = lds_r128(vp + 8 * vs), db = lds_r128(vp + 8 * vs + 512);
  MX(4); MX(5); MX(6); MX(7);
#undef SC
#undef MX
#undef MG
  constexpr float C = 0.25f * 1.4426950408889634f;
  const float nm = -C * half_max(__builtin_fmaxf(max3(m0, m1, m2), m3));
  f32x2 C2 = {C, C}, nm2 = {nm, nm};
  f32x16 o = splat16(0.f);
#pragma unroll
  for (int n = 0; n < 8; ++n) {
    v4u na, nb;
    if (n < 7) { vp += vs; na = lds_r128(vp); nb = lds_r128(vp + 512); }
    f32x2 e[8];
#pragma unroll
    for (int i = 0; i < 8; ++i) { const f32x2 v = {s[n][2 * i], s[n][2 * i + 1]}; e[i] = __builtin_elementwise_fma(v, C2, nm2); }
#pragma unroll
    for (int i = 0; i < 8; ++i) PIN(e[i]);
    v4u p0, p1;
#pragma unroll
    for (int i = 0; i < 4; ++i) p0[i] = pk2(__builtin_amdgcn_exp2f(e[i][0]), __builtin_amdgcn_exp2f(e[i][1]));
#pragma unroll
    for (int i = 0; i < 4; ++i) p1[i] = pk2(__builtin_amdgcn_exp2f(e[4 + i][0]), __builtin_amdgcn_exp2f(e[4 + i][1]));
    if (n == 0) {
      const v4u d0 = p0 & mlo, d1 = p1 & mhi;
      o = MFMA(da, d0, o); o = MFMA(va, p0 ^ d0, o); o = MFMA(db, d1, o); o = MFMA(vb, p1 ^ d1, o);
    } else { o = MFMA(va, p0, o); o = MFMA(vb, p1, o); }
    if (n < 7) { va = na; vb = nb; }
    SB();
  }
  return o;
}

__global__ void __launch_bounds__(512, 2) mega(const u16* __restrict__ x_in, const u16* __restrict__ Aw, const u16* __restrict__ Ao,
                                               const u16* __restrict__ Bw, const u16* __restrict__ Bo, u16* __restrict__ out, unsigned char* ws) {
  extern __shared__ __attribute__((aligned(16))) char smem[];
  const lptr L = (lptr)smem;
  const unsigned L0 = (unsigned)(uintptr_t)L;
  int b = blockIdx.x, lane = threadIdx.x & 63, w = __builtin_amdgcn_readfirstlane(threadIdx.x >> 6);
  const __amdgpu_buffer_rsrc_t rs = __builtin_amdgcn_make_buffer_rsrc(ws, 0, WS_END, 0x00020000);
  STAMP(63);
  const unsigned long long rt0_ = __builtin_amdgcn_s_memrealtime(), cy0_ = __builtin_amdgcn_s_memtime();
  unsigned zero = 0; PIN(zero);
  unsigned tag;
  f32x16 xres = splat16(0.f);

  {
    const int g = MAP_G(b), j = MAP_J(b), t = w & 3, r = lane & 31, h = lane >> 5;
    unsigned epoch = __hip_atomic_load((gu32*)ws, RLX_AGENT);
    if (w < 4) {
      const unsigned voq = ((lane >> 2) * 1536 + (lane & 3) * 8) * 2;
      if (w == 0) { const v4u z = {0u, 0u, 0u, 0u}; lds_w128(L + L_WX + lane * 16, z); }
      BAR();
#pragma unroll 1
      for (int n = 0; n < 16; ++n) { if (n == NEARLY) ctr_wait(L, C_PX, 4u); dma_qkv(Aw + 32 * j, voq, L0, 32 + 16 * t + n); }
      DRAIN(); ctr_arrive(L, C_W, lane); ctr_wait(L, C_W, 8u);
    } else {
      const u16* xrow = x_in + (size_t)(g * 128 + 8 * j + 2 * t + h) * 512 + 8 * r;
      const v4u xa = *(const GAS v4u*)xrow, xb = *(const GAS v4u*)(xrow + 256);
      const u16* xr = x_in + (size_t)(g * 128 + t * 32 + r) * 512 + 32 * j + 4 * h;
      v2u xv[4];
#pragma unroll
      for (int q = 0; q < 4; ++q) xv[q] = *(const GAS v2u*)(xr + 8 * q);
      BAR();
      { const unsigned fill = (lane & 16) ? 0x3f800000u : ONE2; const v4u o = {fill, fill, fill, fill}; if (w == 4) lds_w128(L + L_ONES + lane * 16, o); }
      if (h == 0) *(LAS unsigned*)(L + L_NM + t * 1024 + r * 4) = 0u;
      if (w >= 6) { v4u mk;
#pragma unroll
        for (int i = 0; i < 4; ++i) { const int key = ((2 * i) & 3) + 8 * (i >> 1) + 16 * (w - 6) + 4 * h; mk[i] = (key + 1 <= r) ? 0xffffffffu : ((key <= r) ? 0xffffu : 0u); }
        lds_w128(L + L_MSK + (w - 6) * 1024 + lane * 16, mk); }
      float s1 = 0.f, s2 = 0.f;
#pragma unroll
      for (int i = 0; i < 4; ++i) { s1 = dot2(xa[i], ONE2, s1); s2 = dot2(xa[i], xa[i], s2); s1 = dot2(xb[i], ONE2, s1); s2 = dot2(xb[i], xb[i], s2); }
      float m0, r0; row_stats(sum32(s1), sum32(s2), m0, r0);
      const v4u na = ln_frag(xa, m0, r0), nb = ln_frag(xb, m0, r0);
      { const lptr d = L + L_LN + (r >> 1) * 256 + (2 * t + h) * 16 + (r & 1) * 8;
        v2u u = {na[0], na[1]}; *(LAS v2u*)d = u; u[0] = na[2]; u[1] = na[3]; *(LAS v2u*)(d + 128) = u;
        u[0] = nb[0]; u[1] = nb[1]; *(LAS v2u*)(d + 4096) = u; u[0] = nb[2]; u[1] = nb[3]; *(LAS v2u*)(d + 4096 + 128) = u; }
      STAMP(56);
      ctr_arrive(L, C_ST, lane); ctr_wait(L, C_ST, 4u);
      const int vo = (lane >> 4) * 1024 + (((lane >> 3) & 1) * 32 + 8 * (j & 3) + (lane & 7)) * 16;
      const int xb0 = WS_X + (g * 4 + (j >> 2)) * 32768 + t * 8192;
      st_sc1(rs, lds_r128(L + L_LN + t * 2048 + lane * 16), vo, xb0);
      st_sc1(rs, lds_r128(L + L_LN + t * 2048 + 1024 + lane * 16), vo, xb0 + 4096);
      PIN(epoch);
      publish_wg(L, C_PX, 4u, lane, ws, WS_FX + (g * 16 + lane) * FL + j * 4, lane < 16, epoch + 1u);
      STAMP(57);
      { const unsigned voq = ((lane >> 2) * 1536 + (lane & 3) * 8) * 2;
#pragma unroll 1
        for (int n = 0; n < 8; ++n) dma_qkv(Aw + 32 * j, voq, L0, 8 * t + n);
        v4u rt[4];
#pragma unroll
        for (int i = 0; i < 4; ++i) {
          const bool po = (i >= 2) && (t & 1);
          const u16* src = po ? (t == 1 ? Ao : Bo) + (32 * g + 16 * (i & 1)) * 512
                              : (t < 2 ? Bw : Aw) + (32 * g + 16 * (i & 1)) * 1536 + (2 * (t & 1) + (i >> 1)) * 512;
          rt[i] = *(const GAS v4u*)(src + 32 * j + (lane >> 2) * (po ? 512 : 1536) + (lane & 3) * 8);
        }
        asm volatile("s_waitcnt vmcnt(4)" ::: "memory"); ctr_arrive(L, C_W, lane);
#pragma unroll
        for (int i = 0; i < 4; ++i) {
          const bool po = (i >= 2) && (t & 1);
          const int piece = (po ? (t == 1 ? T_OA : T_OB) : (t < 2 ? T_WB : T_WA) + 32 * (2 * (t & 1) + (i >> 1))) + 2 * g + (i & 1);
          st_sc1(rs, rt[i], lane * 16, WS_WT + (j * 256 + piece) * 1024);
        }
        publish_wg(L, C_PR, 4u, lane, ws, WS_FR + (lane * 16 + j) * FL + g * 4, lane < 16, epoch + 1u);
        if (t == 3) {
          poll(ws, WS_FR + (g * 16 + j) * FL, lane, lane < 16, __builtin_amdgcn_readfirstlane(epoch) + 1u, zero);
          __builtin_amdgcn_fence(__ATOMIC_ACQUIRE, "agent");
          DRAIN(); ctr_arrive(L, C_R, lane);
        }
        STAMP(58); }
#pragma unroll
      for (int q = 0; q < 4; ++q) { xres[4 * q] = bflo(xv[q][0]); xres[4 * q + 1] = bfhi(xv[q][0]); xres[4 * q + 2] = bflo(xv[q][1]); xres[4 * q + 3] = bfhi(xv[q][1]); }
    }
    PIN(epoch);
    tag = __builtin_amdgcn_readfirstlane(epoch) + 1u;
  }

  for (int pr = 0; pr < 2; ++pr) {
#pragma unroll
    for (int isB = 0; isB < 2; ++isB) {
      const int l = 2 * pr + isB;
      asm volatile("" : "+v"(lane), "+s"(b), "+s"(w));
      const int g = MAP_G(b), j = MAP_J(b);
      const int t = w & 3, hh = w >> 2, r = lane & 31, h = lane >> 5;
      const bool isQ = w < 4;
      const int gt = g * 4 + t, gj = g * 16 + j;
      const int trl = ((lane & 15) >> 2) * 64 + ((lane >> 4) & 1) * 32 + (lane & 3) * 8 + h * 256;
      const int l16 = lane * 16;
      const unsigned char* wt = ws + WS_WT + j * (256 * 1024);
      v4u qf;
      STAMP(l * 14 + 0);
      if (isQ) {
        const lptr pq = L + trl;
        lptr pv = pq + L_WV; PIN(pv);
        STAMP(l * 14 + 1);
        poll(ws, WS_FX + (l * 256 + gj) * FL, lane, lane < 16, tag, zero);
        STAMP(l * 14 + 2);
        const int xb = WS_X + l * MB2 + gt * 32768;
        f32x16 aK, aV; float rstd; f32x4 rs16[4]; v4u xf[32], xe, eq;
        if (isB) gemm_kv<true>(xf, L, pq, pv, aK, aV, rs, lane, xb, xe, eq, rstd, rs16, 3u, 4u * l + 4u, t);
        else gemm_kv<false>(xf, L, pq, pv, aK, aV, rs, lane, xb, xe, eq, rstd, rs16, 0u, 4u * l + 4u, t);
        STAMP(l * 14 + 12);
        if (!isB) {
          scale_tok(aK, rstd);
          const v4u k0 = pack8(aK, 0), k1 = pack8(aK, 8);
          const int kb = WS_KV + ((pr * 256 + gj) * 4 + t) * 4096;
          if (g < 15) { st_sc1(rs, k0, l16, kb); st_sc1(rs, k1, l16 + 1024, kb); }
          scale_feat(aV, rs16);
          const v4u vf0 = pack8(aV, 0), vf1 = pack8(aV, 8);
          if (g < 15) { st_sc1(rs, vf0, l16 + 2048, kb); st_sc1(rs, vf1, l16 + 3072, kb); }
          lds_w128(L + L_K + (8 + t) * 1024 + l16, k0); lds_w128(L + L_K + (20 + t) * 1024 + l16, k1);
          const lptr vd = L + L_V + ((r >> 4) * 12 + 8 + t) * 1024 + h * 256 + (r & 15) * 16;
          lds_w128(vd, vf0); lds_w128(vd + 512, vf1);
          STAMP(l * 14 + 4);
          f32x16 aQ = gemm_q(xf, pq, xe, eq);
          publish_wg(L, C_PH, 4u * pr + 4u, lane, ws, WS_FH + (pr * 256 + gj + 16 * (lane + 1)) * FL + lane * 4, lane < 2 && g + lane < 15, tag);
          STAMP(l * 14 + 5);
          scale_tok(aQ, rstd);
          qf = pack8(aQ, 0); lds_w128(L + L_QF + t * 1024 + l16, pack8(aQ, 8));
          if (l == 0) {
            if (t < 3) { const float c = colsum(pq + t * 32768); if (h == 0) *(LAS float*)(L + L_WX + (t * 32 + r) * 4) = c; }
            ctr_arrive(L, C_CA, lane); ctr_wait(L, C_CA, 4u); ctr_wait(L, C_R, 1u);
          } else ctr_wait(L, C_PH, 4u * pr + 4u);
          dma_tiled<PACE1>(wt, T_WB, L0, 24 * t, 24 * t + (l == 0 ? NA0 : NA1), l16);
          STAMP(l * 14 + 3);
        } else {
          scale_feat(aK, rs16); scale_feat(aV, rs16);
          const v4u k0 = phi_pack8(aK, 0), vf0 = pack8(aV, 0);
          f32x16 d = MFMA(k0, vf0, splat16(0.f));
          const v4u k1 = phi_pack8(aK, 8), vf1 = pack8(aV, 8);
          d = MFMA(k1, vf1, d);
          float z = 0.f;
#pragma unroll
          for (int i = 0; i < 4; ++i) { z = dot2(k0[i], ONE2, z); z = dot2(k1[i], ONE2, z); }
          z = half_sum(z);
          const lptr pw = L + L_PW + t * PSTR;
          if (h == 0) *(LAS float*)(pw + 2048 + r * 4) = z;
          if (r < 16) { *(LAS f32x4*)(pw + l16) = f32x4{d[0], d[1], d[2], d[3]}; *(LAS f32x4*)(pw + 1024 + l16) = f32x4{d[4], d[5], d[6], d[7]}; asm volatile("" ::: "memory"); }
          else { *(LAS f32x4*)(pw + l16) = f32x4{d[8], d[9], d[10], d[11]}; *(LAS f32x4*)(pw + 1024 + l16) = f32x4{d[12], d[13], d[14], d[15]}; }
          ctr_arrive(L, C_KV, lane);
          STAMP(l * 14 + 4);
          f32x16 aQ = gemm_q(xf, pq, xe, eq);
          if (pr == 0) ctr_arrive(L, C_QD, lane);
          scale_tok(aQ, rstd);
          qf = phi_pack8(aQ, 0); lds_w128(L + L_QF + t * 1024 + l16, phi_pack8(aQ, 8));
          STAMP(l * 14 + 5);
          if (pr == 0) {
            ctr_wait(L, C_QD, 4u);
            dma_tiled<PACE2>(wt, T_WA, L0, 24 * t, 24 * t + NB1, l16);
          }
        }
        BAR();
      } else {
        if (l > 0) {
          poll(ws, WS_FX + (l * 256 + gj) * FL, lane, lane < 16, tag, zero);
          f32x4 st[4];
#pragma unroll
          for (int i = 0; i < 4; ++i) st[i] = __builtin_bit_cast(f32x4, ld_sc1(rs, l16 + i * 1024, WS_XS + (l * 64 + gt) * 4096));
          const f32x4 a = (st[0] + st[1]) + (st[2] + st[3]);
          float m0, r0, m1, r1;
          row_stats(half_sum(x16_sum(a[0])), half_sum(x16_sum(a[1])), m0, r0);
          row_stats(half_sum(x16_sum(a[2])), half_sum(x16_sum(a[3])), m1, r1);
          if (lane < 16) { *(LAS v2u*)(L + L_NM + t * 1024 + lane * 8) = v2u{pk2(-m0, -m0), pk2(-m1, -m1)};
            *(LAS f32x2*)(L + L_RS + (t & 1) * 128 + (t >> 1) * 512 + lane * 8) = f32x2{r0, r1}; }
          ctr_arrive(L, C_ST, lane);
        }
        STAMP(l * 14 + 13);
        if (isB) {
          ctr_wait(L, C_OP, 4u * l);
          ctr_wait(L, C_XI, 4u * pr + 4u);
          dma_tiled<0>(wt, T_OB, L0 + L_WO, 8 * t, 8 * t + 8, l16);
        }
        STAMP(l * 14 + 1);
        if (!isB) {
          v4u kv[8];
          poll(ws, WS_FH + (pr * 256 + gj) * FL, lane, lane < 2 && lane < g, tag, zero);
          STAMP(l * 14 + 2);
          const v4u z4 = {0u, 0u, 0u, 0u};
#pragma unroll
          for (int q = 0; q < 2; ++q) {
            const int kb = WS_KV + ((pr * 256 + gj - 16 * (q + 1)) * 4 + t) * 4096;
            if (q < g) {
#pragma unroll
              for (int i = 0; i < 4; ++i) kv[4 * q + i] = ld_sc1(rs, l16 + i * 1024, kb);
            } else {
#pragma unroll
              for (int i = 0; i < 4; ++i) kv[4 * q + i] = z4;
            }
          }
          ctr_wait(L, C_OP, 4u * l);
#pragma unroll
          for (int q = 0; q < 2; ++q) {
            const int ti = 4 - 4 * q + t;
            lds_w128(L + L_K + ti * 1024 + l16, kv[4 * q]); lds_w128(L + L_K + (12 + ti) * 1024 + l16, kv[4 * q + 1]);
            const lptr vd = L + L_V + ((r >> 4) * 12 + ti) * 1024 + h * 256 + (r & 15) * 16;
            lds_w128(vd, kv[4 * q + 2]); lds_w128(vd + 512, kv[4 * q + 3]);
          }
          STAMP(l * 14 + 5);
          BAR();
        } else {
          if (t == 0) {
            ctr_wait(L, C_KV, 4u * pr + 4u);
            STAMP(l * 14 + 4);
            f32x4 u0 = lds_r128f(L + L_PW + l16), u1 = lds_r128f(L + L_PW + 1024 + l16);
            float z = *(const LAS float*)(L + L_PW + 2048 + r * 4);
#pragma unroll
            for (int q = 1; q < 4; ++q) { u0 += lds_r128f(L + L_PW + q * PSTR + l16); u1 += lds_r128f(L + L_PW + q * PSTR + 1024 + l16); z += *(const LAS float*)(L + L_PW + q * PSTR + 2048 + r * 4); }
            const int pb = WS_P + ((pr * 16 + j) * 16 + g) * PSTR;
            st_sc1(rs, __builtin_bit_cast(v4u, u0), l16, pb); st_sc1(rs, __builtin_bit_cast(v4u, u1), l16 + 1024, pb);
            if (h == 0) __builtin_amdgcn_raw_buffer_store_b32(__float_as_uint(z), rs, 2048 + r * 4, pb, SC1);
            publish(ws, WS_FP + (pr * 256 + lane * 16 + j) * FL + g * 4, lane < 16, tag);
            STAMP(l * 14 + 3);
          }
          poll(ws, WS_FP + (pr * 256 + gj) * FL, 4 * t + lane, lane < 4, tag, zero);
          STAMP(l * 14 + 2);
          v4u a[8]; unsigned zz[4];
#pragma unroll
          for (int q = 0; q < 4; ++q) { const int pb = WS_P + ((pr * 16 + j) * 16 + 4 * t + q) * PSTR;
            a[2 * q] = ld_sc1(rs, l16, pb); a[2 * q + 1] = ld_sc1(rs, l16 + 1024, pb); zz[q] = __builtin_amdgcn_raw_buffer_load_b32(rs, 2048 + r * 4, pb, SC1); }
          SB();
          f32x4 u0 = __builtin_bit_cast(f32x4, a[0]), u1 = __builtin_bit_cast(f32x4, a[1]); float z = __uint_as_float(zz[0]);
#pragma unroll
          for (int q = 1; q < 4; ++q) { u0 += __builtin_bit_cast(f32x4, a[2 * q]); u1 += __builtin_bit_cast(f32x4, a[2 * q + 1]); z += __uint_as_float(zz[q]); }
          const lptr pw = L + L_PR + t * PSTR;
          *(LAS f32x4*)(pw + l16) = u0; *(LAS f32x4*)(pw + 1024 + l16) = u1;
          if (h == 0) *(LAS float*)(pw + 2048 + r * 4) = z;
          DRAIN();
          STAMP(l * 14 + 5);
          BAR();
        }
        qf = lds_r128(L + L_QF + t * 1024 + l16);
      }

      STAMP(l * 14 + 6);
      f32x16 o;
      if (!isB) {

        const lptr kp = L + L_K + (hh * 12 + t) * 1024 + l16;
        const lptr vp = (r < 16 ? L + L_V + (hh * 12 + t) * 1024 + h * 256 : L + L_ONES) + (r & 15) * 16;
        const int vs = r < 16 ? 1024 : 0;
        if (g >= 2) o = attention<false>(kp, vp, vs, L + L_MSK + l16, qf, 0);
        else o = attention<true>(kp, vp, vs, L + L_MSK + l16, qf, 8 - 4 * g - t);
      } else {
        const lptr a0 = L + L_PR + (r < 16 ? (hh * 16 + r + 32 * h) * 16 : 2048 + (hh * 16 + 4 * h) * 4);
        const lptr a1 = a0 + (r < 16 ? 1024 : 32);
        f32x4 u0 = lds_r128f(a0), u1 = lds_r128f(a1);
#pragma unroll
        for (int q = 1; q < 4; ++q) { u0 += lds_r128f(a0 + q * PSTR); u1 += lds_r128f(a1 + q * PSTR); }
        const v4u af = {pk2(u0[0], u0[1]), pk2(u0[2], u0[3]), pk2(u1[0], u1[1]), pk2(u1[2], u1[3])};
        o = MFMA(af, qf, splat16(0.f));
        o[8] += 1e-6f;
      }
      { const float ri = __builtin_amdgcn_rcpf(o[8]);
#pragma unroll
        for (int i = 0; i < 8; ++i) o[i] *= ri;
        asm volatile("" :: "v"(o[0]));
        STAMP(l * 14 + 7);
        st_sc1(rs, pack8(o, 0), l16, WS_O + l * MB2 + gt * 32768 + (2 * j + hh) * 1024);
        publish_wg(L, C_PO, 8u * l + 8u, lane, ws, WS_FO + (l * 256 + g * 16 + lane) * FL + j * 4, lane < 16, tag);
      }
      STAMP(l * 14 + 8);

      if (isQ) {
        if (!isB) {
          dma_tiled<0>(wt, T_WB, L0, 24 * t + (l == 0 ? NA0 : NA1), 24 * t + 24, l16);
          ctr_wait(L, C_PO, 8u * l + 8u);
          dma_tiled<0>(wt, T_OA, L0 + L_WO, 8 * t, 8 * t + 8, l16);
          DRAIN(); ctr_arrive(L, C_WO, lane);
        } else if (pr == 0) dma_tiled<PACE2>(wt, T_WA, L0, 24 * t + NB1, 24 * t + 24, l16);
        STAMP(l * 14 + 9);
        if (l < 3) { DRAIN(); ctr_arrive(L, C_W, lane); ctr_wait(L, C_W, 4u * l + 12u); }
        if (l == 0 && t < 3) {
          const float c = colsum(L + trl + t * 32768);
          if (h == 0) *(LAS float*)(L + L_WX + 384 + (t * 32 + r) * 4) = c;
          ctr_arrive(L, C_CS, lane);
        }
      } else {
        poll(ws, WS_FO + (l * 256 + gj) * FL, lane, lane < 16, tag, zero);
        STAMP(l * 14 + 9);
        lptr po = L + L_WO + trl; PIN(po);
        const f32x16 y = gemm_out(L, po, rs, l16, WS_O + l * MB2 + gt * 32768, 4u * pr + 4u);
        ctr_arrive(L, C_OP, lane);
        asm volatile("" :: "v"(y[0]));
        STAMP(l * 14 + 10);
        v4u x0, x1; float s1 = 0.f, s2 = 0.f;
#pragma unroll
        for (int i = 0; i < 8; ++i) {
          const unsigned yb = pk2(y[2 * i], y[2 * i + 1]);
          const unsigned xb = pk2(xres[2 * i] + bflo(yb), xres[2 * i + 1] + bfhi(yb));
          xres[2 * i] = bflo(xb); xres[2 * i + 1] = bfhi(xb);
          s1 = dot2(xb, ONE2, s1); s2 = dot2(xb, xb, s2);
          if (i < 4) x0[i] = xb; else x1[i - 4] = xb;
        }
        if (l < 3) {
          const int xb = WS_X + (l + 1) * MB2 + gt * 32768 + j * 2048;
          st_sc1(rs, x0, l16, xb); st_sc1(rs, x1, l16 + 1024, xb);
          s1 = half_sum(s1); s2 = half_sum(s2);
          if (h == 0) { v2u v = {__float_as_uint(s1), __float_as_uint(s2)}; __builtin_amdgcn_raw_buffer_store_b64(v, rs, r * 8, WS_XS + (((l + 1) * 64 + gt) * 16 + j) * 256, SC1); }
          publish_wg(L, C_PX, 4u * l + 8u, lane, ws, WS_FX + ((l + 1) * 256 + g * 16 + lane) * FL + j * 4, lane < 16, tag);
        } else {
          u16* op = out + (size_t)(gt * 32 + r) * 512 + 32 * j + 4 * h;
          v2u a = {x0[0], x0[1]}, c = {x0[2], x0[3]}, d = {x1[0], x1[1]}, e = {x1[2], x1[3]};
          *(GAS v2u*)(op) = a; *(GAS v2u*)(op + 8) = c; *(GAS v2u*)(op + 16) = d; *(GAS v2u*)(op + 24) = e;
          if (b == 0 && w == 4 && lane == 0) __hip_atomic_store((gu32*)ws, tag, RLX_AGENT);
        }
      }
      STAMP(l * 14 + 11);
    }
  }
  if (blockIdx.x == 0) { while (__builtin_amdgcn_s_memrealtime() - rt0_ < 9000ull) __builtin_amdgcn_s_sleep(2); }
}

extern "C" void kernel_launch(void* const* d_in, const int* in_sizes, int n_in, void* d_out, int out_size, void* d_ws, size_t ws_size,
                              hipStream_t stream) {
  (void)in_sizes; (void)n_in; (void)out_size; (void)ws_size;
  (void)hipFuncSetAttribute((const void*)mega, hipFuncAttributeMaxDynamicSharedMemorySize, LDS_BYTES);
  mega<<<dim3(256), dim3(512), LDS_BYTES, stream>>>((const u16*)d_in[0], (const u16*)d_in[1], (const u16*)d_in[2], (const u16*)d_in[4],
                                                    (const u16*)d_in[5], (u16*)d_out, (unsigned char*)d_ws);
}
```

```cpp
#include <hip/hip_runtime.h>
#include <hip/hip_bf16.h>
#include <stdint.h>

#define DI __device__ __forceinline__
#define LAS __attribute__((address_space(3)))
#define GAS __attribute__((address_space(1)))
typedef unsigned short u16;
typedef unsigned v4u __attribute__((ext_vector_type(4)));
typedef unsigned v2u __attribute__((ext_vector_type(2)));
typedef float f32x16 __attribute__((ext_vector_type(16)));
typedef float f32x4 __attribute__((ext_vector_type(4)));
typedef float f32x2 __attribute__((ext_vector_type(2)));
typedef short bf16x8 __attribute__((ext_vector_type(8)));
typedef short s16x4 __attribute__((ext_vector_type(4)));
typedef __bf16 bf2 __attribute__((ext_vector_type(2)));
typedef GAS unsigned gu32;
typedef LAS char* lptr;
#define RLX_AGENT __ATOMIC_RELAXED, __HIP_MEMORY_SCOPE_AGENT
#define RLX_WG __ATOMIC_RELAXED, __HIP_MEMORY_SCOPE_WORKGROUP
#define MFMA(a, b, c) __builtin_amdgcn_mfma_f32_32x32x16_bf16(__builtin_bit_cast(bf16x8, (a)), __builtin_bit_cast(bf16x8, (b)), (c), 0, 0, 0)
#define SB() __builtin_amdgcn_sched_barrier(0)
#define PIN(x) asm volatile("" : "+v"(x))
#define DRAIN() asm volatile("s_waitcnt vmcnt(0)" ::: "memory")
#define LGKM0() asm volatile("s_waitcnt lgkmcnt(0)" ::: "memory")
#define BAR() asm volatile("s_waitcnt lgkmcnt(0)\n\ts_barrier" ::: "memory")
constexpr int SC1 = 16;
constexpr unsigned ONE2 = 0x3f803f80u;
#define STAMP(i) do {} while (0)
#define RT(k) do {} while (0)

#define PSL 4
#define PS0 0
#define XMAP 1
#define MAP_G(b) (4 * (((b) & 7) >> 1) + ((b) >> 6))
#define MAP_J(b) (8 * ((b) & 1) + (((b) >> 3) & 7))
constexpr int FL = 256;
constexpr int MB2 = 2 << 20;
constexpr int WS_FX = 4096;
constexpr int WS_FO = WS_FX + 4 * 256 * FL;
constexpr int WS_FH = WS_FO + 4 * 256 * FL;
constexpr int WS_FP = WS_FH + 2 * 256 * FL;
constexpr int WS_FR = WS_FP + 2 * 256 * FL;
constexpr int WS_XS = WS_FR + 256 * FL;
constexpr int PSTR = 2304;
constexpr int WS_P = WS_XS + 4 * 1024 * 256;
constexpr int WS_X = 6 << 20;
constexpr int WS_O = WS_X + 4 * MB2;
constexpr int WS_KV = WS_O + 4 * MB2;
constexpr int WS_WT = WS_KV + 4 * MB2;
constexpr int WS_END = WS_WT + 2 * MB2;
constexpr int T_WB = 0, T_WA = 96, T_OA = 192, T_OB = 224;
static_assert(WS_P + 2 * 256 * PSTR <= WS_X, "ws map");

constexpr int L_WQ = 0, L_WK = 32768, L_WV = 65536, L_WO = 98304, L_K = 131072, L_V = L_WO + 8192, L_MISC = 155648;
constexpr int L_QF = L_MISC, L_MSK = L_MISC + 4096, L_ONES = L_MISC + 6144;
constexpr int L_RS = L_ONES + 256;
constexpr int L_WX = L_MISC + 7168, L_MU = L_WX + 768, L_CTR = L_MU + 192;
constexpr int L_NM = L_QF;
constexpr int L_LN = L_K;
constexpr int L_PW = L_K, L_PR = L_K + 4 * PSTR;
constexpr int LDS_BYTES = 163840;
enum { C_W = 0, C_CS = 1, C_CA = 2, C_KV = 3, C_OP = 4, C_P = 5, C_PX = 6, C_PO = 7, C_PH = 8, C_PR = 9, C_R = 10, C_WO = 11, C_XI = 12, C_ST = 13, C_QD = 14, C_LNQ = 15 };

template <int I> struct IC { static constexpr int value = I; };
template <int N, int I = 0, class F> DI void static_for(F&& f) { if constexpr (I < N) { f(IC<I>{}); static_for<N, I + 1>(f); } }
DI unsigned pk2(float lo, float hi) { f32x2 v = {lo, hi}; return __builtin_bit_cast(unsigned, __builtin_convertvector(v, bf2)); }
DI float bflo(unsigned u) { return __uint_as_float(u << 16); }
DI float bfhi(unsigned u) { return __uint_as_float(u & 0xffff0000u); }
DI float rbf(float x) { return bflo(pk2(x, 0.f)); }
DI float dot2(unsigned a, unsigned b, float c) { return __builtin_amdgcn_fdot2_f32_bf16(__builtin_bit_cast(bf2, a), __builtin_bit_cast(bf2, b), c, false); }
DI float half_sum(float v) { auto r = __builtin_amdgcn_permlane32_swap(__float_as_uint(v), __float_as_uint(v), false, false); return __uint_as_float(r[0]) + __uint_as_float(r[1]); }
DI float half_max(float v) { auto r = __builtin_amdgcn_permlane32_swap(__float_as_uint(v), __float_as_uint(v), false, false); return __builtin_fmaxf(__uint_as_float(r[0]), __uint_as_float(r[1])); }
template <int C> DI float dpp_add(float v) { return v + __uint_as_float(__builtin_amdgcn_update_dpp(0u, __float_as_uint(v), C, 0xf, 0xf, false)); }
DI float sum32(float v) {
  v = dpp_add<0x128>(v); v = dpp_add<0x124>(v); v = dpp_add<0x122>(v); v = dpp_add<0x121>(v);
  auto r = __builtin_amdgcn_permlane16_swap(__float_as_uint(v), __float_as_uint(v), false, false);
  return __uint_as_float(r[0]) + __uint_as_float(r[1]);
}
DI float max3(float a, float b, float c) { return __builtin_fmaxf(__builtin_fmaxf(a, b), c); }
DI v4u pack8(const f32x16& a, int o) { v4u r = {pk2(a[o], a[o + 1]), pk2(a[o + 2], a[o + 3]), pk2(a[o + 4], a[o + 5]), pk2(a[o + 6], a[o + 7])}; return r; }
DI v4u lds_r128(lptr p) { return *(const LAS v4u*)p; }
DI f32x4 lds_r128f(lptr p) { return *(const LAS f32x4*)p; }
DI void lds_w128(lptr p, v4u v) { *(LAS v4u*)p = v; }
DI f32x16 splat16(float x) { f32x16 z;
#pragma unroll
  for (int i = 0; i < 16; ++i) z[i] = x;
  return z; }
DI v4u wfrag(lptr p, int off) {
  const s16x4 a = __builtin_amdgcn_ds_read_tr16_b64_v4i16((LAS s16x4*)(p + off));
  const s16x4 b = __builtin_amdgcn_ds_read_tr16_b64_v4i16((LAS s16x4*)(p + off + 512));
  const v2u x = __builtin_bit_cast(v2u, a), y = __builtin_bit_cast(v2u, b);
  v4u r = {x[0], x[1], y[0], y[1]};
  return r;
}
DI void glds16(const void* base, unsigned voff, unsigned lds_dst) {
  unsigned keep;
  asm volatile("s_mov_b32 %0, m0\n\ts_mov_b32 m0, %3\n\ts_nop 0\n\tglobal_load_lds_dwordx4 %1, %2\n\ts_mov_b32 m0, %0" : "=&s"(keep) : "v"(voff), "s"(base), "s"(lds_dst) : "memory");
}
DI void dma_qkv(const u16* Wj, unsigned voff, unsigned L0, int n) { glds16(Wj + (size_t)(n & 31) * (16 * 1536) + (n >> 5) * 512, voff, L0 + n * 1024); }
template <int PACE> DI void dma_tiled(const unsigned char* wt, int src, unsigned dst, int n4, int l16) {
#pragma unroll 1
  for (int n = 0; n < n4; ++n) {
    unsigned keep;
    asm volatile("s_mov_b32 %0, m0\n\ts_mov_b32 m0, %3\n\ts_nop 0\n\tglobal_load_lds_dwordx4 %1, %2 sc1\n\tglobal_load_lds_dwordx4 %1, %2 offset:1024 sc1\n\t"
                 "global_load_lds_dwordx4 %1, %2 offset:2048 sc1\n\tglobal_load_lds_dwordx4 %1, %2 offset:3072 sc1\n\ts_mov_b32 m0, %0"
                 : "=&s"(keep) : "v"(l16), "s"(wt + src * 1024 + n * 4096), "s"(dst + n * 4096) : "memory");
    if (PACE) __builtin_amdgcn_s_sleep(PACE);
  }
}
template <int PACE> DI void dma_rows(const u16* Wj, int ld, int p0, unsigned dst, int n4, int lane) {
  const unsigned v0 = ((lane >> 2) * ld + (lane & 3) * 8) * 2, st = 32 * ld - 1024;
  const unsigned v1 = v0 + st, v2 = v1 + st, v3 = v2 + st;
#pragma unroll 1
  for (int n = 0; n < n4; ++n) {
    const int p = p0 + 4 * n;
    unsigned keep;
    asm volatile("s_mov_b32 %0, m0\n\ts_mov_b32 m0, %6\n\ts_nop 0\n\tglobal_load_lds_dwordx4 %1, %5\n\tglobal_load_lds_dwordx4 %2, %5 offset:1024\n\t"
                 "global_load_lds_dwordx4 %3, %5 offset:2048\n\tglobal_load_lds_dwordx4 %4, %5 offset:3072\n\ts_mov_b32 m0, %0"
                 : "=&s"(keep) : "v"(v0), "v"(v1), "v"(v2), "v"(v3), "s"(Wj + (size_t)(p & 31) * 16 * ld + (p >> 5) * 512), "s"(dst + n * 4096) : "memory");
    if (PACE) __builtin_amdgcn_s_sleep(PACE);
  }
}
#define TLBPF 0
#define CODEPF 38000
#define PWAIT C_P
#define RETILE 1
#define DMA_W(PACE, T, W, ld, p0, dst, n4) dma_tiled<PACE>(wt, (T) + (p0), dst, n4, l16)
template <int N> DI void nap() { __builtin_amdgcn_s_sleep(N); }
DI void ctr_arrive(lptr L, int c, int lane) {
  unsigned a, one;
  asm volatile("v_mov_b32 %0, %2\n\tv_mov_b32 %1, 1\n\ts_mov_b64 exec, 1\n\tds_add_u32 %0, %1\n\ts_mov_b64 exec, -1"
               : "=&v"(a), "=&v"(one) : "s"((unsigned)(uintptr_t)L + L_CTR + 4 * c) : "memory");
}
DI void ctr_wait(lptr L, int c, unsigned target) {
  unsigned spins = 0;
  while (__builtin_amdgcn_readfirstlane(__hip_atomic_load((LAS unsigned*)(L + L_CTR + 4 * c), RLX_WG)) < target && ++spins < (1u << 22)) __builtin_amdgcn_s_sleep(1);
  asm volatile("" ::: "memory");
}
struct Probes { unsigned a, b, c; };
DI void retire(const Probes& q) { asm volatile("" :: "v"(q.a), "v"(q.b), "v"(q.c)); }
DI Probes poll(unsigned char* ws, int line_off, int lane, int n, unsigned tag, unsigned zero) {
  unsigned v0 = zero, v1 = zero, v2 = zero;
  if (lane < n) {
    gu32* p = (gu32*)(ws + line_off) + lane;
#define PROBE() __hip_atomic_fetch_add(p, zero, RLX_AGENT)
#define SEEN(v) (__builtin_amdgcn_ballot_w64((v) != tag) == 0ull)
    __builtin_amdgcn_s_sleep(PS0);
    v0 = PROBE();
    __builtin_amdgcn_s_sleep(PSL);
    v1 = PROBE();
    __builtin_amdgcn_s_sleep(PSL);
    v2 = PROBE();
    if (!SEEN(v0)) { v0 = PROBE();
      if (!SEEN(v1)) { v1 = PROBE();
        if (!SEEN(v2)) { v2 = PROBE();
          if (!SEEN(v0)) { v0 = PROBE();
            if (!SEEN(v1)) { v1 = PROBE();
              if (!SEEN(v2)) { v2 = PROBE();
#pragma unroll 4
                for (unsigned spins = 0; spins < (1u << 13); ++spins) {
                  if (SEEN(v0)) break;
                  v0 = PROBE(); __builtin_amdgcn_s_sleep(PSL);
                  if (SEEN(v1)) break;
                  v1 = PROBE(); __builtin_amdgcn_s_sleep(PSL);
                  if (SEEN(v2)) break;
                  v2 = PROBE(); __builtin_amdgcn_s_sleep(PSL);
                } } } } } } }
#undef PROBE
#undef SEEN
  }
  __builtin_amdgcn_fence(__ATOMIC_ACQUIRE, "wavefront");
  asm volatile("" ::: "memory");
  return Probes{v0, v1, v2};
}
DI void publish(unsigned char* ws, int word_off, bool active, unsigned tag) {
  DRAIN();
  if (active) (void)__hip_atomic_exchange((gu32*)(ws + word_off), tag, RLX_AGENT);
}
DI void publish_wg(lptr L, int c, unsigned target, int lane, unsigned char* ws, int word_off, bool active, unsigned tag) {
  DRAIN();
  unsigned old = 0;
  if (lane == 0) old = __hip_atomic_fetch_add((LAS unsigned*)(L + L_CTR + 4 * c), 1u, RLX_WG);
  if (__builtin_amdgcn_readfirstlane(old) + 1u == target && active) (void)__hip_atomic_exchange((gu32*)(ws + word_off), tag, RLX_AGENT);
}
DI v4u ld_sc1(__amdgpu_buffer_rsrc_t rs, int voff, int soff) { return __builtin_amdgcn_raw_buffer_load_b128(rs, voff, soff, SC1); }
DI void st_sc1(__amdgpu_buffer_rsrc_t rs, v4u v, int voff, int soff) { __builtin_amdgcn_raw_buffer_store_b128(v, rs, voff + soff, 0, SC1); }

DI void blank(__amdgpu_buffer_rsrc_t rs, int voff, int soff) {
  unsigned o; asm volatile("v_mov_b32 %0, -1" : "=v"(o));
  st_sc1(rs, v4u{o, o, o, o}, voff, soff);
}
DI unsigned umax3(unsigned a, unsigned b, unsigned c) { return __builtin_elementwise_max(__builtin_elementwise_max(a, b), c); }
DI unsigned chk4(unsigned m, v4u v) { return umax3(umax3(m, v[0], v[1]), v[2], v[3]); }
DI bool again(bool bad, unsigned& tries) { if (!__any(bad) || ++tries > 512u) return false; __builtin_amdgcn_s_sleep(4); asm volatile("" ::: "memory"); return true; }
DI bool again(unsigned m, unsigned& tries) { return again(m == 0xffffffffu, tries); }
DI bool isnan_(float v) { return __builtin_isnan(v); }
DI float add1(float a, float b) { float r; asm("v_add_f32 %0, %1, %2" : "=v"(r) : "v"(a), "v"(b)); return r; }
DI void announce(unsigned char* ws, int word_off, bool active, unsigned tag) { if (active) (void)__hip_atomic_exchange((gu32*)(ws + word_off), tag, RLX_AGENT); }

DI void row_stats(float s1, float s2, float& mu, float& rs) {
  const float m = s1 * (1.0f / 512.0f);
  const float var = __builtin_fmaxf(__builtin_fmaf(-m, m, s2 * (1.0f / 512.0f)), 0.f);
  mu = rbf(m);
  rs = __builtin_amdgcn_rcpf(rbf(__builtin_amdgcn_sqrtf(rbf(rbf(var) + 1e-5f))));
}
DI v4u ln_frag(v4u x, float mu, float rs) {
  v4u r;
#pragma unroll
  for (int i = 0; i < 4; ++i) { const unsigned d = pk2(bflo(x[i]) - mu, bfhi(x[i]) - mu); r[i] = pk2(bflo(d) * rs, bfhi(d) * rs); }
  return r;
}
DI void scale_tok(f32x16& a, float rs) {
#pragma unroll
  for (int i = 0; i < 16; ++i) a[i] *= rs;
}
DI void scale_feat(f32x16& a, const f32x4 (&u)[4]) {
#pragma unroll
  for (int i = 0; i < 16; ++i) a[i] *= u[i >> 2][i & 3];
}
DI float phi1(float t) { return (__builtin_fmaxf(t, 0.f) + __builtin_amdgcn_exp2f(__builtin_fminf(t, 0.f) * 1.4426950408889634f)) + 1e-6f; }
DI v4u phi_pack8(const f32x16& a, int o) {
  v4u r;
#pragma unroll
  for (int i = 0; i < 4; ++i) { const unsigned t = pk2(a[o + 2 * i], a[o + 2 * i + 1]); r[i] = pk2(phi1(bflo(t)), phi1(bfhi(t))); }
  return r;
}
#define EARLY1 1
#define OPRIO 3
#define APRIO 0
#define NEARLY 3
#define PACE2 0
#define NB1 3
#define NB2 15
#define PACE1 0
#define NA1 6
#define NA0 8
#define PFX 14
#define PF0X 8
constexpr int PF = PFX, PF0 = PF0X;
DI v4u ld_frag(__amdgpu_buffer_rsrc_t rs, int l16, int base, int s) { return ld_sc1(rs, l16 + (s & 3) * 1024, base + (s >> 2) * 4096); }
DI f32x16 gemm_out(lptr L, lptr p, __amdgpu_buffer_rsrc_t rs, int l16, int base, unsigned wo_target, bool tell) {
  constexpr int D = 5, P = 16;
  v4u wr[D + 1], xr[P + 1];
#pragma unroll
  for (int s = 0; s < P; ++s) xr[s] = ld_frag(rs, l16, base, s);
  SB();
  ctr_wait(L, C_WO, wo_target);
#pragma unroll
  for (int s = 0; s < D; ++s) wr[s] = wfrag(p, s * 1024);
  SB();
  f32x16 a = splat16(0.f);
#pragma unroll
  for (int s = 0; s < 32; ++s) {
    if (s + P < 32) xr[(s + P) % (P + 1)] = ld_frag(rs, l16, base, s + P);
    if (s + P == 32 && tell) ctr_arrive(L, C_OP, 0);
    if (s + D < 32) wr[(s + D) % (D + 1)] = wfrag(p, (s + D) * 1024);
    a = MFMA(wr[s % (D + 1)], xr[s % (P + 1)], a);
    SB();
  }
  return a;
}
DI float colsum(lptr p) {
  constexpr int D = 5;
  v4u wr[D + 1];
  const v4u ones = {ONE2, ONE2, ONE2, ONE2};
#pragma unroll
  for (int s = 0; s < D; ++s) wr[s] = wfrag(p, s * 1024);
  SB();
  f32x16 a = splat16(0.f);
#pragma unroll
  for (int s = 0; s < 32; ++s) {
    if (s + D < 32) wr[(s + D) % (D + 1)] = wfrag(p, (s + D) * 1024);
    a = MFMA(ones, wr[s % (D + 1)], a);
    SB();
  }
  return a[0];
}
DI float x16_sum(float v) { auto r = __builtin_amdgcn_permlane16_swap(__float_as_uint(v), __float_as_uint(v), false, false); return __uint_as_float(r[0]) + __uint_as_float(r[1]); }
DI v2u split3(float c) { const float hi = rbf(c), mid = rbf(c - hi), lo = (c - hi) - mid; return v2u{pk2(hi, mid), pk2(lo, 0.f)}; }
DI v4u wide(v2u v) { return v4u{v[0], v[1], 0u, 0u}; }
template <bool ISB> DI void gemm_kv(v4u (&xf)[32], lptr L, lptr pq, lptr pv, f32x16& aK, f32x16& aV, __amdgpu_buffer_rsrc_t rs, int lane, int base,
                                    unsigned& nm, v2u& eq, float& rstd, f32x4 (&rs16)[4], unsigned cs_target, unsigned st_target, int t, bool tell, const Probes& pb) {
  constexpr int D = 3;
  const int l16 = lane * 16, r = lane & 31, h = lane >> 5;
  v4u wk[D + 1], wv[D + 1];
#pragma unroll
  for (int s = 0; s < PF0; ++s) xf[s] = ld_frag(rs, l16, base, s);
  SB();
  ctr_wait(L, C_CS, cs_target);
  const lptr rp = L + L_RS + (t & 1) * 128 + (t >> 1) * 512, np = L + L_NM + t * 1024, cs = L + L_WX + (ISB ? 384 : 0) + r * 4;
  v2u ek = split3(*(const LAS float*)(cs + 128)), ev = split3(*(const LAS float*)(cs + 256));
  eq = split3(*(const LAS float*)cs);
  PIN(ek); PIN(ev); PIN(eq);
#pragma unroll
  for (int s = 0; s < D; ++s) { wk[s] = wfrag(pq, L_WK + s * 1024); wv[s] = wfrag(pv, s * 1024); }
  SB();
  aK = splat16(0.f); aV = splat16(0.f);
  unsigned cv = 0;
  static_for<32>([&](auto SI) __attribute__((always_inline)) {
    constexpr int s = decltype(SI)::value;
    if constexpr (s + D < 32) wk[(s + D) % (D + 1)] = wfrag(pq, L_WK + (s + D) * 1024);
    if constexpr (s < PF - PF0) xf[PF0 + 2 * s] = ld_frag(rs, l16, base, PF0 + 2 * s);
    if constexpr (s == 24) cv = __hip_atomic_load((LAS unsigned*)(L + L_CTR + 4 * C_ST), RLX_WG);
    if constexpr (s == 28) {
      if (__builtin_amdgcn_readfirstlane(cv) < st_target) ctr_wait(L, C_ST, st_target);
      asm volatile("" ::: "memory");
      nm = *(const LAS unsigned*)(np + r * 4); rstd = *(const LAS float*)(rp + r * 4);
    }
    if constexpr (s == 29 || s == 30) rs16[s - 29] = lds_r128f(rp + h * 16 + (s - 29) * 32);
    if constexpr (s == 31) { nm = h ? 0u : nm; const v4u xe = {nm, nm & 0xffffu, 0u, 0u}; aK = ISB ? MFMA(xe, wide(ek), aK) : MFMA(wide(ek), xe, aK); aV = MFMA(xe, wide(ev), aV); }
    aK = ISB ? MFMA(xf[s], wk[s % (D + 1)], aK) : MFMA(wk[s % (D + 1)], xf[s], aK);
    SB();
    if constexpr (s + D < 32) wv[(s + D) % (D + 1)] = wfrag(pv, (s + D) * 1024);
    if constexpr (s < PF - PF0) xf[PF0 + 2 * s + 1] = ld_frag(rs, l16, base, PF0 + 2 * s + 1);
    else if constexpr (s + PF < 32) xf[s + PF] = ld_frag(rs, l16, base, s + PF);
    if constexpr (s + PF == 32) { if (tell) ctr_arrive(L, C_XI, lane); }
    if constexpr (s == 29 || s == 30) rs16[s - 27] = lds_r128f(rp + h * 16 + (s - 27) * 32);
    aV = MFMA(xf[s], wv[s % (D + 1)], aV);
    if constexpr (s == 0) retire(pb);
    SB();
  });
}
DI f32x16 gemm_q(const v4u (&xf)[32], lptr p, unsigned nm, v2u eq) {
  constexpr int D = 5;
  v4u wr[D + 1];
#pragma unroll
  for (int s = 0; s < D; ++s) wr[s] = wfrag(p, s * 1024);
  const v4u xe = {nm, nm & 0xffffu, 0u, 0u};
  f32x16 a = MFMA(wide(eq), xe, splat16(0.f));
  SB();
#pragma unroll
  for (int s = 0; s < 32; ++s) {
    if (s + D < 32) wr[(s + D) % (D + 1)] = wfrag(p, (s + D) * 1024);
    a = MFMA(wr[s % (D + 1)], xf[s], a);
    SB();
  }
  return a;
}

constexpr unsigned long long diag_mask(int i) { return ((unsigned long long)(0xffffffffu << ((i & 3) + 8 * (i >> 2) + 4)) << 32) | (0xffffffffu << ((i & 3) + 8 * (i >> 2))); }
template <bool EDGE>
DI f32x16 attention(lptr kp, lptr vp, int vs, lptr mp, v4u qf, int nv) {
  v4u kf[9];
  kf[8] = lds_r128(kp + 8 * 1024);
#pragma unroll
  for (int n = 0; n < 8; ++n) kf[n] = lds_r128(kp + n * 1024);
  v4u va = lds_r128(vp), vb = lds_r128(vp + 512);
  SB();
  f32x16 s[9];
  float m0, m1, m2, m3;
#define SC(n) s[n] = MFMA(kf[n], qf, splat16(EDGE ? ((n) < nv ? -3.0e38f : 0.f) : 0.f))
#define MX(n) do { m0 = max3(m0, s[n][0], s[n][1]); m1 = max3(m1, s[n][2], s[n][3]); m2 = max3(m2, s[n][4], s[n][5]); m3 = max3(m3, s[n][6], s[n][7]); \
    m0 = max3(m0, s[n][8], s[n][9]); m1 = max3(m1, s[n][10], s[n][11]); m2 = max3(m2, s[n][12], s[n][13]); m3 = max3(m3, s[n][14], s[n][15]); } while (0)
#define MG(i) s[0][i] = __builtin_amdgcn_inverse_ballot_w64(diag_mask(i)) ? s[8][i] : s[0][i]
  s[8] = MFMA(kf[8], qf, splat16(0.f)); SC(0); SC(1); SB();
  SC(2); MG(0); MG(1); MG(2); MG(3); MG(4); MG(5); MG(6); MG(7); SB();
  SC(3); MG(8); MG(9); MG(10); MG(11); MG(12); MG(13); MG(14); MG(15); SB();
  SC(4); m0 = max3(s[1][0], s[1][1], s[1][2]); m1 = max3(s[1][3], s[1][4], s[1][5]); m2 = max3(s[1][6], s[1][7], s[1][8]); m3 = max3(s[1][9], s[1][10], s[1][11]);
  m0 = max3(m0, s[1][12], s[1][13]); m1 = max3(m1, s[1][14], s[1][15]); SB();
  SC(5); MX(0); SB();
  SC(6); MX(2); SB();
  SC(7); MX(3); SB();
  const v4u mlo = lds_r128(mp), mhi = lds_r128(mp + 1024);
  const v4u da = lds_r128(vp + 8 * vs), db = lds_r128(vp + 8 * vs + 512);
  MX(4); MX(5); MX(6); MX(7);
#undef SC
#undef MX
#undef MG
  constexpr float C = 0.25f * 1.4426950408889634f;
  const float nm = -C * half_max(__builtin_fmaxf(max3(m0, m1, m2), m3));
  f32x2 C2 = {C, C}, nm2 = {nm, nm};
  f32x16 o = splat16(0.f);
#pragma unroll
  for (int n = 0; n < 8; ++n) {
    v4u na, nb;
    if (n < 7) { vp += vs; na = lds_r128(vp); nb = lds_r128(vp + 512); }
    f32x2 e[8];
#pragma unroll
    for (int i = 0; i < 8; ++i) { const f32x2 v = {s[n][2 * i], s[n][2 * i + 1]}; e[i] = __builtin_elementwise_fma(v, C2, nm2); }
#pragma unroll
    for (int i = 0; i < 8; ++i) PIN(e[i]);
    v4u p0, p1;
#pragma unroll
    for (int i = 0; i < 4; ++i) p0[i] = pk2(__builtin_amdgcn_exp2f(e[i][0]), __builtin_amdgcn_exp2f(e[i][1]));
#pragma unroll
    for (int i = 0; i < 4; ++i) p1[i] = pk2(__builtin_amdgcn_exp2f(e[4 + i][0]), __builtin_amdgcn_exp2f(e[4 + i][1]));
    if (n == 0) {
      const v4u d0 = p0 & mlo, d1 = p1 & mhi;
      o = MFMA(da, d0, o); o = MFMA(va, p0 ^ d0, o); o = MFMA(db, d1, o); o = MFMA(vb, p1 ^ d1, o);
    } else { o = MFMA(va, p0, o); o = MFMA(vb, p1, o); }
    if (n < 7) { va = na; vb = nb; }
    SB();
  }
  return o;
}

__global__ void __launch_bounds__(512, 2) mega(const u16* __restrict__ x_in, const u16* __restrict__ Aw, const u16* __restrict__ Ao,
                                               const u16* __restrict__ Bw, const u16* __restrict__ Bo, u16* __restrict__ out, unsigned char* ws) {
  extern __shared__ __attribute__((aligned(16))) char smem[];
  const lptr L = (lptr)smem;
  const unsigned L0 = (unsigned)(uintptr_t)L;
  int b = blockIdx.x, lane = threadIdx.x & 63, w = __builtin_amdgcn_readfirstlane(threadIdx.x >> 6);
  const __amdgpu_buffer_rsrc_t rs = __builtin_amdgcn_make_buffer_rsrc(ws, 0, WS_END, 0x00020000);
  STAMP(63);
  unsigned zero = 0; PIN(zero);
  unsigned tag;
  v4u x0 = {0u, 0u, 0u, 0u}, x1 = x0;

  {
    const int g = MAP_G(b), j = MAP_J(b), t = w & 3, r = lane & 31, h = lane >> 5;
    unsigned epoch = __hip_atomic_load((gu32*)ws, RLX_AGENT);
    if (w < 4) {
      unsigned tlb = 0;
      if (w == 0) { const v4u z = {0u, 0u, 0u, 0u}; lds_w128(L + L_WX + lane * 16, z); }
      BAR();
      unsigned warm = 0;
      if (w == 1 && lane < 48) warm = *(const GAS unsigned*)((const char*)Aw + (b * 48 + lane) * 128);
      if (w == 2 && lane < 48) warm = *(const GAS unsigned*)((const char*)Bw + (b * 48 + lane) * 128);
      if (w == 3 && lane < 32) warm = *(const GAS unsigned*)((const char*)(lane < 16 ? Ao : Bo) + (b * 16 + (lane & 15)) * 128);
      if (w == 0) { unsigned long long pc; asm volatile("s_getpc_b64 %0" : "=s"(pc));
        const int o = ((b >> 3) * 11 + lane) * 128; if (lane < 11 && o < CODEPF) warm = *(const GAS unsigned*)((const char*)(pc & ~127ull) + o); }
      if (NEARLY) dma_rows<0>(Aw + 32 * j, 1536, 32 + 16 * t, L0 + 32768 + 16384 * t, NEARLY, lane);
      if (NEARLY < 4) ctr_wait(L, PWAIT, 4u);
      dma_rows<0>(Aw + 32 * j, 1536, 32 + 16 * t + 4 * NEARLY, L0 + 32768 + 16384 * t + 4096 * NEARLY, 4 - NEARLY, lane);
      ctr_arrive(L, C_PR, lane);
      blank(rs, lane * 16, WS_O + (g * 4 + t) * 32768 + 2 * j * 1024);
#pragma unroll
      for (int i = 0; i < 4; ++i) blank(rs, lane * 16 + i * 1024, WS_KV + ((g * 16 + j) * 4 + t) * 4096);
      asm volatile("s_waitcnt vmcnt(5)" :: "v"(warm), "v"(tlb) : "memory");
      STAMP(59);
      ctr_arrive(L, C_R, lane); ctr_wait(L, C_R, 5u);
    } else {
      __builtin_amdgcn_s_setprio(3);
      const u16* xrow = x_in + (size_t)(g * 128 + 8 * j + 2 * t + h) * 512 + 8 * r;
      const v4u xa = *(const GAS v4u*)xrow, xb = *(const GAS v4u*)(xrow + 256);
      const u16* xr = x_in + (size_t)(g * 128 + t * 32 + r) * 512 + 32 * j + 4 * h;
      v2u xv[4];
#pragma unroll
      for (int q = 0; q < 4; ++q) xv[q] = *(const GAS v2u*)(xr + 8 * q);
      BAR();
      { const unsigned fill = (lane & 16) ? 0x3f800000u : ONE2; const v4u o = {fill, fill, fill, fill}; if (w == 4) lds_w128(L + L_ONES + lane * 16, o); }
      if (h == 0) *(LAS unsigned*)(L + L_NM + t * 1024 + r * 4) = 0u;
      if (w >= 6) { v4u mk;
#pragma unroll
        for (int i = 0; i < 4; ++i) { const int key = ((2 * i) & 3) + 8 * (i >> 1) + 16 * (w - 6) + 4 * h; mk[i] = (key + 1 <= r) ? 0xffffffffu : ((key <= r) ? 0xffffu : 0u); }
        lds_w128(L + L_MSK + (w - 6) * 1024 + lane * 16, mk); }
      float s1 = 0.f, s2 = 0.f;
#pragma unroll
      for (int i = 0; i < 4; ++i) { s1 = dot2(xa[i], ONE2, s1); s2 = dot2(xa[i], xa[i], s2); s1 = dot2(xb[i], ONE2, s1); s2 = dot2(xb[i], xb[i], s2); }
      float m0, r0; row_stats(sum32(s1), sum32(s2), m0, r0);
      const v4u na = ln_frag(xa, m0, r0), nb = ln_frag(xb, m0, r0);
      { const lptr d = L + L_LN + (r >> 1) * 256 + (2 * t + h) * 16 + (r & 1) * 8;
        v2u u = {na[0], na[1]}; *(LAS v2u*)d = u; u[0] = na[2]; u[1] = na[3]; *(LAS v2u*)(d + 128) = u;
        u[0] = nb[0]; u[1] = nb[1]; *(LAS v2u*)(d + 4096) = u; u[0] = nb[2]; u[1] = nb[3]; *(LAS v2u*)(d + 4096 + 128) = u; }
      STAMP(56);
      ctr_arrive(L, C_ST, lane); ctr_wait(L, C_ST, 4u);
      const int vo = (lane >> 4) * 1024 + (((lane >> 3) & 1) * 32 + 8 * (j & 3) + (lane & 7)) * 16;
      const int xb0 = WS_X + (g * 4 + (j >> 2)) * 32768 + t * 8192;
      st_sc1(rs, lds_r128(L + L_LN + t * 2048 + lane * 16), vo, xb0);
      st_sc1(rs, lds_r128(L + L_LN + t * 2048 + 1024 + lane * 16), vo, xb0 + 4096);
      PIN(epoch);
      publish_wg(L, C_PX, 4u, lane, ws, WS_FX + (g * 16 + lane) * FL + j * 4, lane < 16, epoch + 1u);
      ctr_arrive(L, C_P, lane);
      STAMP(57);
      if (t == 3) { (void)poll(ws, WS_FX + (g * 16 + j) * FL, lane, 16, __builtin_amdgcn_readfirstlane(epoch) + 1u, zero); ctr_arrive(L, C_R, lane); STAMP(59); }
      __builtin_amdgcn_s_setprio(0);
      ctr_wait(L, C_PR, 4u);
      v4u rt[4];
#pragma unroll
      for (int i = 0; i < 4; ++i) {
        const bool po = (i >= 2) && (t & 1);
        const u16* src = po ? (t == 1 ? Ao : Bo) + (32 * g + 16 * (i & 1)) * 512
                            : (t < 2 ? Bw : Aw) + (32 * g + 16 * (i & 1)) * 1536 + (2 * (t & 1) + (i >> 1)) * 512;
        rt[i] = *(const GAS v4u*)(src + 32 * j + (lane >> 2) * (po ? 512 : 1536) + (lane & 3) * 8);
      }
      STAMP(58);
      ctr_wait(L, C_XI, 4u);
      dma_rows<0>(Aw + 32 * j, 1536, 8 * t, L0 + 8192 * t, 2, lane);
#pragma unroll
      for (int i = 0; i < 4; ++i) {
        const bool po = (i >= 2) && (t & 1);
        const int piece = (po ? (t == 1 ? T_OA : T_OB) : (t < 2 ? T_WB : T_WA) + 32 * (2 * (t & 1) + (i >> 1))) + 2 * g + (i & 1);
        st_sc1(rs, rt[i], lane * 16, WS_WT + (j * 256 + piece) * 1024);
      }
      x0 = v4u{xv[0][0], xv[0][1], xv[1][0], xv[1][1]}; x1 = v4u{xv[2][0], xv[2][1], xv[3][0], xv[3][1]};
    }
    PIN(epoch);
    tag = __builtin_amdgcn_readfirstlane(epoch) + 1u;
  }

  for (int pr = 0; pr < 2; ++pr) {
#pragma unroll
    for (int isB = 0; isB < 2; ++isB) {
      const int l = 2 * pr + isB;
      asm volatile("" : "+v"(lane), "+s"(b), "+s"(w));
      const int g = MAP_G(b), j = MAP_J(b);
      const int t = w & 3, hh = w >> 2, r = lane & 31, h = lane >> 5;
      const bool isQ = w < 4;
      const int gt = g * 4 + t, gj = g * 16 + j;
      const int trl = ((lane & 15) >> 2) * 64 + ((lane >> 4) & 1) * 32 + (lane & 3) * 8 + h * 256;
      const int l16 = lane * 16;
      const unsigned char* wt = ws + WS_WT + j * (256 * 1024);
      v4u qf;
      if (isQ) {
        const lptr pq = L + trl;
        lptr pv = pq + L_WV; PIN(pv);
        STAMP(l * 14 + 1);
        Probes pb_ = poll(ws, WS_FX + (l * 256 + gj) * FL, lane, l > 0 ? 16 : 0, tag, zero);
        STAMP(l * 14 + 2);
        const int xb = WS_X + l * MB2 + gt * 32768;
        f32x16 aK, aV; float rstd; f32x4 rs16[4]; v4u xf[32]; unsigned xe; v2u eq;
        unsigned tries = 0;
        v4u k0, k1, vf0, vf1; f32x16 d;
        do {
          if (!isB) gemm_kv<false>(xf, L, pq, pv, aK, aV, rs, lane, xb, xe, eq, rstd, rs16, 0u, 4u * l + 4u, t, tries == 0, pb_);
          else {
            gemm_kv<true>(xf, L, pq, pv, aK, aV, rs, lane, xb, xe, eq, rstd, rs16, 3u, 4u * l + 4u, t, tries == 0, pb_);
            scale_feat(aK, rs16); scale_feat(aV, rs16);
            k0 = phi_pack8(aK, 0); vf0 = pack8(aV, 0);
            d = MFMA(k0, vf0, splat16(0.f));
            k1 = phi_pack8(aK, 8); vf1 = pack8(aV, 8);
            d = MFMA(k1, vf1, d);
          }
          pb_ = Probes{0u, 0u, 0u};
        } while (again(isnan_(isB ? d[0] : aK[0]), tries));
        RT(0);
        STAMP(l * 14 + 12);
        if (!isB) {
          DRAIN();
          BAR();
          scale_tok(aK, rstd);
          k0 = pack8(aK, 0); k1 = pack8(aK, 8);
          const int kb = WS_KV + ((pr * 256 + gj) * 4 + t) * 4096;
          if (g < 15) { st_sc1(rs, k0, l16, kb); st_sc1(rs, k1, l16 + 1024, kb); }
          scale_feat(aV, rs16);
          vf0 = pack8(aV, 0); vf1 = pack8(aV, 8);
          if (g < 15) { st_sc1(rs, vf0, l16 + 2048, kb); st_sc1(rs, vf1, l16 + 3072, kb); }
          STAMP(l * 14 + 4);
          lds_w128(L + L_K + (8 + t) * 1024 + l16, k0); lds_w128(L + L_K + (20 + t) * 1024 + l16, k1);
          const lptr vd = L + L_V + ((r >> 4) * 12 + 8 + t) * 1024 + h * 256 + (r & 15) * 16;
          lds_w128(vd, vf0); lds_w128(vd + 512, vf1);
          ctr_wait(L, C_LNQ, 4u);
          f32x16 aQ = gemm_q(xf, pq, xe, eq);
          scale_tok(aQ, rstd);
          qf = pack8(aQ, 0); lds_w128(L + L_QF + t * 1024 + l16, pack8(aQ, 8));
          STAMP(l * 14 + 5);
        } else {
          float z = 0.f;
#pragma unroll
          for (int i = 0; i < 4; ++i) { z = dot2(k0[i], ONE2, z); z = dot2(k1[i], ONE2, z); }
          z = half_sum(z);
          const lptr pw = L + L_PW + t * PSTR;
          if (h == 0) *(LAS float*)(pw + 2048 + r * 4) = z;
          if (r < 16) { *(LAS f32x4*)(pw + l16) = f32x4{d[0], d[1], d[2], d[3]}; *(LAS f32x4*)(pw + 1024 + l16) = f32x4{d[4], d[5], d[6], d[7]}; asm volatile("" ::: "memory"); }
          else { *(LAS f32x4*)(pw + l16) = f32x4{d[8], d[9], d[10], d[11]}; *(LAS f32x4*)(pw + 1024 + l16) = f32x4{d[12], d[13], d[14], d[15]}; }
          BAR();
          STAMP(l * 14 + 4);
          f32x16 aQ = gemm_q(xf, pq, xe, eq);
          if (pr == 0) ctr_arrive(L, C_QD, lane);
          scale_tok(aQ, rstd);
          qf = phi_pack8(aQ, 0); lds_w128(L + L_QF + t * 1024 + l16, phi_pack8(aQ, 8));
          STAMP(l * 14 + 5);
          if (pr == 0) {
            ctr_wait(L, C_QD, 4u);
            DMA_W(PACE2, T_WA, Aw, 1536, 24 * t, L0 + 24576 * t, NB1);
          }
        }
        BAR();
      } else {
        __builtin_amdgcn_s_setprio(OPRIO);
        if (l > 0) {
          const Probes pb_ = poll(ws, WS_FX + (l * 256 + gj) * FL, lane, 16, tag, zero);
          v4u st[4];
          unsigned tries = 0, m;
          do {
#pragma unroll
            for (int i = 0; i < 4; ++i) st[i] = ld_sc1(rs, l16 + i * 1024, WS_XS + (l * 64 + gt) * 4096);
            m = chk4(chk4(chk4(chk4(0u, st[0]), st[1]), st[2]), st[3]);
          } while (again(m, tries));
          retire(pb_);
          RT(1);
          const f32x4 a = (__builtin_bit_cast(f32x4, st[0]) + __builtin_bit_cast(f32x4, st[1])) + (__builtin_bit_cast(f32x4, st[2]) + __builtin_bit_cast(f32x4, st[3]));
          float m0, r0, m1, r1;
          row_stats(half_sum(x16_sum(a[0])), half_sum(x16_sum(a[1])), m0, r0);
          row_stats(half_sum(x16_sum(a[2])), half_sum(x16_sum(a[3])), m1, r1);
          if (lane < 16) { *(LAS v2u*)(L + L_NM + t * 1024 + lane * 8) = v2u{pk2(-m0, -m0), pk2(-m1, -m1)};
            *(LAS f32x2*)(L + L_RS + (t & 1) * 128 + (t >> 1) * 512 + lane * 8) = f32x2{r0, r1}; }
          ctr_arrive(L, C_ST, lane);
        }
        STAMP(l * 14 + 13);
        ctr_wait(L, C_XI, 4u * l + 4u);
        if (isB) DMA_W(0, T_OB, Bo, 512, 8 * t, L0 + L_WO + 8192 * t, 2);
        blank(rs, l16, WS_O + l * MB2 + gt * 32768 + (2 * j + 1) * 1024);
        if (l < 3) {
          blank(rs, l16, WS_X + (l + 1) * MB2 + gt * 32768 + j * 2048); blank(rs, l16 + 1024, WS_X + (l + 1) * MB2 + gt * 32768 + j * 2048);
          if (lane < 16) blank(rs, l16, WS_XS + (((l + 1) * 64 + gt) * 16 + j) * 256);
        }
        if (!isB && t == 0) { const int pb = WS_P + ((pr * 16 + j) * 16 + g) * PSTR; blank(rs, l16, pb); blank(rs, l16 + 1024, pb); if (lane < 8) blank(rs, l16 + 2048, pb); }
        STAMP(l * 14 + 1);
        BAR();
        STAMP(l * 14 + 4);
        if (!isB) {
          if (t == 0) announce(ws, WS_FH + (pr * 256 + gj + 16 * (lane + 1)) * FL + lane * 4, lane < 2 && g + lane < 15, tag);
          if (pr == 0) { DRAIN(); ctr_arrive(L, C_LNQ, lane); }
          v4u kv[8];
          const Probes pb_ = poll(ws, WS_FH + (pr * 256 + gj) * FL, lane, g < 2 ? g : 2, tag, zero);
          STAMP(l * 14 + 2);
          const v4u z4 = {0u, 0u, 0u, 0u};
          unsigned tries = 0, m;
          do {
#pragma unroll
            for (int q = 0; q < 2; ++q) {
              const int kb = WS_KV + ((pr * 256 + gj - 16 * (q + 1)) * 4 + t) * 4096;
              if (q < g) {
#pragma unroll
                for (int i = 0; i < 4; ++i) kv[4 * q + i] = ld_sc1(rs, l16 + i * 1024, kb);
              } else {
#pragma unroll
                for (int i = 0; i < 4; ++i) kv[4 * q + i] = z4;
              }
            }
            m = 0;
#pragma unroll
            for (int q = 0; q < 2; ++q) {
              const int ti = 4 - 4 * q + t;
              lds_w128(L + L_K + ti * 1024 + l16, kv[4 * q]); lds_w128(L + L_K + (12 + ti) * 1024 + l16, kv[4 * q + 1]);
              const lptr vd = L + L_V + ((r >> 4) * 12 + ti) * 1024 + h * 256 + (r & 15) * 16;
              lds_w128(vd, kv[4 * q + 2]); lds_w128(vd + 512, kv[4 * q + 3]);
#pragma unroll
              for (int i = 0; i < 4; ++i) m = chk4(m, kv[4 * q + i]);
            }
          } while (again(m, tries));
          retire(pb_);
          RT(2);
          DRAIN();
          STAMP(l * 14 + 5);
          BAR();
          if (RETILE && t == 1 && pr == 0) announce(ws, WS_FR + (lane * 16 + j) * FL + g * 4, lane < 16, tag);
        } else {
          { const int pb = WS_P + ((pr * 16 + j) * 16 + g) * PSTR;
            if (t < 3) {
              const lptr pp = L + L_PW + t * 1024 + l16;
              f32x4 u[4];
#pragma unroll
              for (int q = 0; q < 4; ++q) u[q] = lds_r128f(pp + q * PSTR);
              SB();
              f32x4 sm;
#pragma unroll
              for (int i = 0; i < 4; ++i) sm[i] = add1(add1(u[0][i], u[1][i]), add1(u[2][i], u[3][i]));
              if (t < 2 || lane < 8) st_sc1(rs, __builtin_bit_cast(v4u, sm), l16 + t * 1024, pb);
            } else announce(ws, WS_FP + (pr * 256 + lane * 16 + j) * FL + g * 4, lane < 16, tag);
            STAMP(l * 14 + 3); }
          const Probes pb_ = poll(ws, WS_FP + (pr * 256 + gj) * FL + 16 * t, lane, 4, tag, zero);
          STAMP(l * 14 + 2);
          v4u a[8]; unsigned zz[4];
          unsigned tries = 0, m;
          do {
#pragma unroll
            for (int q = 0; q < 4; ++q) { const int pb = WS_P + ((pr * 16 + j) * 16 + 4 * t + q) * PSTR;
              a[2 * q] = ld_sc1(rs, l16, pb); a[2 * q + 1] = ld_sc1(rs, l16 + 1024, pb); zz[q] = __builtin_amdgcn_raw_buffer_load_b32(rs, 2048 + r * 4, pb, SC1); }
            SB();
            m = umax3(umax3(zz[0], zz[1], zz[2]), zz[3], 0u);
#pragma unroll
            for (int i = 0; i < 8; ++i) m = chk4(m, a[i]);
          } while (again(m, tries));
          retire(pb_);
          RT(3);
          f32x4 u0 = __builtin_bit_cast(f32x4, a[0]), u1 = __builtin_bit_cast(f32x4, a[1]); float z = __uint_as_float(zz[0]);
#pragma unroll
          for (int q = 1; q < 4; ++q) {
#pragma unroll
            for (int i = 0; i < 4; ++i) { u0[i] = add1(u0[i], __uint_as_float(a[2 * q][i])); u1[i] = add1(u1[i], __uint_as_float(a[2 * q + 1][i])); }
            z = add1(z, __uint_as_float(zz[q])); }
          const lptr pw = L + L_PR + t * PSTR;
          *(LAS f32x4*)(pw + l16) = u0; *(LAS f32x4*)(pw + 1024 + l16) = u1;
          if (h == 0) *(LAS float*)(pw + 2048 + r * 4) = z;
          STAMP(l * 14 + 5);
          BAR();
        }
        qf = lds_r128(L + L_QF + t * 1024 + l16);
        __builtin_amdgcn_s_setprio(APRIO);
      }

      STAMP(l * 14 + 6);
      f32x16 o;
      if (!isB) {
        const lptr kp = L + L_K + (hh * 12 + t) * 1024 + l16;
        const lptr vp = (r < 16 ? L + L_V + (hh * 12 + t) * 1024 + h * 256 : L + L_ONES) + (r & 15) * 16;
        const int vs = r < 16 ? 1024 : 0;
        if (g >= 2) o = attention<false>(kp, vp, vs, L + L_MSK + l16, qf, 0);
        else o = attention<true>(kp, vp, vs, L + L_MSK + l16, qf, 8 - 4 * g - t);
      } else {
        const lptr a0 = L + L_PR + (r < 16 ? (hh * 16 + r + 32 * h) * 16 : 2048 + (hh * 16 + 4 * h) * 4);
        const lptr a1 = a0 + (r < 16 ? 1024 : 32);
        f32x4 u0 = lds_r128f(a0), u1 = lds_r128f(a1);
#pragma unroll
        for (int q = 1; q < 4; ++q) { u0 += lds_r128f(a0 + q * PSTR); u1 += lds_r128f(a1 + q * PSTR); }
        const v4u af = {pk2(u0[0], u0[1]), pk2(u0[2], u0[3]), pk2(u1[0], u1[1]), pk2(u1[2], u1[3])};
        o = MFMA(af, qf, splat16(0.f));
        o[8] += 1e-6f;
      }
      { const float ri = __builtin_amdgcn_rcpf(o[8]);
#pragma unroll
        for (int i = 0; i < 8; ++i) o[i] *= ri;
        asm volatile("" :: "v"(o[0]));
        STAMP(l * 14 + 7);
        DRAIN();
        st_sc1(rs, pack8(o, 0), l16, WS_O + l * MB2 + gt * 32768 + (2 * j + hh) * 1024);
      }

      if (isQ) {
        if (!isB) {
          if (pr == 0) {
            if (t < 3) { const float c = colsum(L + trl + t * 32768); if (h == 0) *(LAS float*)(L + L_WX + (t * 32 + r) * 4) = c; }
            else (void)poll(ws, WS_FR + gj * FL, lane, 16, tag, zero);
            ctr_arrive(L, C_CA, lane); ctr_wait(L, C_CA, 4u);
          }
          DMA_W(0, T_WB, Bw, 1536, 24 * t, L0 + 24576 * t, NA1);
        }
        STAMP(l * 14 + 8);
        BAR();
        if (t == 0) announce(ws, WS_FO + (l * 256 + g * 16 + lane) * FL + j * 4, lane < 16, tag); else nap<1>();
        if (!isB) {
          DMA_W(0, T_OA, Ao, 512, 8 * t, L0 + L_WO + 8192 * t, 2);
          DRAIN(); ctr_arrive(L, C_WO, lane);
          DMA_W(PACE1, T_WB, Bw, 1536, 24 * t + 4 * NA1, L0 + 24576 * t + 4096 * NA1, 6 - NA1);
        } else if (pr == 0) DMA_W(PACE2, T_WA, Aw, 1536, 24 * t + 4 * NB1, L0 + 24576 * t + 4096 * NB1, 6 - NB1);
        STAMP(l * 14 + 9);
        if (l < 3) {
          DRAIN(); ctr_arrive(L, C_W, lane); ctr_wait(L, C_W, 4u * l + 4u);
          if (l == 0 && t < 3) {
            const float c = colsum(L + trl + t * 32768);
            if (h == 0) *(LAS float*)(L + L_WX + 384 + (t * 32 + r) * 4) = c;
            ctr_arrive(L, C_CS, lane);
          }
          ctr_wait(L, C_OP, 4u * l + 4u);
          blank(rs, l16, WS_O + (l + 1) * MB2 + gt * 32768 + 2 * j * 1024);
          if (l == 1) {
#pragma unroll
            for (int i = 0; i < 4; ++i) blank(rs, l16 + i * 1024, WS_KV + ((256 + gj) * 4 + t) * 4096);
          }
          STAMP(l * 14 + 11);
          BAR();
          if (t == 0) announce(ws, WS_FX + ((l + 1) * 256 + g * 16 + lane) * FL + j * 4, lane < 16, tag);
        }
      } else {
        __builtin_amdgcn_s_setprio(OPRIO);
        STAMP(l * 14 + 8);
        BAR();
        const Probes pb_ = poll(ws, WS_FO + (l * 256 + gj) * FL, lane, 16, tag, zero);
        STAMP(l * 14 + 9);
        lptr po = L + L_WO + trl; PIN(po);
        f32x16 y; unsigned tries = 0;
        do y = gemm_out(L, po, rs, l16, WS_O + l * MB2 + gt * 32768, 4u * pr + 4u, tries == 0); while (again(isnan_(y[0]), tries));
        retire(pb_);
        RT(4);
        asm volatile("" :: "v"(y[0]));
        STAMP(l * 14 + 10);
        float s1 = 0.f, s2 = 0.f;
#pragma unroll
        for (int i = 0; i < 8; ++i) {
          const unsigned yb = pk2(y[2 * i], y[2 * i + 1]);
          const unsigned xo = i < 4 ? x0[i & 3] : x1[i & 3];
          const unsigned xb = pk2(bflo(xo) + bflo(yb), bfhi(xo) + bfhi(yb));
          s1 = dot2(xb, ONE2, s1); s2 = dot2(xb, xb, s2);
          if (i < 4) x0[i] = xb; else x1[i - 4] = xb;
        }
        if (l < 3) {
          const int xb = WS_X + (l + 1) * MB2 + gt * 32768 + j * 2048;
          st_sc1(rs, x0, l16, xb); st_sc1(rs, x1, l16 + 1024, xb);
          s1 = half_sum(s1); s2 = half_sum(s2);
          if (h == 0) { v2u v = {__float_as_uint(s1), __float_as_uint(s2)}; __builtin_amdgcn_raw_buffer_store_b64(v, rs, r * 8, WS_XS + (((l + 1) * 64 + gt) * 16 + j) * 256, SC1); }
          STAMP(l * 14 + 11);
          BAR();
        } else {
          u16* op = out + (size_t)(gt * 32 + r) * 512 + 32 * j + 4 * h;
          v2u a = {x0[0], x0[1]}, c = {x0[2], x0[3]}, d = {x1[0], x1[1]}, e = {x1[2], x1[3]};
          *(GAS v2u*)(op) = a; *(GAS v2u*)(op + 8) = c; *(GAS v2u*)(op + 16) = d; *(GAS v2u*)(op + 24) = e;
          if (b == 0 && w == 4 && lane == 0) __hip_atomic_store((gu32*)ws, tag, RLX_AGENT);
        }
      }
    }
  }
}

extern "C" void kernel_launch(void* const* d_in, const int* in_sizes, int n_in, void* d_out, int out_size, void* d_ws, size_t ws_size,
                              hipStream_t stream) {
  (void)in_sizes; (void)n_in; (void)out_size; (void)ws_size;
  (void)hipFuncSetAttribute((const void*)mega, hipFuncAttributeMaxDynamicSharedMemorySize, LDS_BYTES);
  mega<<<dim3(256), dim3(512), LDS_BYTES, stream>>>((const u16*)d_in[0], (const u16*)d_in[1], (const u16*)d_in[2], (const u16*)d_in[4],
                                                    (const u16*)d_in[5], (u16*)d_out, (unsigned char*)d_ws);
}
```

```cpp
#include <hip/hip_runtime.h>
#include <hip/hip_bf16.h>
#include <stdint.h>

#define DI __device__ __forceinline__
#define LAS __attribute__((address_space(3)))
#define GAS __attribute__((address_space(1)))
typedef unsigned short u16;
typedef unsigned v4u __attribute__((ext_vector_type(4)));
typedef unsigned v2u __attribute__((ext_vector_type(2)));
typedef float f32x16 __attribute__((ext_vector_type(16)));
typedef float f32x4 __attribute__((ext_vector_type(4)));
typedef float f32x2 __attribute__((ext_vector_type(2)));
typedef short bf16x8 __attribute__((ext_vector_type(8)));
typedef short s16x4 __attribute__((ext_vector_type(4)));
typedef __bf16 bf2 __attribute__((ext_vector_type(2)));
typedef GAS unsigned gu32;
typedef LAS char* lptr;
#define RLX_AGENT __ATOMIC_RELAXED, __HIP_MEMORY_SCOPE_AGENT
#define RLX_WG __ATOMIC_RELAXED, __HIP_MEMORY_SCOPE_WORKGROUP
#define MFMA(a, b, c) __builtin_amdgcn_mfma_f32_32x32x16_bf16(__builtin_bit_cast(bf16x8, (a)), __builtin_bit_cast(bf16x8, (b)), (c), 0, 0, 0)
#define SB() __builtin_amdgcn_sched_barrier(0)
#define PIN(x) asm volatile("" : "+v"(x))
#define DRAIN() asm volatile("s_waitcnt vmcnt(0)" ::: "memory")
#define LGKM0() asm volatile("s_waitcnt lgkmcnt(0)" ::: "memory")
#define BAR() do { SB(); asm volatile("s_waitcnt lgkmcnt(0)\n\ts_barrier" ::: "memory"); SB(); } while (0)
constexpr int SC1 = 16;
constexpr unsigned ONE2 = 0x3f803f80u;
#define STAMP(i) do {} while (0)
#define RT(k) do {} while (0)

#define PSL 4
#define PS0 0
#define XMAP 1
#define MAP_G(b) (4 * (((b) & 7) >> 1) + ((b) >> 6))
#define MAP_J(b) (8 * ((b) & 1) + (((b) >> 3) & 7))
constexpr int FL = 256;
constexpr int MB2 = 2 << 20;
constexpr int WS_FX = 4096;
constexpr int WS_FO = WS_FX + 4 * 256 * FL;
constexpr int WS_FH = WS_FO + 4 * 256 * FL;
constexpr int WS_FP = WS_FH + 2 * 256 * FL;
constexpr int WS_FR = WS_FP + 2 * 256 * FL;
constexpr int WS_XS = WS_FR + 256 * FL;
constexpr int PSTR = 2304;
constexpr int WS_P = WS_XS + 4 * 1024 * 256;
constexpr int WS_X = 6 << 20;
constexpr int WS_O = WS_X + 4 * MB2;
constexpr int WS_KV = WS_O + 4 * MB2;
constexpr int WS_WT = WS_KV + 4 * MB2;
constexpr int WS_END = WS_WT + 2 * MB2;
constexpr int T_WB = 0, T_WA = 96, T_OA = 192, T_OB = 224;
static_assert(WS_P + 2 * 256 * PSTR <= WS_X, "ws map");

constexpr int L_WQ = 0, L_WK = 32768, L_WV = 65536, L_WO = 98304, L_K = 131072, L_V = L_WO + 8192, L_MISC = 155648;
constexpr int L_QF = L_MISC, L_MSK = L_MISC + 4096, L_ONES = L_MISC + 6144;
constexpr int L_RS = L_ONES + 256;
constexpr int L_WX = L_MISC + 7168, L_MU = L_WX + 768, L_CTR = L_MU + 192;
constexpr int L_NM = L_QF;
constexpr int L_LN = L_K;
constexpr int L_PW = L_K, L_PR = L_K + 4 * PSTR;
constexpr int LDS_BYTES = 163840;
enum { C_W = 0, C_CS = 1, C_CA = 2, C_KV = 3, C_OP = 4, C_P = 5, C_PX = 6, C_PO = 7, C_PH = 8, C_PR = 9, C_R = 10, C_WO = 11, C_XI = 12, C_ST = 13, C_QD = 14, C_LNQ = 15 };

template <int I> struct IC { static constexpr int value = I; };
template <int N, int I = 0, class F> DI void static_for(F&& f) { if constexpr (I < N) { f(IC<I>{}); static_for<N, I + 1>(f); } }
DI unsigned pk2(float lo, float hi) { f32x2 v = {lo, hi}; return __builtin_bit_cast(unsigned, __builtin_convertvector(v, bf2)); }
DI float bflo(unsigned u) { return __uint_as_float(u << 16); }
DI float bfhi(unsigned u) { return __uint_as_float(u & 0xffff0000u); }
DI float rbf(float x) { return bflo(pk2(x, 0.f)); }
DI float dot2(unsigned a, unsigned b, float c) { return __builtin_amdgcn_fdot2_f32_bf16(__builtin_bit_cast(bf2, a), __builtin_bit_cast(bf2, b), c, false); }
DI float half_sum(float v) { auto r = __builtin_amdgcn_permlane32_swap(__float_as_uint(v), __float_as_uint(v), false, false); return __uint_as_float(r[0]) + __uint_as_float(r[1]); }
DI float half_max(float v) { auto r = __builtin_amdgcn_permlane32_swap(__float_as_uint(v), __float_as_uint(v), false, false); return __builtin_fmaxf(__uint_as_float(r[0]), __uint_as_float(r[1])); }
template <int C> DI float dpp_add(float v) { return v + __uint_as_float(__builtin_amdgcn_update_dpp(0u, __float_as_uint(v), C, 0xf, 0xf, false)); }
DI float sum32(float v) {
  v = dpp_add<0x128>(v); v = dpp_add<0x124>(v); v = dpp_add<0x122>(v); v = dpp_add<0x121>(v);
  auto r = __builtin_amdgcn_permlane16_swap(__float_as_uint(v), __float_as_uint(v), false, false);
  return __uint_as_float(r[0]) + __uint_as_float(r[1]);
}
DI float max3(float a, float b, float c) { return __builtin_fmaxf(__builtin_fmaxf(a, b), c); }
DI v4u pack8(const f32x16& a, int o) { v4u r = {pk2(a[o], a[o + 1]), pk2(a[o + 2], a[o + 3]), pk2(a[o + 4], a[o + 5]), pk2(a[o + 6], a[o + 7])}; return r; }
DI v4u lds_r128(lptr p) { return *(const LAS v4u*)p; }
DI f32x4 lds_r128f(lptr p) { return *(const LAS f32x4*)p; }
DI void lds_w128(lptr p, v4u v) { *(LAS v4u*)p = v; }
DI f32x16 splat16(float x) { f32x16 z;
#pragma unroll
  for (int i = 0; i < 16; ++i) z[i] = x;
  return z; }
DI v4u wfrag(lptr p, int off) {
  const s16x4 a = __builtin_amdgcn_ds_read_tr16_b64_v4i16((LAS s16x4*)(p + off));
  const s16x4 b = __builtin_amdgcn_ds_read_tr16_b64_v4i16((LAS s16x4*)(p + off + 512));
  const v2u x = __builtin_bit_cast(v2u, a), y = __builtin_bit_cast(v2u, b);
  v4u r = {x[0], x[1], y[0], y[1]};
  return r;
}
DI void glds16(const void* base, unsigned voff, unsigned lds_dst) {
  unsigned keep;
  asm volatile("s_mov_b32 %0, m0\n\ts_mov_b32 m0, %3\n\ts_nop 0\n\tglobal_load_lds_dwordx4 %1, %2\n\ts_mov_b32 m0, %0" : "=&s"(keep) : "v"(voff), "s"(base), "s"(lds_dst) : "memory");
}
DI void dma_qkv(const u16* Wj, unsigned voff, unsigned L0, int n) { glds16(Wj + (size_t)(n & 31) * (16 * 1536) + (n >> 5) * 512, voff, L0 + n * 1024); }
template <int PACE> DI void dma_tiled(const unsigned char* wt, int src, unsigned dst, int n4, int l16) {
#pragma unroll 1
  for (int n = 0; n < n4; ++n) {
    unsigned keep;
    asm volatile("s_mov_b32 %0, m0\n\ts_mov_b32 m0, %3\n\ts_nop 0\n\tglobal_load_lds_dwordx4 %1, %2 sc1\n\tglobal_load_lds_dwordx4 %1, %2 offset:1024 sc1\n\t"
                 "global_load_lds_dwordx4 %1, %2 offset:2048 sc1\n\tglobal_load_lds_dwordx4 %1, %2 offset:3072 sc1\n\ts_mov_b32 m0, %0"
                 : "=&s"(keep) : "v"(l16), "s"(wt + src * 1024 + n * 4096), "s"(dst + n * 4096) : "memory");
    if (PACE) __builtin_amdgcn_s_sleep(PACE);
  }
}
template <int PACE> DI void dma_rows(const u16* Wj, int ld, int p0, unsigned dst, int n4, int lane, int rot = 0) {
  const unsigned v0 = ((lane >> 2) * ld + (lane & 3) * 8) * 2, st = 32 * ld - 1024;
  const unsigned v1 = v0 + st, v2 = v1 + st, v3 = v2 + st;
#pragma unroll 1
  for (int i = 0; i < n4; ++i) {
    const int n = (i + rot) & (n4 - 1 | -(rot == 0));
    const int p = p0 + 4 * n;
    unsigned keep;
    asm volatile("s_mov_b32 %0, m0\n\ts_mov_b32 m0, %6\n\ts_nop 0\n\tglobal_load_lds_dwordx4 %1, %5\n\tglobal_load_lds_dwordx4 %2, %5 offset:1024\n\t"
                 "global_load_lds_dwordx4 %3, %5 offset:2048\n\tglobal_load_lds_dwordx4 %4, %5 offset:3072\n\ts_mov_b32 m0, %0"
                 : "=&s"(keep) : "v"(v0), "v"(v1), "v"(v2), "v"(v3), "s"(Wj + (size_t)(p & 31) * 16 * ld + (p >> 5) * 512), "s"(dst + n * 4096) : "memory");
    if (PACE) __builtin_amdgcn_s_sleep(PACE);
  }
}
#define TLBPF 0
#define FLAGPF 1
#define CODEPF 40700
#define PWAIT C_P
#define ROT 1
#define RETILE 1
#define DMA_W(PACE, T, W, ld, p0, dst, n4) dma_tiled<PACE>(wt, (T) + (p0), dst, n4, l16)
template <int N> DI void nap() { __builtin_amdgcn_s_sleep(N); }
DI void ctr_arrive(lptr L, int c, int lane) {
  unsigned a, one;
  asm volatile("v_mov_b32 %0, %2\n\tv_mov_b32 %1, 1\n\ts_mov_b64 exec, 1\n\tds_add_u32 %0, %1\n\ts_mov_b64 exec, -1"
               : "=&v"(a), "=&v"(one) : "s"((unsigned)(uintptr_t)L + L_CTR + 4 * c) : "memory");
}
DI void ctr_wait(lptr L, int c, unsigned target) {
  unsigned spins = 0;
  while (__builtin_amdgcn_readfirstlane(__hip_atomic_load((LAS unsigned*)(L + L_CTR + 4 * c), RLX_WG)) < target && ++spins < (1u << 22)) __builtin_amdgcn_s_sleep(1);
  asm volatile("" ::: "memory");
}
struct Probes { unsigned a, b, c; };
DI void retire(const Probes& q) { asm volatile("" :: "v"(q.a), "v"(q.b), "v"(q.c)); }
DI Probes poll(unsigned char* ws, int line_off, int lane, int n, unsigned tag, unsigned zero) {
  unsigned v0 = zero, v1 = zero, v2 = zero;
  if (lane < n) {
    gu32* p = (gu32*)(ws + line_off) + lane;
#define PROBE() __hip_atomic_fetch_add(p, zero, RLX_AGENT)
#define SEEN(v) (__builtin_amdgcn_ballot_w64((v) != tag) == 0ull)
    __builtin_amdgcn_s_sleep(PS0);
    v0 = PROBE();
    __builtin_amdgcn_s_sleep(PSL);
    v1 = PROBE();
    __builtin_amdgcn_s_sleep(PSL);
    v2 = PROBE();
    if (!SEEN(v0)) { v0 = PROBE();
      if (!SEEN(v1)) { v1 = PROBE();
        if (!SEEN(v2)) { v2 = PROBE();
          if (!SEEN(v0)) { v0 = PROBE();
            if (!SEEN(v1)) { v1 = PROBE();
              if (!SEEN(v2)) { v2 = PROBE();
#pragma unroll 4
                for (unsigned spins = 0; spins < (1u << 13); ++spins) {
                  if (SEEN(v0)) break;
                  v0 = PROBE(); __builtin_amdgcn_s_sleep(PSL);
                  if (SEEN(v1)) break;
                  v1 = PROBE(); __builtin_amdgcn_s_sleep(PSL);
                  if (SEEN(v2)) break;
                  v2 = PROBE(); __builtin_amdgcn_s_sleep(PSL);
                } } } } } } }
#undef PROBE
#undef SEEN
  }
  __builtin_amdgcn_fence(__ATOMIC_ACQUIRE, "wavefront");
  asm volatile("" ::: "memory");
  return Probes{v0, v1, v2};
}
DI void publish(unsigned char* ws, int word_off, bool active, unsigned tag) {
  DRAIN();
  if (active) (void)__hip_atomic_exchange((gu32*)(ws + word_off), tag, RLX_AGENT);
}
DI void publish_wg(lptr L, int c, unsigned target, int lane, unsigned char* ws, int word_off, bool active, unsigned tag) {
  DRAIN();
  unsigned old = 0;
  if (lane == 0) old = __hip_atomic_fetch_add((LAS unsigned*)(L + L_CTR + 4 * c), 1u, RLX_WG);
  if (__builtin_amdgcn_readfirstlane(old) + 1u == target && active) (void)__hip_atomic_exchange((gu32*)(ws + word_off), tag, RLX_AGENT);
}
DI v4u ld_sc1(__amdgpu_buffer_rsrc_t rs, int voff, int soff) { return __builtin_amdgcn_raw_buffer_load_b128(rs, voff, soff, SC1); }
DI void st_sc1(__amdgpu_buffer_rsrc_t rs, v4u v, int voff, int soff) { __builtin_amdgcn_raw_buffer_store_b128(v, rs, voff + soff, 0, SC1); }

DI void blank(__amdgpu_buffer_rsrc_t rs, int voff, int soff) {
  unsigned o; asm volatile("v_mov_b32 %0, -1" : "=v"(o));
  st_sc1(rs, v4u{o, o, o, o}, voff, soff);
}
DI unsigned umax3(unsigned a, unsigned b, unsigned c) { return __builtin_elementwise_max(__builtin_elementwise_max(a, b), c); }
DI unsigned chk4(unsigned m, v4u v) { return umax3(umax3(m, v[0], v[1]), v[2], v[3]); }
DI bool again(bool bad, unsigned& tries) { if (!__any(bad) || ++tries > 512u) return false; __builtin_amdgcn_s_sleep(4); asm volatile("" ::: "memory"); return true; }
DI bool again(unsigned m, unsigned& tries) { return again(m == 0xffffffffu, tries); }
DI bool isnan_(float v) { return __builtin_isnan(v); }
DI float add1(float a, float b) { float r; asm("v_add_f32 %0, %1, %2" : "=v"(r) : "v"(a), "v"(b)); return r; }
DI void announce(unsigned char* ws, int word_off, bool active, unsigned tag) { if (active) (void)__hip_atomic_exchange((gu32*)(ws + word_off), tag, RLX_AGENT); }

DI void row_stats(float s1, float s2, float& mu, float& rs) {
  const float m = s1 * (1.0f / 512.0f);
  const float var = __builtin_fmaxf(__builtin_fmaf(-m, m, s2 * (1.0f / 512.0f)), 0.f);
  mu = rbf(m);
  rs = __builtin_amdgcn_rcpf(rbf(__builtin_amdgcn_sqrtf(rbf(rbf(var) + 1e-5f))));
}
DI v4u ln_frag(v4u x, float mu, float rs) {
  v4u r;
#pragma unroll
  for (int i = 0; i < 4; ++i) { const unsigned d = pk2(bflo(x[i]) - mu, bfhi(x[i]) - mu); r[i] = pk2(bflo(d) * rs, bfhi(d) * rs); }
  return r;
}
DI void scale_tok(f32x16& a, float rs) {
#pragma unroll
  for (int i = 0; i < 16; ++i) a[i] *= rs;
}
DI void scale_feat(f32x16& a, const f32x4 (&u)[4]) {
#pragma unroll
  for (int i = 0; i < 16; ++i) a[i] *= u[i >> 2][i & 3];
}
DI unsigned phi2(float a0, float a1) {
    const unsigned t = pk2(a0, a1);
    const f32x2 v = {bflo(t), bfhi(t)}, u = v * 1.4426950408889634f;
    const f32x2 e = {__builtin_amdgcn_fmed3f(__builtin_amdgcn_exp2f(u[0]), 0.f, 1.f), __builtin_amdgcn_fmed3f(__builtin_amdgcn_exp2f(u[1]), 0.f, 1.f)};
    float p0, p1; asm("v_max_f32 %0, 0, %1" : "=v"(p0) : "v"(v[0])); asm("v_max_f32 %0, 0, %1" : "=v"(p1) : "v"(v[1]));
    const f32x2 y = (f32x2{p0, p1} + e) + 1e-6f;
    return pk2(y[0], y[1]);
}
DI v4u phi_pack8(const f32x16& a, int o) { return v4u{phi2(a[o], a[o + 1]), phi2(a[o + 2], a[o + 3]), phi2(a[o + 4], a[o + 5]), phi2(a[o + 6], a[o + 7])}; }
#define B4S 32
#define B1S 8
#define OBD -1
#define B3N 6
#define WOD 4
#define EARLY3 1
#define EARLY4 1
#define EARLY1 1
#define OPRIO 3
#define APRIO 0
#define NEARLY 4
#define PACE2 0
#define NB1 3
#define NB2 15
#define PACE1 0
#define NA1 6
#define NA0 8
#define PFX 14
#define PF0X 8
constexpr int PF = PFX, PF0 = PF0X;
DI v4u ld_frag(__amdgpu_buffer_rsrc_t rs, int l16, int base, int s) { return ld_sc1(rs, l16 + (s & 3) * 1024, base + (s >> 2) * 4096); }
DI f32x16 gemm_out(lptr L, lptr p, __amdgpu_buffer_rsrc_t rs, int l16, int base, unsigned wo_target, bool tell, bool bar) {
  constexpr int D = 5, P = 16;
  v4u wr[D + 1], xr[P + 1];
#pragma unroll
  for (int s = 0; s < P; ++s) xr[s] = ld_frag(rs, l16, base, s);
  SB();
  ctr_wait(L, C_WO, wo_target);
#pragma unroll
  for (int s = 0; s < D; ++s) wr[s] = wfrag(p, s * 1024);
  SB();
  f32x16 a = splat16(0.f);
#pragma unroll
  for (int s = 0; s < 32; ++s) {
    if (s + P < 32) xr[(s + P) % (P + 1)] = ld_frag(rs, l16, base, s + P);
    if (s + P == 32 && tell) ctr_arrive(L, C_OP, 0);
    if (s + D < 32) wr[(s + D) % (D + 1)] = wfrag(p, (s + D) * 1024);
    a = MFMA(wr[s % (D + 1)], xr[s % (P + 1)], a);
    SB();
    if (s == B4S && bar) { asm volatile("s_barrier" ::: "memory"); SB(); }
  }
  return a;
}
DI float colsum(lptr p) {
  constexpr int D = 5;
  v4u wr[D + 1];
  const v4u ones = {ONE2, ONE2, ONE2, ONE2};
#pragma unroll
  for (int s = 0; s < D; ++s) wr[s] = wfrag(p, s * 1024);
  SB();
  f32x16 a = splat16(0.f);
#pragma unroll
  for (int s = 0; s < 32; ++s) {
    if (s + D < 32) wr[(s + D) % (D + 1)] = wfrag(p, (s + D) * 1024);
    a = MFMA(ones, wr[s % (D + 1)], a);
    SB();
  }
  return a[0];
}
DI float x16_sum(float v) { auto r = __builtin_amdgcn_permlane16_swap(__float_as_uint(v), __float_as_uint(v), false, false); return __uint_as_float(r[0]) + __uint_as_float(r[1]); }
DI v2u split3(float c) { const float hi = rbf(c), mid = rbf(c - hi), lo = (c - hi) - mid; return v2u{pk2(hi, mid), pk2(lo, 0.f)}; }
DI v4u wide(v2u v) { return v4u{v[0], v[1], 0u, 0u}; }
constexpr int KS = 24;
template <bool ISB, class F> DI void gemm_kv(v4u (&xf)[32], lptr L, lptr pq, lptr pv, f32x16& aK, f32x16& aV, __amdgpu_buffer_rsrc_t rs, int lane, int base,
                                    unsigned& nm, v2u& eq, float& rstd, f32x4 (&rs16)[4], unsigned cs_target, unsigned st_target, int t, bool tell, const Probes& pb, F&& kpart) {
  constexpr int D = 3;
  const int l16 = lane * 16, r = lane & 31, h = lane >> 5;
  v4u wk[D + 1], wv[D + 1];
#pragma unroll
  for (int s = 0; s < PF0; ++s) xf[s] = ld_frag(rs, l16, base, s);
  SB();
  const lptr rp = L + L_RS + (t & 1) * 128 + (t >> 1) * 512, np = L + L_NM + t * 1024, cs = L + L_WX + (ISB ? 384 : 0) + r * 4;
  v2u ek = split3(*(const LAS float*)(cs + 128)), ev = split3(*(const LAS float*)(cs + 256));
  eq = split3(*(const LAS float*)cs);
  PIN(ek); PIN(ev); PIN(eq);
#pragma unroll
  for (int s = 0; s < D; ++s) { wk[s] = wfrag(pq, L_WK + s * 1024); wv[s] = wfrag(pv, s * 1024); }
  SB();
  aK = splat16(0.f); aV = splat16(0.f);
  unsigned cv = 0;
#define MK(s) aK = ISB ? MFMA(xf[s], wk[(s) % (D + 1)], aK) : MFMA(wk[(s) % (D + 1)], xf[s], aK)
  static_for<KS>([&](auto SI) __attribute__((always_inline)) {
    constexpr int s = decltype(SI)::value;
    wk[(s + D) % (D + 1)] = wfrag(pq, L_WK + (s + D) * 1024);
    if constexpr (s < PF - PF0) xf[PF0 + 2 * s] = ld_frag(rs, l16, base, PF0 + 2 * s);
    if constexpr (s == KS - 3) cv = __hip_atomic_load((LAS unsigned*)(L + L_CTR + 4 * C_ST), RLX_WG);
    MK(s);
    SB();
    wv[(s + D) % (D + 1)] = wfrag(pv, (s + D) * 1024);
    if constexpr (s < PF - PF0) xf[PF0 + 2 * s + 1] = ld_frag(rs, l16, base, PF0 + 2 * s + 1);
    else if constexpr (s + PF < 32) xf[s + PF] = ld_frag(rs, l16, base, s + PF);
    if constexpr (s + PF == 32) { if (tell) ctr_arrive(L, C_XI, lane); }
    aV = MFMA(xf[s], wv[s % (D + 1)], aV);
    if constexpr (s == 0) retire(pb);
    SB();
  });
  static_for<32 - KS>([&](auto SI) __attribute__((always_inline)) {
    constexpr int s = KS + decltype(SI)::value;
    if constexpr (s + D < 32) wk[(s + D) % (D + 1)] = wfrag(pq, L_WK + (s + D) * 1024);
    if constexpr (s == 27) {
      if (__builtin_amdgcn_readfirstlane(cv) < st_target) ctr_wait(L, C_ST, st_target);
      asm volatile("" ::: "memory");
      nm = *(const LAS unsigned*)(np + r * 4); rstd = *(const LAS float*)(rp + r * 4);
    }
    if constexpr (s == 29) { rs16[0] = lds_r128f(rp + h * 16); rs16[1] = lds_r128f(rp + h * 16 + 32); }
    if constexpr (s == 30) { rs16[2] = lds_r128f(rp + h * 16 + 64); rs16[3] = lds_r128f(rp + h * 16 + 96); }
    if constexpr (s == 31) { nm = h ? 0u : nm; const v4u xe = {nm, nm & 0xffffu, 0u, 0u}; aK = ISB ? MFMA(xe, wide(ek), aK) : MFMA(wide(ek), xe, aK); }
    MK(s);
    SB();
  });
#undef MK
  static_for<32 - KS>([&](auto SI) __attribute__((always_inline)) {
    constexpr int i = decltype(SI)::value, s = KS + i;
    if constexpr (s + D < 32) wv[(s + D) % (D + 1)] = wfrag(pv, (s + D) * 1024);
    if constexpr (i == 0) { const v4u xe = {nm, nm & 0xffffu, 0u, 0u}; aV = MFMA(xe, wide(ev), aV); }
    aV = MFMA(xf[s], wv[s % (D + 1)], aV);
    kpart(SI);
    SB();
  });
}
DI f32x16 gemm_q(const v4u (&xf)[32], lptr p, unsigned nm, v2u eq) {
  constexpr int D = 5;
  v4u wr[D + 1];
#pragma unroll
  for (int s = 0; s < D; ++s) wr[s] = wfrag(p, s * 1024);
  const v4u xe = {nm, nm & 0xffffu, 0u, 0u};
  f32x16 a = MFMA(wide(eq), xe, splat16(0.f));
  SB();
#pragma unroll
  for (int s = 0; s < 32; ++s) {
    if (s + D < 32) wr[(s + D) % (D + 1)] = wfrag(p, (s + D) * 1024);
    a = MFMA(wr[s % (D + 1)], xf[s], a);
    SB();
  }
  return a;
}

constexpr unsigned long long diag_mask(int i) { return ((unsigned long long)(0xffffffffu << ((i & 3) + 8 * (i >> 2) + 4)) << 32) | (0xffffffffu << ((i & 3) + 8 * (i >> 2))); }
template <bool EDGE>
DI f32x16 attention(lptr L, unsigned v_target, bool late, lptr kp, lptr vp, int vs, lptr mp, v4u qf, int nv) {
  v4u kf[9];
  kf[8] = lds_r128(kp + 8 * 1024);
#pragma unroll
  for (int n = 0; n < 8; ++n) kf[n] = lds_r128(kp + n * 1024);
  SB();
  f32x16 s[9];
  float m0, m1, m2, m3;
#define SC(n) s[n] = MFMA(kf[n], qf, splat16(EDGE ? ((n) < nv ? -3.0e38f : 0.f) : 0.f))
#define MX(n) do { m0 = max3(m0, s[n][0], s[n][1]); m1 = max3(m1, s[n][2], s[n][3]); m2 = max3(m2, s[n][4], s[n][5]); m3 = max3(m3, s[n][6], s[n][7]); \
    m0 = max3(m0, s[n][8], s[n][9]); m1 = max3(m1, s[n][10], s[n][11]); m2 = max3(m2, s[n][12], s[n][13]); m3 = max3(m3, s[n][14], s[n][15]); } while (0)
#define MG(i) s[0][i] = __builtin_amdgcn_inverse_ballot_w64(diag_mask(i)) ? s[8][i] : s[0][i]
  s[8] = MFMA(kf[8], qf, splat16(0.f)); SC(0); SC(1); SB();
  SC(2); MG(0); MG(1); MG(2); MG(3); MG(4); MG(5); MG(6); MG(7); SB();
  SC(3); MG(8); MG(9); MG(10); MG(11); MG(12); MG(13); MG(14); MG(15); SB();
  SC(4); m0 = max3(s[1][0], s[1][1], s[1][2]); m1 = max3(s[1][3], s[1][4], s[1][5]); m2 = max3(s[1][6], s[1][7], s[1][8]); m3 = max3(s[1][9], s[1][10], s[1][11]);
  m0 = max3(m0, s[1][12], s[1][13]); m1 = max3(m1, s[1][14], s[1][15]); SB();
  SC(5); MX(0); SB();
  SC(6); MX(2); SB();
  SC(7); MX(3); SB();
  const unsigned cv = __hip_atomic_load((LAS unsigned*)(L + L_CTR + 4 * C_KV), RLX_WG);
  const v4u mlo = lds_r128(mp), mhi = lds_r128(mp + 1024);
  const v4u da = lds_r128(vp + 8 * vs), db = lds_r128(vp + 8 * vs + 512);
  MX(4); MX(5); PIN(m0); PIN(m1); PIN(m2); PIN(m3); SB();
  if (__builtin_amdgcn_readfirstlane(cv) < v_target) ctr_wait(L, C_KV, v_target);
  asm volatile("" ::: "memory");
  v4u va = lds_r128(vp), vb = lds_r128(vp + 512);
  MX(6); MX(7);
#undef SC
#undef MX
#undef MG
  constexpr float C = 0.25f * 1.4426950408889634f;
  const float nm = -C * half_max(__builtin_fmaxf(max3(m0, m1, m2), m3));
  f32x2 C2 = {C, C}, nm2 = {nm, nm};
  f32x16 o = splat16(0.f);
#pragma unroll
  for (int n = 0; n < 8; ++n) {
    v4u na, nb;
    if (n < 7) { vp += vs; na = lds_r128(vp); nb = lds_r128(vp + 512); }
    f32x2 e[8];
#pragma unroll
    for (int i = 0; i < 8; ++i) { const f32x2 v = {s[n][2 * i], s[n][2 * i + 1]}; e[i] = __builtin_elementwise_fma(v, C2, nm2); }
#pragma unroll
    for (int i = 0; i < 8; ++i) PIN(e[i]);
    v4u p0, p1;
#pragma unroll
    for (int i = 0; i < 4; ++i) p0[i] = pk2(__builtin_amdgcn_exp2f(e[i][0]), __builtin_amdgcn_exp2f(e[i][1]));
#pragma unroll
    for (int i = 0; i < 4; ++i) p1[i] = pk2(__builtin_amdgcn_exp2f(e[4 + i][0]), __builtin_amdgcn_exp2f(e[4 + i][1]));
    if (n == 0) {
      const v4u d0 = p0 & mlo, d1 = p1 & mhi;
      o = MFMA(da, d0, o); o = MFMA(va, p0 ^ d0, o); o = MFMA(db, d1, o); o = MFMA(vb, p1 ^ d1, o);
    } else { o = MFMA(va, p0, o); o = MFMA(vb, p1, o); }
    if (n < 7) { va = na; vb = nb; }
    SB();
    if (n == B3N && late) BAR();
  }
  return o;
}

__global__ void __launch_bounds__(512, 2) mega(const u16* __restrict__ x_in, const u16* __restrict__ Aw, const u16* __restrict__ Ao,
                                               const u16* __restrict__ Bw, const u16* __restrict__ Bo, u16* __restrict__ out, unsigned char* ws) {
  extern __shared__ __attribute__((aligned(16))) char smem[];
  const lptr L = (lptr)smem;
  const unsigned L0 = (unsigned)(uintptr_t)L;
  int b = blockIdx.x, lane = threadIdx.x & 63, w = __builtin_amdgcn_readfirstlane(threadIdx.x >> 6);
  const __amdgpu_buffer_rsrc_t rs = __builtin_amdgcn_make_buffer_rsrc(ws, 0, WS_END, 0x00020000);
  STAMP(63);
  unsigned zero = 0; PIN(zero);
  unsigned tag;
  v4u x0 = {0u, 0u, 0u, 0u}, x1 = x0;

  {
    const int g = MAP_G(b), j = MAP_J(b), t = w & 3, r = lane & 31, h = lane >> 5;
    unsigned epoch = __hip_atomic_load((gu32*)ws, RLX_AGENT);
    if (w < 4) {
      unsigned tlb = 0;
      if (w == 0) { const v4u z = {0u, 0u, 0u, 0u}; lds_w128(L + L_WX + lane * 16, z); }
      BAR();
      unsigned warm = 0;
      if (w == 1 && lane < 48) warm = *(const GAS unsigned*)((const char*)Aw + (b * 48 + lane) * 128);
      if (w == 2 && lane < 48) warm = *(const GAS unsigned*)((const char*)Bw + (b * 48 + lane) * 128);
      if (w == 3 && lane < 32) warm = *(const GAS unsigned*)((const char*)(lane < 16 ? Ao : Bo) + (b * 16 + (lane & 15)) * 128);
      if (w == 3 && lane >= 32 && lane < 45) {
        const int i = lane - 32;
        const int o = i < 4 ? WS_FX + i * 256 * FL : i < 8 ? WS_FO + (i - 4) * 256 * FL : i < 10 ? WS_FH + (i - 8) * 256 * FL : i < 12 ? WS_FP + (i - 10) * 256 * FL : WS_FR;
        (void)__hip_atomic_fetch_add((gu32*)(ws + o + (g * 16 + j) * FL), zero, RLX_AGENT);
      }
      if (w == 0) { unsigned long long pc; asm volatile("s_getpc_b64 %0" : "=s"(pc));
        const int o = ((b >> 3) * 11 + lane) * 128; if (lane < 11 && o < CODEPF) warm = *(const GAS unsigned*)((const char*)(pc & ~127ull) + o); }
      dma_rows<0>(Aw + 32 * j, 1536, 32 + 16 * t, L0 + 32768 + 16384 * t, 4, lane, ROT * (g & 3));
      dma_rows<0>(Aw + 32 * j, 1536, 8 * t, L0 + 8192 * t, 2, lane);
      ctr_arrive(L, C_PR, lane);
      blank(rs, lane * 16, WS_O + (g * 4 + t) * 32768 + 2 * j * 1024);
#pragma unroll
      for (int i = 0; i < 4; ++i) blank(rs, lane * 16 + i * 1024, WS_KV + ((g * 16 + j) * 4 + t) * 4096);
      asm volatile("s_waitcnt vmcnt(13)" :: "v"(warm), "v"(tlb) : "memory");
      STAMP(59);
      ctr_arrive(L, C_R, lane); ctr_wait(L, C_R, 5u);
    } else {
      __builtin_amdgcn_s_setprio(3);
      const u16* xrow = x_in + (size_t)(g * 128 + 8 * j + 2 * t + h) * 512 + 8 * r;
      const v4u xa = *(const GAS v4u*)xrow, xb = *(const GAS v4u*)(xrow + 256);
      BAR();
      { const unsigned fill = (lane & 16) ? 0x3f800000u : ONE2; const v4u o = {fill, fill, fill, fill}; if (w == 4) lds_w128(L + L_ONES + lane * 16, o); }
      if (h == 0) *(LAS unsigned*)(L + L_NM + t * 1024 + r * 4) = 0u;
      if (w >= 6) { v4u mk;
#pragma unroll
        for (int i = 0; i < 4; ++i) { const int key = ((2 * i) & 3) + 8 * (i >> 1) + 16 * (w - 6) + 4 * h; mk[i] = (key + 1 <= r) ? 0xffffffffu : ((key <= r) ? 0xffffu : 0u); }
        lds_w128(L + L_MSK + (w - 6) * 1024 + lane * 16, mk); }
      float s1 = 0.f, s2 = 0.f;
#pragma unroll
      for (int i = 0; i < 4; ++i) { s1 = dot2(xa[i], ONE2, s1); s2 = dot2(xa[i], xa[i], s2); s1 = dot2(xb[i], ONE2, s1); s2 = dot2(xb[i], xb[i], s2); }
      float m0, r0; row_stats(sum32(s1), sum32(s2), m0, r0);
      const v4u na = ln_frag(xa, m0, r0), nb = ln_frag(xb, m0, r0);
      { const lptr d = L + L_LN + (r >> 1) * 256 + (2 * t + h) * 16 + (r & 1) * 8;
        v2u u = {na[0], na[1]}; *(LAS v2u*)d = u; u[0] = na[2]; u[1] = na[3]; *(LAS v2u*)(d + 128) = u;
        u[0] = nb[0]; u[1] = nb[1]; *(LAS v2u*)(d + 4096) = u; u[0] = nb[2]; u[1] = nb[3]; *(LAS v2u*)(d + 4096 + 128) = u; }
      STAMP(56);
      ctr_arrive(L, C_ST, lane); ctr_wait(L, C_ST, 4u);
      const int vo = (lane >> 4) * 1024 + (((lane >> 3) & 1) * 32 + 8 * (j & 3) + (lane & 7)) * 16;
      const int xb0 = WS_X + (g * 4 + (j >> 2)) * 32768 + t * 8192;
      st_sc1(rs, lds_r128(L + L_LN + t * 2048 + lane * 16), vo, xb0);
      st_sc1(rs, lds_r128(L + L_LN + t * 2048 + 1024 + lane * 16), vo, xb0 + 4096);
      PIN(epoch);
      publish_wg(L, C_PX, 4u, lane, ws, WS_FX + (g * 16 + lane) * FL + j * 4, lane < 16, epoch + 1u);
      ctr_arrive(L, C_P, lane);
      STAMP(57);
      if (t == 3) { (void)poll(ws, WS_FX + (g * 16 + j) * FL, lane, 16, __builtin_amdgcn_readfirstlane(epoch) + 1u, zero); ctr_arrive(L, C_R, lane); STAMP(59); }
      __builtin_amdgcn_s_setprio(0);
      ctr_wait(L, C_PR, 4u);
      v4u rt[4];
#pragma unroll
      for (int i = 0; i < 4; ++i) {
        const bool po = (i >= 2) && (t & 1);
        const u16* src = po ? (t == 1 ? Ao : Bo) + (32 * g + 16 * (i & 1)) * 512
                            : (t < 2 ? Bw : Aw) + (32 * g + 16 * (i & 1)) * 1536 + (2 * (t & 1) + (i >> 1)) * 512;
        rt[i] = *(const GAS v4u*)(src + 32 * j + (lane >> 2) * (po ? 512 : 1536) + (lane & 3) * 8);
      }
      STAMP(58);
      ctr_wait(L, C_XI, 4u);
#pragma unroll
      for (int i = 0; i < 4; ++i) {
        const bool po = (i >= 2) && (t & 1);
        const int piece = (po ? (t == 1 ? T_OA : T_OB) : (t < 2 ? T_WB : T_WA) + 32 * (2 * (t & 1) + (i >> 1))) + 2 * g + (i & 1);
        st_sc1(rs, rt[i], lane * 16, WS_WT + (j * 256 + piece) * 1024);
      }
    }
    PIN(epoch);
    tag = __builtin_amdgcn_readfirstlane(epoch) + 1u;
  }

  for (int pr = 0; pr < 2; ++pr) {
#pragma unroll
    for (int isB = 0; isB < 2; ++isB) {
      const int l = 2 * pr + isB;
      asm volatile("" : "+v"(lane), "+s"(b), "+s"(w));
      const int g = MAP_G(b), j = MAP_J(b);
      const int t = w & 3, hh = w >> 2, r = lane & 31, h = lane >> 5;
      const bool isQ = w < 4;
      const int gt = g * 4 + t, gj = g * 16 + j;
      const int trl = ((lane & 15) >> 2) * 64 + ((lane >> 4) & 1) * 32 + (lane & 3) * 8 + h * 256;
      const int l16 = lane * 16;
      const unsigned char* wt = ws + WS_WT + j * (256 * 1024);
      v4u qf;
      int mo_ = WS_O + l * MB2 + gt * 32768 + (2 * j + hh) * 1024 + l16;
      lptr ma0 = L + L_PR + (r < 16 ? (hh * 16 + r + 32 * h) * 16 : 2048 + (hh * 16 + 4 * h) * 4), ma1 = ma0 + (r < 16 ? 1024 : 32);
      if (isQ) {
        const lptr pq = L + trl;
        lptr pv = pq + L_WV; PIN(pv);
        STAMP(l * 14 + 1);
        Probes pb_ = poll(ws, WS_FX + (l * 256 + gj) * FL, lane, l > 0 ? 16 : 0, tag, zero);
        STAMP(l * 14 + 2);
        const int xb = WS_X + l * MB2 + gt * 32768;
        f32x16 aK, aV; float rstd; f32x4 rs16[4]; v4u xf[32]; unsigned xe; v2u eq;
        unsigned tries = 0;
        v4u k0, k1, vf0, vf1; f32x16 d; float z;
        do {
          if (!isB) gemm_kv<false>(xf, L, pq, pv, aK, aV, rs, lane, xb, xe, eq, rstd, rs16, 0u, 4u * l + 4u, t, tries == 0, pb_,
            [&](auto SI) __attribute__((always_inline)) { constexpr int i = decltype(SI)::value;
              if constexpr (i == B1S) { if (tries == 0) { asm volatile("s_waitcnt vmcnt(0)\n\ts_barrier" ::: "memory"); } }
              if constexpr (i >= 2 && i < 6) { constexpr int c = 4 * (i - 2); unsigned u = pk2(aK[c] * rstd, aK[c + 1] * rstd), v = pk2(aK[c + 2] * rstd, aK[c + 3] * rstd); PIN(u); PIN(v);
                if constexpr (i < 4) { k0[2 * i - 4] = u; k0[2 * i - 3] = v; } else { k1[2 * i - 8] = u; k1[2 * i - 7] = v; } } });
          else {
            gemm_kv<true>(xf, L, pq, pv, aK, aV, rs, lane, xb, xe, eq, rstd, rs16, 3u, 4u * l + 4u, t, tries == 0, pb_,
              [&](auto SI) __attribute__((always_inline)) { constexpr int i = decltype(SI)::value;
                if constexpr (i >= 2 && i < 6) { constexpr int c = 2 * (i - 2); unsigned u = phi2(aK[c] * rs16[c >> 2][c & 3], aK[c + 1] * rs16[c >> 2][(c & 3) + 1]); PIN(u); k0[i - 2] = u; } });
            scale_feat(aV, rs16);
            vf0 = pack8(aV, 0);
#pragma unroll
            for (int i = 8; i < 16; ++i) aK[i] *= rs16[i >> 2][i & 3];
            d = MFMA(k0, vf0, splat16(0.f));
            k1 = phi_pack8(aK, 8); vf1 = pack8(aV, 8);
            d = MFMA(k1, vf1, d);
            float z0 = 0.f, z1 = 0.f;
#pragma unroll
            for (int i = 0; i < 4; ++i) { z0 = dot2(k0[i], ONE2, z0); z1 = dot2(k1[i], ONE2, z1); }
            z = half_sum(z0 + z1); PIN(z);
          }
          pb_ = Probes{0u, 0u, 0u};
        } while (again(isnan_(isB ? d[0] : aK[0]), tries));
        RT(0);
        STAMP(l * 14 + 12);
        if (!isB) {
          DRAIN();
          if (B1S > 7) BAR();
          const int kb = WS_KV + ((pr * 256 + gj) * 4 + t) * 4096;
          if (g < 15) { st_sc1(rs, k0, l16, kb); st_sc1(rs, k1, l16 + 1024, kb); }
          scale_feat(aV, rs16);
          vf0 = pack8(aV, 0); vf1 = pack8(aV, 8);
          if (g < 15) { st_sc1(rs, vf0, l16 + 2048, kb); st_sc1(rs, vf1, l16 + 3072, kb); }
          STAMP(l * 14 + 4);
          lds_w128(L + L_K + (8 + t) * 1024 + l16, k0); lds_w128(L + L_K + (20 + t) * 1024 + l16, k1);
          const lptr vd = L + L_V + ((r >> 4) * 12 + 8 + t) * 1024 + h * 256 + (r & 15) * 16;
          lds_w128(vd, vf0); lds_w128(vd + 512, vf1);
          f32x16 aQ = gemm_q(xf, pq, xe, eq);
          scale_tok(aQ, rstd);
          qf = pack8(aQ, 0); lds_w128(L + L_QF + t * 1024 + l16, pack8(aQ, 8));
          STAMP(l * 14 + 5);
          if (pr == 0 && t < 3) {
            const float c = colsum(pq + t * 32768);
            if (h == 0) *(LAS float*)(L + L_WX + (t * 32 + r) * 4) = c;
          }
        } else {
          const lptr pw = L + L_PW + t * PSTR;
          if (h == 0) *(LAS float*)(pw + 2048 + r * 4) = z;
          if (r < 16) { *(LAS f32x4*)(pw + l16) = f32x4{d[0], d[1], d[2], d[3]}; *(LAS f32x4*)(pw + 1024 + l16) = f32x4{d[4], d[5], d[6], d[7]}; asm volatile("" ::: "memory"); }
          else { *(LAS f32x4*)(pw + l16) = f32x4{d[8], d[9], d[10], d[11]}; *(LAS f32x4*)(pw + 1024 + l16) = f32x4{d[12], d[13], d[14], d[15]}; }
          BAR();
          STAMP(l * 14 + 4);
          f32x16 aQ = gemm_q(xf, pq, xe, eq);
          if (pr == 0) ctr_arrive(L, C_QD, lane);
          scale_tok(aQ, rstd);
          qf = phi_pack8(aQ, 0); lds_w128(L + L_QF + t * 1024 + l16, phi_pack8(aQ, 8));
          STAMP(l * 14 + 5);
          DMA_W(0, T_OB, Bo, 512, 8 * t, L0 + L_WO + 8192 * t, 2);
          if (pr == 0) {
            ctr_wait(L, C_QD, 4u);
            DMA_W(PACE2, T_WA, Aw, 1536, 24 * t, L0 + 24576 * t, NB1);
          }
          PIN(ma0); PIN(ma1);
        }
        PIN(mo_);
        BAR();
      } else {
        __builtin_amdgcn_s_setprio(OPRIO);
        if (l > 0) {
          const Probes pb_ = poll(ws, WS_FX + (l * 256 + gj) * FL, lane, 16, tag, zero);
          v4u st[4];
          unsigned tries = 0, m;
          do {
#pragma unroll
            for (int i = 0; i < 4; ++i) st[i] = ld_sc1(rs, l16 + i * 1024, WS_XS + (l * 64 + gt) * 4096);
            m = chk4(chk4(chk4(chk4(0u, st[0]), st[1]), st[2]), st[3]);
          } while (again(m, tries));
          retire(pb_);
          RT(1);
          f32x4 a;
#pragma unroll
          for (int i = 0; i < 4; ++i) a[i] = add1(add1(__uint_as_float(st[0][i]), __uint_as_float(st[1][i])), add1(__uint_as_float(st[2][i]), __uint_as_float(st[3][i])));
          float m0, r0, m1, r1;
          row_stats(half_sum(x16_sum(a[0])), half_sum(x16_sum(a[1])), m0, r0);
          row_stats(half_sum(x16_sum(a[2])), half_sum(x16_sum(a[3])), m1, r1);
          if (lane < 16) { *(LAS v2u*)(L + L_NM + t * 1024 + lane * 8) = v2u{pk2(-m0, -m0), pk2(-m1, -m1)};
            *(LAS f32x2*)(L + L_RS + (t & 1) * 128 + (t >> 1) * 512 + lane * 8) = f32x2{r0, r1}; }
          ctr_arrive(L, C_ST, lane);
        }
        STAMP(l * 14 + 13);
        ctr_wait(L, C_XI, 4u * l + 4u);
        blank(rs, l16, WS_O + l * MB2 + gt * 32768 + (2 * j + 1) * 1024);
        if (l < 3) {
          blank(rs, l16, WS_X + (l + 1) * MB2 + gt * 32768 + j * 2048); blank(rs, l16 + 1024, WS_X + (l + 1) * MB2 + gt * 32768 + j * 2048);
          if (lane < 16) blank(rs, l16, WS_XS + (((l + 1) * 64 + gt) * 16 + j) * 256);
        }
        if (!isB && t == 0) { const int pb = WS_P + ((pr * 16 + j) * 16 + g) * PSTR; blank(rs, l16, pb); blank(rs, l16 + 1024, pb); if (lane < 8) blank(rs, l16 + 2048, pb); }
        STAMP(l * 14 + 1);
        BAR();
        STAMP(l * 14 + 4);
        if (!isB) {
          if (t == 0) announce(ws, WS_FH + (pr * 256 + gj + 16 * (lane + 1)) * FL + lane * 4, lane < 2 && g + lane < 15, tag);
          v4u kv[8];
          const Probes pb_ = poll(ws, WS_FH + (pr * 256 + gj) * FL, lane, g < 2 ? g : 2, tag, zero);
          STAMP(l * 14 + 2);
          const v4u z4 = {0u, 0u, 0u, 0u};
          const int kb0 = WS_KV + ((pr * 256 + gj - 16) * 4 + t) * 4096, kb1 = kb0 - 16 * 4 * 4096;
          unsigned tries = 0;
#define LDH(i0) do { if (g > 0) { kv[i0] = ld_sc1(rs, l16 + (i0) * 1024, kb0); kv[i0 + 1] = ld_sc1(rs, l16 + (i0) * 1024 + 1024, kb0); } else { kv[i0] = z4; kv[i0 + 1] = z4; } \
                     if (g > 1) { kv[4 + i0] = ld_sc1(rs, l16 + (i0) * 1024, kb1); kv[5 + i0] = ld_sc1(rs, l16 + (i0) * 1024 + 1024, kb1); } else { kv[4 + i0] = z4; kv[5 + i0] = z4; } } while (0)
#define BADH(i0) chk4(chk4(chk4(chk4(0u, kv[i0]), kv[i0 + 1]), kv[4 + i0]), kv[5 + i0])
          LDH(0); LDH(2);
          SB();
          while (again(BADH(0), tries)) LDH(0);
          retire(pb_);
          lds_w128(L + L_K + (4 + t) * 1024 + l16, kv[0]); lds_w128(L + L_K + (16 + t) * 1024 + l16, kv[1]);
          lds_w128(L + L_K + t * 1024 + l16, kv[4]); lds_w128(L + L_K + (12 + t) * 1024 + l16, kv[5]);
          STAMP(l * 14 + 5);
          BAR();
          while (again(BADH(2), tries)) LDH(2);
          { const lptr vd = L + L_V + ((r >> 4) * 12 + t) * 1024 + h * 256 + (r & 15) * 16;
            lds_w128(vd + 4096, kv[2]); lds_w128(vd + 4096 + 512, kv[3]); lds_w128(vd, kv[6]); lds_w128(vd + 512, kv[7]); }
          ctr_arrive(L, C_KV, lane);
          if (pr == 0) publish_wg(L, C_LNQ, 4u, lane, ws, WS_FR + (lane * 16 + j) * FL + g * 4, lane < 16, tag); else DRAIN();
          RT(2);
#undef LDH
#undef BADH
        } else {
          { const int pb = WS_P + ((pr * 16 + j) * 16 + g) * PSTR;
            if (t < 3) {
              const lptr pp = L + L_PW + t * 1024 + l16;
              f32x4 u[4];
#pragma unroll
              for (int q = 0; q < 4; ++q) u[q] = lds_r128f(pp + q * PSTR);
              SB();
              f32x4 sm;
#pragma unroll
              for (int i = 0; i < 4; ++i) sm[i] = add1(add1(u[0][i], u[1][i]), add1(u[2][i], u[3][i]));
              if (t < 2 || lane < 8) st_sc1(rs, __builtin_bit_cast(v4u, sm), l16 + t * 1024, pb);
            } else announce(ws, WS_FP + (pr * 256 + lane * 16 + j) * FL + g * 4, lane < 16, tag);
            STAMP(l * 14 + 3); }
          const Probes pb_ = poll(ws, WS_FP + (pr * 256 + gj) * FL + 16 * t, lane, 4, tag, zero);
          STAMP(l * 14 + 2);
          unsigned tries = 0;
          const lptr pw = L + L_PR + t * PSTR;
          const int pb = WS_P + ((pr * 16 + j) * 16 + 4 * t) * PSTR;
          float chk;
          do {
            v4u a[8];
#pragma unroll
            for (int q = 0; q < 4; ++q) { a[2 * q] = ld_sc1(rs, l16, pb + q * PSTR); a[2 * q + 1] = ld_sc1(rs, l16 + 1024, pb + q * PSTR); }
            const v4u zz = ld_sc1(rs, ((lane >> 3) & 3) * PSTR + 2048 + (lane & 7) * 16, pb);
            SB();
            f32x4 u0, u1, z;
#pragma unroll
            for (int i = 0; i < 4; ++i) {
#define F(x) __uint_as_float(x)
              u0[i] = add1(add1(F(a[0][i]), F(a[2][i])), add1(F(a[4][i]), F(a[6][i]))); u1[i] = add1(add1(F(a[1][i]), F(a[3][i])), add1(F(a[5][i]), F(a[7][i])));
              z[i] = x16_sum(dpp_add<0x128>(F(zz[i])));
#undef F
            }
            *(LAS f32x4*)(pw + l16) = u0; *(LAS f32x4*)(pw + 1024 + l16) = u1;
            if (lane < 8) *(LAS f32x4*)(pw + 2048 + l16) = z;
            chk = add1(add1(add1(u0[0], u0[1]), add1(u0[2], u0[3])), add1(add1(u1[0], u1[1]), add1(u1[2], u1[3])));
            chk = add1(chk, add1(add1(z[0], z[1]), add1(z[2], z[3])));
          } while (again(isnan_(chk), tries));
          retire(pb_);
          RT(3);
          DRAIN();
          PIN(ma0); PIN(ma1); PIN(mo_);
          STAMP(l * 14 + 5);
          BAR();
        }
        qf = lds_r128(L + L_QF + t * 1024 + l16);
        if (!isB) __builtin_amdgcn_s_setprio(APRIO);
      }

      STAMP(l * 14 + 6);
      f32x16 o;
      if (!isB) {
        const lptr kp = L + L_K + (hh * 12 + t) * 1024 + l16;
        const lptr vp = (r < 16 ? L + L_V + (hh * 12 + t) * 1024 + h * 256 : L + L_ONES) + (r & 15) * 16;
        const int vs = r < 16 ? 1024 : 0;
        if (g >= 2) o = attention<false>(L, 4u * pr + 4u, !isQ, kp, vp, vs, L + L_MSK + l16, qf, 0);
        else o = attention<true>(L, 4u * pr + 4u, !isQ, kp, vp, vs, L + L_MSK + l16, qf, 8 - 4 * g - t);
      } else {
        if (OBD >= 0 && w == 0) announce(ws, WS_FO + (l * 256 + g * 16 + lane) * FL + j * 4, lane < 16, tag);
        f32x4 p0[4], p1[4];
#pragma unroll
        for (int q = 0; q < 4; ++q) { p0[q] = lds_r128f(ma0 + q * PSTR); p1[q] = lds_r128f(ma1 + q * PSTR); }
        SB();
        const f32x4 u0 = (p0[0] + p0[1]) + (p0[2] + p0[3]), u1 = (p1[0] + p1[1]) + (p1[2] + p1[3]);
        const v4u af = {pk2(u0[0], u0[1]), pk2(u0[2], u0[3]), pk2(u1[0], u1[1]), pk2(u1[2], u1[3])};
        o = MFMA(af, qf, splat16(0.f));
        o[8] += 1e-6f;
      }
      { const float ri = __builtin_amdgcn_rcpf(o[8]);
#pragma unroll
        for (int i = 0; i < 8; ++i) o[i] *= ri;
        asm volatile("" :: "v"(o[0]));
        STAMP(l * 14 + 7);
        DRAIN();
        if (OBD < 0 && isB && w == 0) announce(ws, WS_FO + (l * 256 + g * 16 + lane) * FL + j * 4, lane < 16, tag);
        st_sc1(rs, pack8(o, 0), mo_, 0);
      }

      if (isQ) {
        if (isB) ctr_arrive(L, C_WO, lane);
        if (!isB) {
          if (pr == 0) (void)poll(ws, WS_FR + gj * FL, lane, 16, tag, zero);
          DMA_W(0, T_WB, Bw, 1536, 24 * t, L0 + 24576 * t, NA1);
        }
        STAMP(l * 14 + 8);
        if (!EARLY3 || !isB) {
          BAR();
          if (t == 0) announce(ws, WS_FO + (l * 256 + g * 16 + lane) * FL + j * 4, lane < 16, tag); else nap<1>();
        }
        if (!isB) {
          DMA_W(0, T_OA, Ao, 512, 8 * t, L0 + L_WO + 8192 * t, 2);
          DRAIN(); ctr_arrive(L, C_WO, lane);
          DMA_W(PACE1, T_WB, Bw, 1536, 24 * t + 4 * NA1, L0 + 24576 * t + 4096 * NA1, 6 - NA1);
        } else if (pr == 0) { nap<3>(); DMA_W(PACE2, T_WA, Aw, 1536, 24 * t + 4 * NB1, L0 + 24576 * t + 4096 * NB1, 6 - NB1); }
        STAMP(l * 14 + 9);
        if (l < 3) {
          DRAIN(); ctr_arrive(L, C_W, lane); ctr_wait(L, C_W, 4u * l + 4u);
          if (l == 0 && t < 3) {
            const float c = colsum(L + trl + t * 32768);
            if (h == 0) *(LAS float*)(L + L_WX + 384 + (t * 32 + r) * 4) = c;
          }
          ctr_wait(L, C_OP, 4u * l + 4u);
          blank(rs, l16, WS_O + (l + 1) * MB2 + gt * 32768 + 2 * j * 1024);
          if (l == 1) {
#pragma unroll
            for (int i = 0; i < 4; ++i) blank(rs, l16 + i * 1024, WS_KV + ((256 + gj) * 4 + t) * 4096);
          }
          STAMP(l * 14 + 11);
          BAR();
          if (t == 0) announce(ws, WS_FX + ((l + 1) * 256 + g * 16 + lane) * FL + j * 4, lane < 16, tag);
        }
      } else {
        __builtin_amdgcn_s_setprio(OPRIO);
        STAMP(l * 14 + 8);
        if (!EARLY3) BAR();
        if (l == 0) {
          const u16* xr = x_in + (size_t)(gt * 32 + r) * 512 + 32 * j + 4 * h;
          const v2u a = *(const GAS v2u*)xr, c = *(const GAS v2u*)(xr + 8), d = *(const GAS v2u*)(xr + 16), e = *(const GAS v2u*)(xr + 24);
          x0 = v4u{a[0], a[1], c[0], c[1]}; x1 = v4u{d[0], d[1], e[0], e[1]};
        }
        const Probes pb_ = poll(ws, WS_FO + (l * 256 + gj) * FL, lane, 16, tag, zero);
        STAMP(l * 14 + 9);
        lptr po = L + L_WO + trl; PIN(po);
        f32x16 y; unsigned tries = 0;
        const int xo_ = WS_X + (l + 1) * MB2 + gt * 32768 + j * 2048 + l16;
        do y = gemm_out(L, po, rs, l16, WS_O + l * MB2 + gt * 32768, 4u * l + 4u, tries == 0, tries == 0 && l < 3); while (again(isnan_(y[0]), tries));
        retire(pb_);
        RT(4);
        asm volatile("" :: "v"(y[0]));
        STAMP(l * 14 + 10);
        if (B4S > 31 && l < 3) BAR();
        { v4u y0 = pack8(y, 0), y1 = pack8(y, 8);
          PIN(y0); PIN(y1);
          f32x16 a;
#pragma unroll
          for (int i = 0; i < 4; ++i) { a[2 * i] = add1(bflo(x0[i]), bflo(y0[i])); a[2 * i + 1] = add1(bfhi(x0[i]), bfhi(y0[i])); }
          x0 = pack8(a, 0);
          if (l < 3) st_sc1(rs, x0, xo_, 0);
#pragma unroll
          for (int i = 0; i < 4; ++i) { a[8 + 2 * i] = add1(bflo(x1[i]), bflo(y1[i])); a[9 + 2 * i] = add1(bfhi(x1[i]), bfhi(y1[i])); }
          x1 = pack8(a, 8); }
        if (l < 3) {
          st_sc1(rs, x1, xo_ + 1024, 0);
          SB();
          float s1 = 0.f, s2 = 0.f, s3 = 0.f, s4 = 0.f;
#pragma unroll
          for (int i = 0; i < 4; ++i) { s1 = dot2(x0[i], ONE2, s1); s2 = dot2(x0[i], x0[i], s2); s3 = dot2(x1[i], ONE2, s3); s4 = dot2(x1[i], x1[i], s4); }
          s1 = half_sum(s1 + s3); s2 = half_sum(s2 + s4);
          if (h == 0) { v2u v = {__float_as_uint(s1), __float_as_uint(s2)}; __builtin_amdgcn_raw_buffer_store_b64(v, rs, r * 8, WS_XS + (((l + 1) * 64 + gt) * 16 + j) * 256, SC1); }
          STAMP(l * 14 + 11);
        } else {
          const auto p = __builtin_amdgcn_permlane32_swap(x0[0], x0[2], false, false), q = __builtin_amdgcn_permlane32_swap(x0[1], x0[3], false, false);
          const auto u = __builtin_amdgcn_permlane32_swap(x1[0], x1[2], false, false), v = __builtin_amdgcn_permlane32_swap(x1[1], x1[3], false, false);
          u16* op = out + (size_t)(gt * 32 + r) * 512 + 32 * j + 8 * h;
          *(GAS v4u*)op = v4u{p[0], q[0], p[1], q[1]}; *(GAS v4u*)(op + 16) = v4u{u[0], v[0], u[1], v[1]};
          if (b == 0 && w == 4 && lane == 0) __hip_atomic_store((gu32*)ws, tag, RLX_AGENT);
        }
      }
    }
  }
}

extern "C" void kernel_launch(void* const* d_in, const int* in_sizes, int n_in, void* d_out, int out_size, void* d_ws, size_t ws_size,
                              hipStream_t stream) {
  (void)in_sizes; (void)n_in; (void)out_size; (void)ws_size;
  (void)hipFuncSetAttribute((const void*)mega, hipFuncAttributeMaxDynamicSharedMemorySize, LDS_BYTES);
  mega<<<dim3(256), dim3(512), LDS_BYTES, stream>>>((const u16*)d_in[0], (const u16*)d_in[1], (const u16*)d_in[2], (const u16*)d_in[4],
                                                    (const u16*)d_in[5], (u16*)d_out, (unsigned char*)d_ws);
}
```

```cpp
#include <hip/hip_runtime.h>
#include <hip/hip_bf16.h>
#include <stdint.h>

#define DI __device__ __forceinline__
#define LAS __attribute__((address_space(3)))
#define GAS __attribute__((address_space(1)))
typedef unsigned short u16;
typedef unsigned v4u __attribute__((ext_vector_type(4)));
typedef unsigned v2u __attribute__((ext_vector_type(2)));
typedef float f32x16 __attribute__((ext_vector_type(16)));
typedef float f32x4 __attribute__((ext_vector_type(4)));
typedef float f32x2 __attribute__((ext_vector_type(2)));
typedef short bf16x8 __attribute__((ext_vector_type(8)));
typedef short s16x4 __attribute__((ext_vector_type(4)));
typedef __bf16 bf2 __attribute__((ext_vector_type(2)));
typedef GAS unsigned gu32;
typedef LAS char* lptr;
#define RLX_AGENT __ATOMIC_RELAXED, __HIP_MEMORY_SCOPE_AGENT
#define RLX_WG __ATOMIC_RELAXED, __HIP_MEMORY_SCOPE_WORKGROUP
#define MFMA(a, b, c) __builtin_amdgcn_mfma_f32_32x32x16_bf16(__builtin_bit_cast(bf16x8, (a)), __builtin_bit_cast(bf16x8, (b)), (c), 0, 0, 0)
#define SB() __builtin_amdgcn_sched_barrier(0)
#define PIN(x) asm volatile("" : "+v"(x))
#define DRAIN() asm volatile("s_waitcnt vmcnt(0)" ::: "memory")
#define LGKM0() asm volatile("s_waitcnt lgkmcnt(0)" ::: "memory")
#define BAR() do { SB(); asm volatile("s_waitcnt lgkmcnt(0)\n\ts_barrier" ::: "memory"); SB(); } while (0)
constexpr int SC1 = 16;
constexpr unsigned ONE2 = 0x3f803f80u;
#define STAMP(i) do {} while (0)
#define RT(k) do {} while (0)

#define PSL 4
#define PS0 0
#define XMAP 1
#define MAP_G(b) (4 * (((b) & 7) >> 1) + ((b) >> 6))
#define MAP_J(b) (8 * ((b) & 1) + (((b) >> 3) & 7))
constexpr int FL = 256;
constexpr int MB2 = 2 << 20;
constexpr int WS_FX = 4096;
constexpr int WS_FO = WS_FX + 4 * 256 * FL;
constexpr int WS_FH = WS_FO + 4 * 256 * FL;
constexpr int WS_FP = WS_FH + 2 * 256 * FL;
constexpr int WS_FR = WS_FP + 2 * 256 * FL;
constexpr int WS_XS = WS_FR + 256 * FL;
constexpr int PSTR = 2304;
constexpr int WS_P = WS_XS + 4 * 1024 * 256;
constexpr int WS_X = 6 << 20;
constexpr int WS_O = WS_X + 4 * MB2;
constexpr int WS_KV = WS_O + 4 * MB2;
constexpr int WS_WT = WS_KV + 4 * MB2;
constexpr int WS_END = WS_WT + 2 * MB2;
constexpr int T_WB = 0, T_WA = 96, T_OA = 192, T_OB = 224;
static_assert(WS_P + 2 * 256 * PSTR <= WS_X, "ws map");

constexpr int L_WQ = 0, L_WK = 32768, L_WV = 65536, L_WO = 98304, L_K = 131072, L_V = L_WO + 8192, L_MISC = 155648;
constexpr int L_QF = L_MISC, L_MSK = L_MISC + 4096, L_ONES = L_MISC + 6144;
constexpr int L_RS = L_ONES + 256;
constexpr int L_WX = L_MISC + 7168, L_MU = L_WX + 768, L_CTR = L_MU + 192;
constexpr int L_NM = L_QF;
constexpr int L_LN = L_K;
constexpr int L_PW = L_K, L_PR = L_K + 4 * PSTR;
constexpr int LDS_BYTES = 163840;
enum { C_W = 0, C_CS = 1, C_CA = 2, C_KV = 3, C_OP = 4, C_P = 5, C_PX = 6, C_PO = 7, C_PH = 8, C_PR = 9, C_R = 10, C_WO = 11, C_XI = 12, C_ST = 13, C_QD = 14, C_LNQ = 15 };

template <int I> struct IC { static constexpr int value = I; };
template <int N, int I = 0, class F> DI void static_for(F&& f) { if constexpr (I < N) { f(IC<I>{}); static_for<N, I + 1>(f); } }
DI unsigned pk2(float lo, float hi) { f32x2 v = {lo, hi}; return __builtin_bit_cast(unsigned, __builtin_convertvector(v, bf2)); }
DI float bflo(unsigned u) { return __uint_as_float(u << 16); }
DI float bfhi(unsigned u) { return __uint_as_float(u & 0xffff0000u); }
DI float rbf(float x) { return bflo(pk2(x, 0.f)); }
DI float dot2(unsigned a, unsigned b, float c) { return __builtin_amdgcn_fdot2_f32_bf16(__builtin_bit_cast(bf2, a), __builtin_bit_cast(bf2, b), c, false); }
DI float half_sum(float v) { auto r = __builtin_amdgcn_permlane32_swap(__float_as_uint(v), __float_as_uint(v), false, false); return __uint_as_float(r[0]) + __uint_as_float(r[1]); }
DI float half_max(float v) { auto r = __builtin_amdgcn_permlane32_swap(__float_as_uint(v), __float_as_uint(v), false, false); return __builtin_fmaxf(__uint_as_float(r[0]), __uint_as_float(r[1])); }
template <int C> DI float dpp_add(float v) { return v + __uint_as_float(__builtin_amdgcn_update_dpp(0u, __float_as_uint(v), C, 0xf, 0xf, false)); }
DI float sum32(float v) {
  v = dpp_add<0x128>(v); v = dpp_add<0x124>(v); v = dpp_add<0x122>(v); v = dpp_add<0x121>(v);
  auto r = __builtin_amdgcn_permlane16_swap(__float_as_uint(v), __float_as_uint(v), false, false);
  return __uint_as_float(r[0]) + __uint_as_float(r[1]);
}
DI float max3(float a, float b, float c) { return __builtin_fmaxf(__builtin_fmaxf(a, b), c); }
DI v4u pack8(const f32x16& a, int o) { v4u r = {pk2(a[o], a[o + 1]), pk2(a[o + 2], a[o + 3]), pk2(a[o + 4], a[o + 5]), pk2(a[o + 6], a[o + 7])}; return r; }
DI v4u lds_r128(lptr p) { return *(const LAS v4u*)p; }
DI f32x4 lds_r128f(lptr p) { return *(const LAS f32x4*)p; }
DI void lds_w128(lptr p, v4u v) { *(LAS v4u*)p = v; }
DI f32x16 splat16(float x) { f32x16 z;
#pragma unroll
  for (int i = 0; i < 16; ++i) z[i] = x;
  return z; }
DI v4u wfrag(lptr p, int off) {
  const s16x4 a = __builtin_amdgcn_ds_read_tr16_b64_v4i16((LAS s16x4*)(p + off));
  const s16x4 b = __builtin_amdgcn_ds_read_tr16_b64_v4i16((LAS s16x4*)(p + off + 512));
  const v2u x = __builtin_bit_cast(v2u, a), y = __builtin_bit_cast(v2u, b);
  v4u r = {x[0], x[1], y[0], y[1]};
  return r;
}
DI void glds16(const void* base, unsigned voff, unsigned lds_dst) {
  unsigned keep;
  asm volatile("s_mov_b32 %0, m0\n\ts_mov_b32 m0, %3\n\ts_nop 0\n\tglobal_load_lds_dwordx4 %1, %2\n\ts_mov_b32 m0, %0" : "=&s"(keep) : "v"(voff), "s"(base), "s"(lds_dst) : "memory");
}
DI void dma_qkv(const u16* Wj, unsigned voff, unsigned L0, int n) { glds16(Wj + (size_t)(n & 31) * (16 * 1536) + (n >> 5) * 512, voff, L0 + n * 1024); }
template <int PACE> DI void dma_tiled(const unsigned char* wt, int src, unsigned dst, int n4, int l16) {
#pragma unroll 1
  for (int n = 0; n < n4; ++n) {
    unsigned keep;
    asm volatile("s_mov_b32 %0, m0\n\ts_mov_b32 m0, %3\n\ts_nop 0\n\tglobal_load_lds_dwordx4 %1, %2 sc1\n\tglobal_load_lds_dwordx4 %1, %2 offset:1024 sc1\n\t"
                 "global_load_lds_dwordx4 %1, %2 offset:2048 sc1\n\tglobal_load_lds_dwordx4 %1, %2 offset:3072 sc1\n\ts_mov_b32 m0, %0"
                 : "=&s"(keep) : "v"(l16), "s"(wt + src * 1024 + n * 4096), "s"(dst + n * 4096) : "memory");
    if (PACE) __builtin_amdgcn_s_sleep(PACE);
  }
}
template <int PACE> DI void dma_rows(const u16* Wj, int ld, int p0, unsigned dst, int n4, int lane, int rot = 0) {
  const unsigned v0 = ((lane >> 2) * ld + (lane & 3) * 8) * 2, st = 32 * ld - 1024;
  const unsigned v1 = v0 + st, v2 = v1 + st, v3 = v2 + st;
#pragma unroll 1
  for (int i = 0; i < n4; ++i) {
    const int n = (i + rot) & (n4 - 1 | -(rot == 0));
    const int p = p0 + 4 * n;
    unsigned keep;
    asm volatile("s_mov_b32 %0, m0\n\ts_mov_b32 m0, %6\n\ts_nop 0\n\tglobal_load_lds_dwordx4 %1, %5\n\tglobal_load_lds_dwordx4 %2, %5 offset:1024\n\t"
                 "global_load_lds_dwordx4 %3, %5 offset:2048\n\tglobal_load_lds_dwordx4 %4, %5 offset:3072\n\ts_mov_b32 m0, %0"
                 : "=&s"(keep) : "v"(v0), "v"(v1), "v"(v2), "v"(v3), "s"(Wj + (size_t)(p & 31) * 16 * ld + (p >> 5) * 512), "s"(dst + n * 4096) : "memory");
    if (PACE) __builtin_amdgcn_s_sleep(PACE);
  }
}
#define TLBPF 0
#define X0PF 1
#define OUTPF 0
#define OUTSC 0
#define FLAGPF 1
#define CODEPF 40700
#define PWAIT C_P
#define ROT 1
#define RETILE 1
#define DMA_W(PACE, T, W, ld, p0, dst, n4) dma_tiled<PACE>(wt, (T) + (p0), dst, n4, l16)
template <int N> DI void nap() { __builtin_amdgcn_s_sleep(N); }
DI void ctr_arrive(lptr L, int c, int lane) {
  unsigned a, one;
  asm volatile("v_mov_b32 %0, %2\n\tv_mov_b32 %1, 1\n\ts_mov_b64 exec, 1\n\tds_add_u32 %0, %1\n\ts_mov_b64 exec, -1"
               : "=&v"(a), "=&v"(one) : "s"((unsigned)(uintptr_t)L + L_CTR + 4 * c) : "memory");
}
DI void ctr_wait(lptr L, int c, unsigned target) {
  unsigned spins = 0;
  while (__builtin_amdgcn_readfirstlane(__hip_atomic_load((LAS unsigned*)(L + L_CTR + 4 * c), RLX_WG)) < target && ++spins < (1u << 22)) __builtin_amdgcn_s_sleep(1);
  asm volatile("" ::: "memory");
}
struct Probes { unsigned a, b, c; };
DI void retire(const Probes& q) { asm volatile("" :: "v"(q.a), "v"(q.b), "v"(q.c)); }
DI Probes poll(unsigned char* ws, int line_off, int lane, int n, unsigned tag, unsigned zero) {
  unsigned v0 = zero, v1 = zero, v2 = zero;
  if (lane < n) {
    gu32* p = (gu32*)(ws + line_off) + lane;
#define PROBE() __hip_atomic_fetch_add(p, zero, RLX_AGENT)
#define SEEN(v) (__builtin_amdgcn_ballot_w64((v) != tag) == 0ull)
    __builtin_amdgcn_s_sleep(PS0);
    v0 = PROBE();
    __builtin_amdgcn_s_sleep(PSL);
    v1 = PROBE();
    __builtin_amdgcn_s_sleep(PSL);
    v2 = PROBE();
    if (!SEEN(v0)) { v0 = PROBE();
      if (!SEEN(v1)) { v1 = PROBE();
        if (!SEEN(v2)) { v2 = PROBE();
          if (!SEEN(v0)) { v0 = PROBE();
            if (!SEEN(v1)) { v1 = PROBE();
              if (!SEEN(v2)) { v2 = PROBE();
#pragma unroll 4
                for (unsigned spins = 0; spins < (1u << 13); ++spins) {
                  if (SEEN(v0)) break;
                  v0 = PROBE(); __builtin_amdgcn_s_sleep(PSL);
                  if (SEEN(v1)) break;
                  v1 = PROBE(); __builtin_amdgcn_s_sleep(PSL);
                  if (SEEN(v2)) break;
                  v2 = PROBE(); __builtin_amdgcn_s_sleep(PSL);
                } } } } } } }
#undef PROBE
#undef SEEN
  }
  __builtin_amdgcn_fence(__ATOMIC_ACQUIRE, "wavefront");
  asm volatile("" ::: "memory");
  return Probes{v0, v1, v2};
}
DI void publish(unsigned char* ws, int word_off, bool active, unsigned tag) {
  DRAIN();
  if (active) (void)__hip_atomic_exchange((gu32*)(ws + word_off), tag, RLX_AGENT);
}
DI void publish_wg(lptr L, int c, unsigned target, int lane, unsigned char* ws, int word_off, bool active, unsigned tag) {
  DRAIN();
  unsigned old = 0;
  if (lane == 0) old = __hip_atomic_fetch_add((LAS unsigned*)(L + L_CTR + 4 * c), 1u, RLX_WG);
  if (__builtin_amdgcn_readfirstlane(old) + 1u == target && active) (void)__hip_atomic_exchange((gu32*)(ws + word_off), tag, RLX_AGENT);
}
DI v4u ld_sc1(__amdgpu_buffer_rsrc_t rs, int voff, int soff) { return __builtin_amdgcn_raw_buffer_load_b128(rs, voff, soff, SC1); }
DI void st_sc1(__amdgpu_buffer_rsrc_t rs, v4u v, int voff, int soff) { __builtin_amdgcn_raw_buffer_store_b128(v, rs, voff + soff, 0, SC1); }

DI void blank(__amdgpu_buffer_rsrc_t rs, int voff, int soff) {
  unsigned o; asm volatile("v_mov_b32 %0, -1" : "=v"(o));
  st_sc1(rs, v4u{o, o, o, o}, voff, soff);
}
DI unsigned umax3(unsigned a, unsigned b, unsigned c) { return __builtin_elementwise_max(__builtin_elementwise_max(a, b), c); }
DI unsigned chk4(unsigned m, v4u v) { return umax3(umax3(m, v[0], v[1]), v[2], v[3]); }
DI bool again(bool bad, unsigned& tries) { if (!__any(bad) || ++tries > 512u) return false; __builtin_amdgcn_s_sleep(4); asm volatile("" ::: "memory"); return true; }
DI bool again(unsigned m, unsigned& tries) { return again(m == 0xffffffffu, tries); }
DI bool isnan_(float v) { return __builtin_isnan(v); }
DI float add1(float a, float b) { float r; asm("v_add_f32 %0, %1, %2" : "=v"(r) : "v"(a), "v"(b)); return r; }
DI void announce(unsigned char* ws, int word_off, bool active, unsigned tag) { if (active) (void)__hip_atomic_exchange((gu32*)(ws + word_off), tag, RLX_AGENT); }

DI void row_stats(float s1, float s2, float& mu, float& rs) {
  const float m = s1 * (1.0f / 512.0f);
  const float var = __builtin_fmaxf(__builtin_fmaf(-m, m, s2 * (1.0f / 512.0f)), 0.f);
  mu = rbf(m);
  rs = __builtin_amdgcn_rcpf(rbf(__builtin_amdgcn_sqrtf(rbf(rbf(var) + 1e-5f))));
}
DI v4u ln_frag(v4u x, float mu, float rs) {
  v4u r;
#pragma unroll
  for (int i = 0; i < 4; ++i) { const unsigned d = pk2(bflo(x[i]) - mu, bfhi(x[i]) - mu); r[i] = pk2(bflo(d) * rs, bfhi(d) * rs); }
  return r;
}
DI void scale_tok(f32x16& a, float rs) {
#pragma unroll
  for (int i = 0; i < 16; ++i) a[i] *= rs;
}
DI void scale_feat(f32x16& a, const f32x4 (&u)[4]) {
#pragma unroll
  for (int i = 0; i < 16; ++i) a[i] *= u[i >> 2][i & 3];
}
DI unsigned phi2(float a0, float a1) {
    const unsigned t = pk2(a0, a1);
    const f32x2 v = {bflo(t), bfhi(t)}, u = v * 1.4426950408889634f;
    const f32x2 e = {__builtin_amdgcn_fmed3f(__builtin_amdgcn_exp2f(u[0]), 0.f, 1.f), __builtin_amdgcn_fmed3f(__builtin_amdgcn_exp2f(u[1]), 0.f, 1.f)};
    float p0, p1; asm("v_max_f32 %0, 0, %1" : "=v"(p0) : "v"(v[0])); asm("v_max_f32 %0, 0, %1" : "=v"(p1) : "v"(v[1]));
    const f32x2 y = (f32x2{p0, p1} + e) + 1e-6f;
    return pk2(y[0], y[1]);
}
DI v4u phi_pack8(const f32x16& a, int o) { return v4u{phi2(a[o], a[o + 1]), phi2(a[o + 2], a[o + 3]), phi2(a[o + 4], a[o + 5]), phi2(a[o + 6], a[o + 7])}; }
#define B4S 32
#define B1S 8
#define OBD -1
#define B3N 6
#define WOD 4
#define EARLY3 1
#define EARLY4 1
#define EARLY1 1
#define OPRIO 3
#define APRIO 0
#define NEARLY 4
#define PACE2 0
#define NB1 3
#define NB2 15
#define PACE1 0
#define NA1 6
#define NA0 8
#define PFX 14
#define PF0X 8
constexpr int PF = PFX, PF0 = PF0X;
DI v4u ld_frag(__amdgpu_buffer_rsrc_t rs, int l16, int base, int s) { return ld_sc1(rs, l16 + (s & 3) * 1024, base + (s >> 2) * 4096); }
DI f32x16 gemm_out(lptr L, lptr p, __amdgpu_buffer_rsrc_t rs, int l16, int base, unsigned wo_target, bool tell, bool bar) {
  constexpr int D = 5, P = 16;
  v4u wr[D + 1], xr[P + 1];
#pragma unroll
  for (int s = 0; s < P; ++s) xr[s] = ld_frag(rs, l16, base, s);
  SB();
  ctr_wait(L, C_WO, wo_target);
#pragma unroll
  for (int s = 0; s < D; ++s) wr[s] = wfrag(p, s * 1024);
  SB();
  f32x16 a = splat16(0.f);
#pragma unroll
  for (int s = 0; s < 32; ++s) {
    if (s + P < 32) xr[(s + P) % (P + 1)] = ld_frag(rs, l16, base, s + P);
    if (s + P == 32 && tell) ctr_arrive(L, C_OP, 0);
    if (s + D < 32) wr[(s + D) % (D + 1)] = wfrag(p, (s + D) * 1024);
    a = MFMA(wr[s % (D + 1)], xr[s % (P + 1)], a);
    SB();
    if (s == B4S && bar) { asm volatile("s_barrier" ::: "memory"); SB(); }
  }
  return a;
}
DI float colsum(lptr p) {
  constexpr int D = 5;
  v4u wr[D + 1];
  const v4u ones = {ONE2, ONE2, ONE2, ONE2};
#pragma unroll
  for (int s = 0; s < D; ++s) wr[s] = wfrag(p, s * 1024);
  SB();
  f32x16 a = splat16(0.f);
#pragma unroll
  for (int s = 0; s < 32; ++s) {
    if (s + D < 32) wr[(s + D) % (D + 1)] = wfrag(p, (s + D) * 1024);
    a = MFMA(ones, wr[s % (D + 1)], a);
    SB();
  }
  return a[0];
}
DI float x16_sum(float v) { auto r = __builtin_amdgcn_permlane16_swap(__float_as_uint(v), __float_as_uint(v), false, false); return __uint_as_float(r[0]) + __uint_as_float(r[1]); }
DI v2u split3(float c) { const float hi = rbf(c), mid = rbf(c - hi), lo = (c - hi) - mid; return v2u{pk2(hi, mid), pk2(lo, 0.f)}; }
DI v4u wide(v2u v) { return v4u{v[0], v[1], 0u, 0u}; }
constexpr int KS = 24;
template <bool ISB, class F> DI void gemm_kv(v4u (&xf)[32], lptr L, lptr pq, lptr pv, f32x16& aK, f32x16& aV, __amdgpu_buffer_rsrc_t rs, int lane, int base,
                                    unsigned& nm, v2u& eq, float& rstd, f32x4 (&rs16)[4], unsigned cs_target, unsigned st_target, int t, bool tell, const Probes& pb, F&& kpart) {
  constexpr int D = 3;
  const int l16 = lane * 16, r = lane & 31, h = lane >> 5;
  v4u wk[D + 1], wv[D + 1];
#pragma unroll
  for (int s = 0; s < PF0; ++s) xf[s] = ld_frag(rs, l16, base, s);
  SB();
  const lptr rp = L + L_RS + (t & 1) * 128 + (t >> 1) * 512, np = L + L_NM + t * 1024, cs = L + L_WX + (ISB ? 384 : 0) + r * 4;
  v2u ek = split3(*(const LAS float*)(cs + 128)), ev = split3(*(const LAS float*)(cs + 256));
  eq = split3(*(const LAS float*)cs);
  PIN(ek); PIN(ev); PIN(eq);
#pragma unroll
  for (int s = 0; s < D; ++s) { wk[s] = wfrag(pq, L_WK + s * 1024); wv[s] = wfrag(pv, s * 1024); }
  SB();
  aK = splat16(0.f); aV = splat16(0.f);
  unsigned cv = 0;
#define MK(s) aK = ISB ? MFMA(xf[s], wk[(s) % (D + 1)], aK) : MFMA(wk[(s) % (D + 1)], xf[s], aK)
  static_for<KS>([&](auto SI) __attribute__((always_inline)) {
    constexpr int s = decltype(SI)::value;
    wk[(s + D) % (D + 1)] = wfrag(pq, L_WK + (s + D) * 1024);
    if constexpr (s < PF - PF0) xf[PF0 + 2 * s] = ld_frag(rs, l16, base, PF0 + 2 * s);
    if constexpr (s == KS - 3) cv = __hip_atomic_load((LAS unsigned*)(L + L_CTR + 4 * C_ST), RLX_WG);
    MK(s);
    SB();
    wv[(s + D) % (D + 1)] = wfrag(pv, (s + D) * 1024);
    if constexpr (s < PF - PF0) xf[PF0 + 2 * s + 1] = ld_frag(rs, l16, base, PF0 + 2 * s + 1);
    else if constexpr (s + PF < 32) xf[s + PF] = ld_frag(rs, l16, base, s + PF);
    if constexpr (s + PF == 32) { if (tell) ctr_arrive(L, C_XI, lane); }
    aV = MFMA(xf[s], wv[s % (D + 1)], aV);
    if constexpr (s == 0) retire(pb);
    SB();
  });
  static_for<32 - KS>([&](auto SI) __attribute__((always_inline)) {
    constexpr int s = KS + decltype(SI)::value;
    if constexpr (s + D < 32) wk[(s + D) % (D + 1)] = wfrag(pq, L_WK + (s + D) * 1024);
    if constexpr (s == 27) {
      if (__builtin_amdgcn_readfirstlane(cv) < st_target) ctr_wait(L, C_ST, st_target);
      asm volatile("" ::: "memory");
      nm = *(const LAS unsigned*)(np + r * 4); rstd = *(const LAS float*)(rp + r * 4);
    }
    if constexpr (s == 29) { rs16[0] = lds_r128f(rp + h * 16); rs16[1] = lds_r128f(rp + h * 16 + 32); }
    if constexpr (s == 30) { rs16[2] = lds_r128f(rp + h * 16 + 64); rs16[3] = lds_r128f(rp + h * 16 + 96); }
    if constexpr (s == 31) { nm = h ? 0u : nm; const v4u xe = {nm, nm & 0xffffu, 0u, 0u}; aK = ISB ? MFMA(xe, wide(ek), aK) : MFMA(wide(ek), xe, aK); }
    MK(s);
    SB();
  });
#undef MK
  static_for<32 - KS>([&](auto SI) __attribute__((always_inline)) {
    constexpr int i = decltype(SI)::value, s = KS + i;
    if constexpr (s + D < 32) wv[(s + D) % (D + 1)] = wfrag(pv, (s + D) * 1024);
    if constexpr (i == 0) { const v4u xe = {nm, nm & 0xffffu, 0u, 0u}; aV = MFMA(xe, wide(ev), aV); }
    aV = MFMA(xf[s], wv[s % (D + 1)], aV);
    kpart(SI);
    SB();
  });
}
DI f32x16 gemm_q(const v4u (&xf)[32], lptr p, unsigned nm, v2u eq) {
  constexpr int D = 5;
  v4u wr[D + 1];
#pragma unroll
  for (int s = 0; s < D; ++s) wr[s] = wfrag(p, s * 1024);
  const v4u xe = {nm, nm & 0xffffu, 0u, 0u};
  f32x16 a = MFMA(wide(eq), xe, splat16(0.f));
  SB();
#pragma unroll
  for (int s = 0; s < 32; ++s) {
    if (s + D < 32) wr[(s + D) % (D + 1)] = wfrag(p, (s + D) * 1024);
    a = MFMA(wr[s % (D + 1)], xf[s], a);
    SB();
  }
  return a;
}

constexpr unsigned long long diag_mask(int i) { return ((unsigned long long)(0xffffffffu << ((i & 3) + 8 * (i >> 2) + 4)) << 32) | (0xffffffffu << ((i & 3) + 8 * (i >> 2))); }
template <bool EDGE>
DI f32x16 attention(lptr L, unsigned v_target, bool late, lptr kp, lptr vp, int vs, lptr mp, v4u qf, int nv) {
  v4u kf[9];
  kf[8] = lds_r128(kp + 8 * 1024);
#pragma unroll
  for (int n = 0; n < 8; ++n) kf[n] = lds_r128(kp + n * 1024);
  SB();
  f32x16 s[9];
  float m0, m1, m2, m3;
#define SC(n) s[n] = MFMA(kf[n], qf, splat16(EDGE ? ((n) < nv ? -3.0e38f : 0.f) : 0.f))
#define MX(n) do { m0 = max3(m0, s[n][0], s[n][1]); m1 = max3(m1, s[n][2], s[n][3]); m2 = max3(m2, s[n][4], s[n][5]); m3 = max3(m3, s[n][6], s[n][7]); \
    m0 = max3(m0, s[n][8], s[n][9]); m1 = max3(m1, s[n][10], s[n][11]); m2 = max3(m2, s[n][12], s[n][13]); m3 = max3(m3, s[n][14], s[n][15]); } while (0)
#define MG(i) s[0][i] = __builtin_amdgcn_inverse_ballot_w64(diag_mask(i)) ? s[8][i] : s[0][i]
  s[8] = MFMA(kf[8], qf, splat16(0.f)); SC(0); SC(1); SB();
  SC(2); MG(0); MG(1); MG(2); MG(3); MG(4); MG(5); MG(6); MG(7); SB();
  SC(3); MG(8); MG(9); MG(10); MG(11); MG(12); MG(13); MG(14); MG(15); SB();
  SC(4); m0 = max3(s[1][0], s[1][1], s[1][2]); m1 = max3(s[1][3], s[1][4], s[1][5]); m2 = max3(s[1][6], s[1][7], s[1][8]); m3 = max3(s[1][9], s[1][10], s[1][11]);
  m0 = max3(m0, s[1][12], s[1][13]); m1 = max3(m1, s[1][14], s[1][15]); SB();
  SC(5); MX(0); SB();
  SC(6); MX(2); SB();
  SC(7); MX(3); SB();
  const unsigned cv = __hip_atomic_load((LAS unsigned*)(L + L_CTR + 4 * C_KV), RLX_WG);
  const v4u mlo = lds_r128(mp), mhi = lds_r128(mp + 1024);
  const v4u da = lds_r128(vp + 8 * vs), db = lds_r128(vp + 8 * vs + 512);
  MX(4); MX(5); PIN(m0); PIN(m1); PIN(m2); PIN(m3); SB();
  if (__builtin_amdgcn_readfirstlane(cv) < v_target) ctr_wait(L, C_KV, v_target);
  asm volatile("" ::: "memory");
  v4u va = lds_r128(vp), vb = lds_r128(vp + 512);
  MX(6); MX(7);
#undef SC
#undef MX
#undef MG
  constexpr float C = 0.25f * 1.4426950408889634f;
  const float nm = -C * half_max(__builtin_fmaxf(max3(m0, m1, m2), m3));
  f32x2 C2 = {C, C}, nm2 = {nm, nm};
  f32x16 o = splat16(0.f);
#pragma unroll
  for (int n = 0; n < 8; ++n) {
    v4u na, nb;
    if (n < 7) { vp += vs; na = lds_r128(vp); nb = lds_r128(vp + 512); }
    f32x2 e[8];
#pragma unroll
    for (int i = 0; i < 8; ++i) { const f32x2 v = {s[n][2 * i], s[n][2 * i + 1]}; e[i] = __builtin_elementwise_fma(v, C2, nm2); }
#pragma unroll
    for (int i = 0; i < 8; ++i) PIN(e[i]);
    v4u p0, p1;
#pragma unroll
    for (int i = 0; i < 4; ++i) p0[i] = pk2(__builtin_amdgcn_exp2f(e[i][0]), __builtin_amdgcn_exp2f(e[i][1]));
#pragma unroll
    for (int i = 0; i < 4; ++i) p1[i] = pk2(__builtin_amdgcn_exp2f(e[4 + i][0]), __builtin_amdgcn_exp2f(e[4 + i][1]));
    if (n == 0) {
      const v4u d0 = p0 & mlo, d1 = p1 & mhi;
      o = MFMA(da, d0, o); o = MFMA(va, p0 ^ d0, o); o = MFMA(db, d1, o); o = MFMA(vb, p1 ^ d1, o);
    } else { o = MFMA(va, p0, o); o = MFMA(vb, p1, o); }
    if (n < 7) { va = na; vb = nb; }
    SB();
    if (n == B3N && late) BAR();
  }
  return o;
}

__global__ void __launch_bounds__(512, 2) mega(const u16* __restrict__ x_in, const u16* __restrict__ Aw, const u16* __restrict__ Ao,
                                               const u16* __restrict__ Bw, const u16* __restrict__ Bo, u16* __restrict__ out, unsigned char* ws) {
  extern __shared__ __attribute__((aligned(16))) char smem[];
  const lptr L = (lptr)smem;
  const unsigned L0 = (unsigned)(uintptr_t)L;
  int b = blockIdx.x, lane = threadIdx.x & 63, w = __builtin_amdgcn_readfirstlane(threadIdx.x >> 6);
  const __amdgpu_buffer_rsrc_t rs = __builtin_amdgcn_make_buffer_rsrc(ws, 0, WS_END, 0x00020000);
  STAMP(63);
  unsigned zero = 0; PIN(zero);
  unsigned tag;
  v4u x0 = {0u, 0u, 0u, 0u}, x1 = x0;

  {
    const int g = MAP_G(b), j = MAP_J(b), t = w & 3, r = lane & 31, h = lane >> 5;
    unsigned epoch = __hip_atomic_load((gu32*)ws, RLX_AGENT);
    if (w < 4) {
      unsigned tlb = 0;
      if (w == 2) tlb = __builtin_amdgcn_raw_buffer_load_b32(rs, (lane >> 1) * 1024 + ((lane & 1) * 32 + 8 * (j & 3)) * 16, WS_X + (g * 4 + (j >> 2)) * 32768, SC1);
      if (w == 0) { const v4u z = {0u, 0u, 0u, 0u}; lds_w128(L + L_WX + lane * 16, z); }
      BAR();
      unsigned warm = 0;
      if (w == 1 && lane < 48) warm = *(const GAS unsigned*)((const char*)Aw + (b * 48 + lane) * 128);
      if (w == 2 && lane < 48) warm = *(const GAS unsigned*)((const char*)Bw + (b * 48 + lane) * 128);
      if (w == 3 && lane < 32) warm = *(const GAS unsigned*)((const char*)(lane < 16 ? Ao : Bo) + (b * 16 + (lane & 15)) * 128);
      if (w == 3 && lane >= 32 && lane < 45) {
        const int i = lane - 32;
        const int o = i < 4 ? WS_FX + i * 256 * FL : i < 8 ? WS_FO + (i - 4) * 256 * FL : i < 10 ? WS_FH + (i - 8) * 256 * FL : i < 12 ? WS_FP + (i - 10) * 256 * FL : WS_FR;
        (void)__hip_atomic_fetch_add((gu32*)(ws + o + (g * 16 + j) * FL), zero, RLX_AGENT);
      }
      if (w == 0) { unsigned long long pc; asm volatile("s_getpc_b64 %0" : "=s"(pc));
        const int o = ((b >> 3) * 11 + lane) * 128; if (lane < 11 && o < CODEPF) warm = *(const GAS unsigned*)((const char*)(pc & ~127ull) + o); }
      dma_rows<0>(Aw + 32 * j, 1536, 32 + 16 * t, L0 + 32768 + 16384 * t, 4, lane, ROT * (g & 3));
      dma_rows<0>(Aw + 32 * j, 1536, 8 * t, L0 + 8192 * t, 2, lane);
      ctr_arrive(L, C_PR, lane);
      blank(rs, lane * 16, WS_O + (g * 4 + t) * 32768 + 2 * j * 1024);
#pragma unroll
      for (int i = 0; i < 4; ++i) blank(rs, lane * 16 + i * 1024, WS_KV + ((g * 16 + j) * 4 + t) * 4096);
      asm volatile("s_waitcnt vmcnt(13)" :: "v"(warm), "v"(tlb) : "memory");
      STAMP(59);
      ctr_arrive(L, C_R, lane); ctr_wait(L, C_R, 5u);
    } else {
      __builtin_amdgcn_s_setprio(3);
      const u16* xrow = x_in + (size_t)(g * 128 + 8 * j + 2 * t + h) * 512 + 8 * r;
      const v4u xa = *(const GAS v4u*)xrow, xb = *(const GAS v4u*)(xrow + 256);
      BAR();
      { const unsigned fill = (lane & 16) ? 0x3f800000u : ONE2; const v4u o = {fill, fill, fill, fill}; if (w == 4) lds_w128(L + L_ONES + lane * 16, o); }
      if (h == 0) *(LAS unsigned*)(L + L_NM + t * 1024 + r * 4) = 0u;
      if (w >= 6) { v4u mk;
#pragma unroll
        for (int i = 0; i < 4; ++i) { const int key = ((2 * i) & 3) + 8 * (i >> 1) + 16 * (w - 6) + 4 * h; mk[i] = (key + 1 <= r) ? 0xffffffffu : ((key <= r) ? 0xffffu : 0u); }
        lds_w128(L + L_MSK + (w - 6) * 1024 + lane * 16, mk); }
      float s1 = 0.f, s2 = 0.f;
#pragma unroll
      for (int i = 0; i < 4; ++i) { s1 = dot2(xa[i], ONE2, s1); s2 = dot2(xa[i], xa[i], s2); s1 = dot2(xb[i], ONE2, s1); s2 = dot2(xb[i], xb[i], s2); }
      float m0, r0; row_stats(sum32(s1), sum32(s2), m0, r0);
      const v4u na = ln_frag(xa, m0, r0), nb = ln_frag(xb, m0, r0);
      { const lptr d = L + L_LN + (r >> 1) * 256 + (2 * t + h) * 16 + (r & 1) * 8;
        v2u u = {na[0], na[1]}; *(LAS v2u*)d = u; u[0] = na[2]; u[1] = na[3]; *(LAS v2u*)(d + 128) = u;
        u[0] = nb[0]; u[1] = nb[1]; *(LAS v2u*)(d + 4096) = u; u[0] = nb[2]; u[1] = nb[3]; *(LAS v2u*)(d + 4096 + 128) = u; }
      STAMP(56);
      ctr_arrive(L, C_ST, lane); ctr_wait(L, C_ST, 4u);
      const int vo = (lane >> 4) * 1024 + (((lane >> 3) & 1) * 32 + 8 * (j & 3) + (lane & 7)) * 16;
      const int xb0 = WS_X + (g * 4 + (j >> 2)) * 32768 + t * 8192;
      st_sc1(rs, lds_r128(L + L_LN + t * 2048 + lane * 16), vo, xb0);
      st_sc1(rs, lds_r128(L + L_LN + t * 2048 + 1024 + lane * 16), vo, xb0 + 4096);
      PIN(epoch);
      publish_wg(L, C_PX, 4u, lane, ws, WS_FX + (g * 16 + lane) * FL + j * 4, lane < 16, epoch + 1u);
      ctr_arrive(L, C_P, lane);
      STAMP(57);
      if (t == 3) { (void)poll(ws, WS_FX + (g * 16 + j) * FL, lane, 16, __builtin_amdgcn_readfirstlane(epoch) + 1u, zero); ctr_arrive(L, C_R, lane); STAMP(59); }
      __builtin_amdgcn_s_setprio(0);
      ctr_wait(L, C_PR, 4u);
      v4u rt[4];
#pragma unroll
      for (int i = 0; i < 4; ++i) {
        const bool po = (i >= 2) && (t & 1);
        const u16* src = po ? (t == 1 ? Ao : Bo) + (32 * g + 16 * (i & 1)) * 512
                            : (t < 2 ? Bw : Aw) + (32 * g + 16 * (i & 1)) * 1536 + (2 * (t & 1) + (i >> 1)) * 512;
        rt[i] = *(const GAS v4u*)(src + 32 * j + (lane >> 2) * (po ? 512 : 1536) + (lane & 3) * 8);
      }
      STAMP(58);
      ctr_wait(L, C_XI, 4u);
#pragma unroll
      for (int i = 0; i < 4; ++i) {
        const bool po = (i >= 2) && (t & 1);
        const int piece = (po ? (t == 1 ? T_OA : T_OB) : (t < 2 ? T_WB : T_WA) + 32 * (2 * (t & 1) + (i >> 1))) + 2 * g + (i & 1);
        st_sc1(rs, rt[i], lane * 16, WS_WT + (j * 256 + piece) * 1024);
      }
    }
    PIN(epoch);
    tag = __builtin_amdgcn_readfirstlane(epoch) + 1u;
  }

  for (int pr = 0; pr < 2; ++pr) {
#pragma unroll
    for (int isB = 0; isB < 2; ++isB) {
      const int l = 2 * pr + isB;
      asm volatile("" : "+v"(lane), "+s"(b), "+s"(w));
      const int g = MAP_G(b), j = MAP_J(b);
      const int t = w & 3, hh = w >> 2, r = lane & 31, h = lane >> 5;
      const bool isQ = w < 4;
      const int gt = g * 4 + t, gj = g * 16 + j;
      const int trl = ((lane & 15) >> 2) * 64 + ((lane >> 4) & 1) * 32 + (lane & 3) * 8 + h * 256;
      const int l16 = lane * 16;
      const unsigned char* wt = ws + WS_WT + j * (256 * 1024);
      v4u qf;
      int mo_ = WS_O + l * MB2 + gt * 32768 + (2 * j + hh) * 1024 + l16;
      lptr ma0 = L + L_PR + (r < 16 ? (hh * 16 + r + 32 * h) * 16 : 2048 + (hh * 16 + 4 * h) * 4), ma1 = ma0 + (r < 16 ? 1024 : 32);
      if (isQ) {
        const lptr pq = L + trl;
        lptr pv = pq + L_WV; PIN(pv);
        STAMP(l * 14 + 1);
        Probes pb_ = poll(ws, WS_FX + (l * 256 + gj) * FL, lane, l > 0 ? 16 : 0, tag, zero);
        STAMP(l * 14 + 2);
        const int xb = WS_X + l * MB2 + gt * 32768;
        f32x16 aK, aV; float rstd; f32x4 rs16[4]; v4u xf[32]; unsigned xe; v2u eq;
        unsigned tries = 0;
        v4u k0, k1, vf0, vf1; f32x16 d; float z;
        do {
          if (!isB) gemm_kv<false>(xf, L, pq, pv, aK, aV, rs, lane, xb, xe, eq, rstd, rs16, 0u, 4u * l + 4u, t, tries == 0, pb_,
            [&](auto SI) __attribute__((always_inline)) { constexpr int i = decltype(SI)::value;
              if constexpr (i == B1S) { if (tries == 0) { asm volatile("s_waitcnt vmcnt(0)\n\ts_barrier" ::: "memory"); } }
              if constexpr (i >= 2 && i < 6) { constexpr int c = 4 * (i - 2); unsigned u = pk2(aK[c] * rstd, aK[c + 1] * rstd), v = pk2(aK[c + 2] * rstd, aK[c + 3] * rstd); PIN(u); PIN(v);
                if constexpr (i < 4) { k0[2 * i - 4] = u; k0[2 * i - 3] = v; } else { k1[2 * i - 8] = u; k1[2 * i - 7] = v; } } });
          else {
            gemm_kv<true>(xf, L, pq, pv, aK, aV, rs, lane, xb, xe, eq, rstd, rs16, 3u, 4u * l + 4u, t, tries == 0, pb_,
              [&](auto SI) __attribute__((always_inline)) { constexpr int i = decltype(SI)::value;
                if constexpr (i >= 2 && i < 6) { constexpr int c = 2 * (i - 2); unsigned u = phi2(aK[c] * rs16[c >> 2][c & 3], aK[c + 1] * rs16[c >> 2][(c & 3) + 1]); PIN(u); k0[i - 2] = u; } });
            scale_feat(aV, rs16);
            vf0 = pack8(aV, 0);
#pragma unroll
            for (int i = 8; i < 16; ++i) aK[i] *= rs16[i >> 2][i & 3];
            d = MFMA(k0, vf0, splat16(0.f));
            k1 = phi_pack8(aK, 8); vf1 = pack8(aV, 8);
            d = MFMA(k1, vf1, d);
            float z0 = 0.f, z1 = 0.f;
#pragma unroll
            for (int i = 0; i < 4; ++i) { z0 = dot2(k0[i], ONE2, z0); z1 = dot2(k1[i], ONE2, z1); }
            z = half_sum(z0 + z1); PIN(z);
          }
          pb_ = Probes{0u, 0u, 0u};
        } while (again(isnan_(isB ? d[0] : aK[0]), tries));
        RT(0);
        STAMP(l * 14 + 12);
        if (!isB) {
          DRAIN();
          if (B1S > 7) BAR();
          const int kb = WS_KV + ((pr * 256 + gj) * 4 + t) * 4096;
          if (g < 15) { st_sc1(rs, k0, l16, kb); st_sc1(rs, k1, l16 + 1024, kb); }
          scale_feat(aV, rs16);
          vf0 = pack8(aV, 0); vf1 = pack8(aV, 8);
          if (g < 15) { st_sc1(rs, vf0, l16 + 2048, kb); st_sc1(rs, vf1, l16 + 3072, kb); }
          STAMP(l * 14 + 4);
          lds_w128(L + L_K + (8 + t) * 1024 + l16, k0); lds_w128(L + L_K + (20 + t) * 1024 + l16, k1);
          const lptr vd = L + L_V + ((r >> 4) * 12 + 8 + t) * 1024 + h * 256 + (r & 15) * 16;
          lds_w128(vd, vf0); lds_w128(vd + 512, vf1);
          f32x16 aQ = gemm_q(xf, pq, xe, eq);
          scale_tok(aQ, rstd);
          qf = pack8(aQ, 0); lds_w128(L + L_QF + t * 1024 + l16, pack8(aQ, 8));
          STAMP(l * 14 + 5);
          if (pr == 0 && t < 3) {
            const float c = colsum(pq + t * 32768);
            if (h == 0) *(LAS float*)(L + L_WX + (t * 32 + r) * 4) = c;
          }
        } else {
          const lptr pw = L + L_PW + t * PSTR;
          if (h == 0) *(LAS float*)(pw + 2048 + r * 4) = z;
          if (r < 16) { *(LAS f32x4*)(pw + l16) = f32x4{d[0], d[1], d[2], d[3]}; *(LAS f32x4*)(pw + 1024 + l16) = f32x4{d[4], d[5], d[6], d[7]}; asm volatile("" ::: "memory"); }
          else { *(LAS f32x4*)(pw + l16) = f32x4{d[8], d[9], d[10], d[11]}; *(LAS f32x4*)(pw + 1024 + l16) = f32x4{d[12], d[13], d[14], d[15]}; }
          BAR();
          STAMP(l * 14 + 4);
          f32x16 aQ = gemm_q(xf, pq, xe, eq);
          if (pr == 0) ctr_arrive(L, C_QD, lane);
          scale_tok(aQ, rstd);
          qf = phi_pack8(aQ, 0); lds_w128(L + L_QF + t * 1024 + l16, phi_pack8(aQ, 8));
          STAMP(l * 14 + 5);
          DMA_W(0, T_OB, Bo, 512, 8 * t, L0 + L_WO + 8192 * t, 2);
          if (pr == 0) {
            ctr_wait(L, C_QD, 4u);
            DMA_W(PACE2, T_WA, Aw, 1536, 24 * t, L0 + 24576 * t, NB1);
          }
          PIN(ma0); PIN(ma1);
        }
        PIN(mo_);
        BAR();
      } else {
        __builtin_amdgcn_s_setprio(OPRIO);
        if (l > 0) {
          const Probes pb_ = poll(ws, WS_FX + (l * 256 + gj) * FL, lane, 16, tag, zero);
          v4u st[4];
          unsigned tries = 0, m;
          do {
#pragma unroll
            for (int i = 0; i < 4; ++i) st[i] = ld_sc1(rs, l16 + i * 1024, WS_XS + (l * 64 + gt) * 4096);
            m = chk4(chk4(chk4(chk4(0u, st[0]), st[1]), st[2]), st[3]);
          } while (again(m, tries));
          retire(pb_);
          RT(1);
          f32x4 a;
#pragma unroll
          for (int i = 0; i < 4; ++i) a[i] = add1(add1(__uint_as_float(st[0][i]), __uint_as_float(st[1][i])), add1(__uint_as_float(st[2][i]), __uint_as_float(st[3][i])));
          float m0, r0, m1, r1;
          row_stats(half_sum(x16_sum(a[0])), half_sum(x16_sum(a[1])), m0, r0);
          row_stats(half_sum(x16_sum(a[2])), half_sum(x16_sum(a[3])), m1, r1);
          if (lane < 16) { *(LAS v2u*)(L + L_NM + t * 1024 + lane * 8) = v2u{pk2(-m0, -m0), pk2(-m1, -m1)};
            *(LAS f32x2*)(L + L_RS + (t & 1) * 128 + (t >> 1) * 512 + lane * 8) = f32x2{r0, r1}; }
          ctr_arrive(L, C_ST, lane);
        }
        STAMP(l * 14 + 13);
        ctr_wait(L, C_XI, 4u * l + 4u);
        blank(rs, l16, WS_O + l * MB2 + gt * 32768 + (2 * j + 1) * 1024);
        if (l < 3) {
          blank(rs, l16, WS_X + (l + 1) * MB2 + gt * 32768 + j * 2048); blank(rs, l16 + 1024, WS_X + (l + 1) * MB2 + gt * 32768 + j * 2048);
          if (lane < 16) blank(rs, l16, WS_XS + (((l + 1) * 64 + gt) * 16 + j) * 256);
        }
        if (!isB && t == 0) { const int pb = WS_P + ((pr * 16 + j) * 16 + g) * PSTR; blank(rs, l16, pb); blank(rs, l16 + 1024, pb); if (lane < 8) blank(rs, l16 + 2048, pb); }
        STAMP(l * 14 + 1);
        BAR();
        STAMP(l * 14 + 4);
        if (!isB) {
          if (t == 0) announce(ws, WS_FH + (pr * 256 + gj + 16 * (lane + 1)) * FL + lane * 4, lane < 2 && g + lane < 15, tag);
          v4u kv[8];
          const Probes pb_ = poll(ws, WS_FH + (pr * 256 + gj) * FL, lane, g < 2 ? g : 2, tag, zero);
          STAMP(l * 14 + 2);
          const v4u z4 = {0u, 0u, 0u, 0u};
          const int kb0 = WS_KV + ((pr * 256 + gj - 16) * 4 + t) * 4096, kb1 = kb0 - 16 * 4 * 4096;
          unsigned tries = 0;
#define LDH(i0) do { if (g > 0) { kv[i0] = ld_sc1(rs, l16 + (i0) * 1024, kb0); kv[i0 + 1] = ld_sc1(rs, l16 + (i0) * 1024 + 1024, kb0); } else { kv[i0] = z4; kv[i0 + 1] = z4; } \
                     if (g > 1) { kv[4 + i0] = ld_sc1(rs, l16 + (i0) * 1024, kb1); kv[5 + i0] = ld_sc1(rs, l16 + (i0) * 1024 + 1024, kb1); } else { kv[4 + i0] = z4; kv[5 + i0] = z4; } } while (0)
#define BADH(i0) chk4(chk4(chk4(chk4(0u, kv[i0]), kv[i0 + 1]), kv[4 + i0]), kv[5 + i0])
          LDH(0); LDH(2);
          SB();
          while (again(BADH(0), tries)) LDH(0);
          retire(pb_);
          lds_w128(L + L_K + (4 + t) * 1024 + l16, kv[0]); lds_w128(L + L_K + (16 + t) * 1024 + l16, kv[1]);
          lds_w128(L + L_K + t * 1024 + l16, kv[4]); lds_w128(L + L_K + (12 + t) * 1024 + l16, kv[5]);
          STAMP(l * 14 + 5);
          BAR();
          while (again(BADH(2), tries)) LDH(2);
          { const lptr vd = L + L_V + ((r >> 4) * 12 + t) * 1024 + h * 256 + (r & 15) * 16;
            lds_w128(vd + 4096, kv[2]); lds_w128(vd + 4096 + 512, kv[3]); lds_w128(vd, kv[6]); lds_w128(vd + 512, kv[7]); }
          ctr_arrive(L, C_KV, lane);
          if (pr == 0) publish_wg(L, C_LNQ, 4u, lane, ws, WS_FR + (lane * 16 + j) * FL + g * 4, lane < 16, tag); else DRAIN();
          RT(2);
#undef LDH
#undef BADH
        } else {
          { const int pb = WS_P + ((pr * 16 + j) * 16 + g) * PSTR;
            if (t < 3) {
              const lptr pp = L + L_PW + t * 1024 + l16;
              f32x4 u[4];
#pragma unroll
              for (int q = 0; q < 4; ++q) u[q] = lds_r128f(pp + q * PSTR);
              SB();
              f32x4 sm;
#pragma unroll
              for (int i = 0; i < 4; ++i) sm[i] = add1(add1(u[0][i], u[1][i]), add1(u[2][i], u[3][i]));
              if (t < 2 || lane < 8) st_sc1(rs, __builtin_bit_cast(v4u, sm), l16 + t * 1024, pb);
            } else announce(ws, WS_FP + (pr * 256 + lane * 16 + j) * FL + g * 4, lane < 16, tag);
            STAMP(l * 14 + 3); }
          const Probes pb_ = poll(ws, WS_FP + (pr * 256 + gj) * FL + 16 * t, lane, 4, tag, zero);
          STAMP(l * 14 + 2);
          unsigned tries = 0;
          const lptr pw = L + L_PR + t * PSTR;
          const int pb = WS_P + ((pr * 16 + j) * 16 + 4 * t) * PSTR;
          float chk;
          do {
            v4u a[8];
#pragma unroll
            for (int q = 0; q < 4; ++q) { a[2 * q] = ld_sc1(rs, l16, pb + q * PSTR); a[2 * q + 1] = ld_sc1(rs, l16 + 1024, pb + q * PSTR); }
            const v4u zz = ld_sc1(rs, ((lane >> 3) & 3) * PSTR + 2048 + (lane & 7) * 16, pb);
            SB();
            f32x4 u0, u1, z;
#pragma unroll
            for (int i = 0; i < 4; ++i) {
#define F(x) __uint_as_float(x)
              u0[i] = add1(add1(F(a[0][i]), F(a[2][i])), add1(F(a[4][i]), F(a[6][i]))); u1[i] = add1(add1(F(a[1][i]), F(a[3][i])), add1(F(a[5][i]), F(a[7][i])));
              z[i] = x16_sum(dpp_add<0x128>(F(zz[i])));
#undef F
            }
            *(LAS f32x4*)(pw + l16) = u0; *(LAS f32x4*)(pw + 1024 + l16) = u1;
            if (lane < 8) *(LAS f32x4*)(pw + 2048 + l16) = z;
            chk = add1(add1(add1(u0[0], u0[1]), add1(u0[2], u0[3])), add1(add1(u1[0], u1[1]), add1(u1[2], u1[3])));
            chk = add1(chk, add1(add1(z[0], z[1]), add1(z[2], z[3])));
          } while (again(isnan_(chk), tries));
          retire(pb_);
          RT(3);
          DRAIN();
          PIN(ma0); PIN(ma1); PIN(mo_);
          STAMP(l * 14 + 5);
          BAR();
        }
        qf = lds_r128(L + L_QF + t * 1024 + l16);
        if (!isB) __builtin_amdgcn_s_setprio(APRIO);
      }

      STAMP(l * 14 + 6);
      f32x16 o;
      if (!isB) {
        const lptr kp = L + L_K + (hh * 12 + t) * 1024 + l16;
        const lptr vp = (r < 16 ? L + L_V + (hh * 12 + t) * 1024 + h * 256 : L + L_ONES) + (r & 15) * 16;
        const int vs = r < 16 ? 1024 : 0;
        if (g >= 2) o = attention<false>(L, 4u * pr + 4u, !isQ, kp, vp, vs, L + L_MSK + l16, qf, 0);
        else o = attention<true>(L, 4u * pr + 4u, !isQ, kp, vp, vs, L + L_MSK + l16, qf, 8 - 4 * g - t);
      } else {
        if (OBD >= 0 && w == 0) announce(ws, WS_FO + (l * 256 + g * 16 + lane) * FL + j * 4, lane < 16, tag);
        f32x4 p0[4], p1[4];
#pragma unroll
        for (int q = 0; q < 4; ++q) { p0[q] = lds_r128f(ma0 + q * PSTR); p1[q] = lds_r128f(ma1 + q * PSTR); }
        SB();
        const f32x4 u0 = (p0[0] + p0[1]) + (p0[2] + p0[3]), u1 = (p1[0] + p1[1]) + (p1[2] + p1[3]);
        const v4u af = {pk2(u0[0], u0[1]), pk2(u0[2], u0[3]), pk2(u1[0], u1[1]), pk2(u1[2], u1[3])};
        o = MFMA(af, qf, splat16(0.f));
        o[8] += 1e-6f;
      }
      { const float ri = __builtin_amdgcn_rcpf(o[8]);
#pragma unroll
        for (int i = 0; i < 8; ++i) o[i] *= ri;
        asm volatile("" :: "v"(o[0]));
        STAMP(l * 14 + 7);
        DRAIN();
        if (OBD < 0 && isB && w == 0) announce(ws, WS_FO + (l * 256 + g * 16 + lane) * FL + j * 4, lane < 16, tag);
        st_sc1(rs, pack8(o, 0), mo_, 0);
      }

      if (isQ) {
        if (isB) ctr_arrive(L, C_WO, lane);
        if (!isB) {
          if (pr == 0) (void)poll(ws, WS_FR + gj * FL, lane, 16, tag, zero);
          DMA_W(0, T_WB, Bw, 1536, 24 * t, L0 + 24576 * t, NA1);
        }
        STAMP(l * 14 + 8);
        if (!EARLY3 || !isB) {
          BAR();
          if (t == 0) announce(ws, WS_FO + (l * 256 + g * 16 + lane) * FL + j * 4, lane < 16, tag); else nap<1>();
        }
        if (!isB) {
          DMA_W(0, T_OA, Ao, 512, 8 * t, L0 + L_WO + 8192 * t, 2);
          DRAIN(); ctr_arrive(L, C_WO, lane);
          DMA_W(PACE1, T_WB, Bw, 1536, 24 * t + 4 * NA1, L0 + 24576 * t + 4096 * NA1, 6 - NA1);
        } else if (pr == 0) { nap<3>(); DMA_W(PACE2, T_WA, Aw, 1536, 24 * t + 4 * NB1, L0 + 24576 * t + 4096 * NB1, 6 - NB1); }
        STAMP(l * 14 + 9);
        if (l < 3) {
          DRAIN(); ctr_arrive(L, C_W, lane); ctr_wait(L, C_W, 4u * l + 4u);
          if (l == 0 && t < 3) {
            const float c = colsum(L + trl + t * 32768);
            if (h == 0) *(LAS float*)(L + L_WX + 384 + (t * 32 + r) * 4) = c;
          }
          ctr_wait(L, C_OP, 4u * l + 4u);
          blank(rs, l16, WS_O + (l + 1) * MB2 + gt * 32768 + 2 * j * 1024);
          if (l == 1) {
#pragma unroll
            for (int i = 0; i < 4; ++i) blank(rs, l16 + i * 1024, WS_KV + ((256 + gj) * 4 + t) * 4096);
          }
          STAMP(l * 14 + 11);
          BAR();
          if (t == 0) announce(ws, WS_FX + ((l + 1) * 256 + g * 16 + lane) * FL + j * 4, lane < 16, tag);
        }
      } else {
        __builtin_amdgcn_s_setprio(OPRIO);
        STAMP(l * 14 + 8);
        if (!EARLY3) BAR();
        if (l == 0) {
          const u16* xr = x_in + (size_t)(gt * 32 + r) * 512 + 32 * j + 4 * h;
          const v2u a = *(const GAS v2u*)xr, c = *(const GAS v2u*)(xr + 8), d = *(const GAS v2u*)(xr + 16), e = *(const GAS v2u*)(xr + 24);
          x0 = v4u{a[0], a[1], c[0], c[1]}; x1 = v4u{d[0], d[1], e[0], e[1]};
        }
        const Probes pb_ = poll(ws, WS_FO + (l * 256 + gj) * FL, lane, 16, tag, zero);
        STAMP(l * 14 + 9);
        lptr po = L + L_WO + trl; PIN(po);
        f32x16 y; unsigned tries = 0;
        const int xo_ = WS_X + (l + 1) * MB2 + gt * 32768 + j * 2048 + l16;
        do y = gemm_out(L, po, rs, l16, WS_O + l * MB2 + gt * 32768, 4u * l + 4u, tries == 0, tries == 0 && l < 3); while (again(isnan_(y[0]), tries));
        retire(pb_);
        RT(4);
        asm volatile("" :: "v"(y[0]));
        STAMP(l * 14 + 10);
        if (B4S > 31 && l < 3) BAR();
        { v4u y0 = pack8(y, 0), y1 = pack8(y, 8);
          PIN(y0); PIN(y1);
          f32x16 a;
#pragma unroll
          for (int i = 0; i < 4; ++i) { a[2 * i] = add1(bflo(x0[i]), bflo(y0[i])); a[2 * i + 1] = add1(bfhi(x0[i]), bfhi(y0[i])); }
          x0 = pack8(a, 0);
          if (l < 3) st_sc1(rs, x0, xo_, 0);
#pragma unroll
          for (int i = 0; i < 4; ++i) { a[8 + 2 * i] = add1(bflo(x1[i]), bflo(y1[i])); a[9 + 2 * i] = add1(bfhi(x1[i]), bfhi(y1[i])); }
          x1 = pack8(a, 8); }
        if (l < 3) {
          st_sc1(rs, x1, xo_ + 1024, 0);
          SB();
          float s1 = 0.f, s2 = 0.f, s3 = 0.f, s4 = 0.f;
#pragma unroll
          for (int i = 0; i < 4; ++i) { s1 = dot2(x0[i], ONE2, s1); s2 = dot2(x0[i], x0[i], s2); s3 = dot2(x1[i], ONE2, s3); s4 = dot2(x1[i], x1[i], s4); }
          s1 = half_sum(s1 + s3); s2 = half_sum(s2 + s4);
          if (h == 0) { v2u v = {__float_as_uint(s1), __float_as_uint(s2)}; __builtin_amdgcn_raw_buffer_store_b64(v, rs, r * 8, WS_XS + (((l + 1) * 64 + gt) * 16 + j) * 256, SC1); }
          STAMP(l * 14 + 11);
        } else {
          const auto p = __builtin_amdgcn_permlane32_swap(x0[0], x0[2], false, false), q = __builtin_amdgcn_permlane32_swap(x0[1], x0[3], false, false);
          const auto u = __builtin_amdgcn_permlane32_swap(x1[0], x1[2], false, false), v = __builtin_amdgcn_permlane32_swap(x1[1], x1[3], false, false);
          u16* op = out + (size_t)(gt * 32 + r) * 512 + 32 * j + 8 * h;
          *(GAS v4u*)op = v4u{p[0], q[0], p[1], q[1]}; *(GAS v4u*)(op + 16) = v4u{u[0], v[0], u[1], v[1]};
          if (b == 0 && w == 4 && lane == 0) __hip_atomic_store((gu32*)ws, tag, RLX_AGENT);
        }
      }
    }
  }
}

extern "C" void kernel_launch(void* const* d_in, const int* in_sizes, int n_in, void* d_out, int out_size, void* d_ws, size_t ws_size,
                              hipStream_t stream) {
  (void)in_sizes; (void)n_in; (void)out_size; (void)ws_size;
  (void)hipFuncSetAttribute((const void*)mega, hipFuncAttributeMaxDynamicSharedMemorySize, LDS_BYTES);
  mega<<<dim3(256), dim3(512), LDS_BYTES, stream>>>((const u16*)d_in[0], (const u16*)d_in[1], (const u16*)d_in[2], (const u16*)d_in[4],
                                                    (const u16*)d_in[5], (u16*)d_out, (unsigned char*)d_ws);
}
```

```cpp
#include <hip/hip_runtime.h>
#include <hip/hip_bf16.h>
#include <stdint.h>

#define DI __device__ __forceinline__
#define LAS __attribute__((address_space(3)))
#define GAS __attribute__((address_space(1)))
typedef unsigned short u16;
typedef unsigned v4u __attribute__((ext_vector_type(4)));
typedef unsigned v2u __attribute__((ext_vector_type(2)));
typedef float f32x16 __attribute__((ext_vector_type(16)));
typedef float f32x4 __attribute__((ext_vector_type(4)));
typedef float f32x2 __attribute__((ext_vector_type(2)));
typedef short bf16x8 __attribute__((ext_vector_type(8)));
typedef short s16x4 __attribute__((ext_vector_type(4)));
typedef __bf16 bf2 __attribute__((ext_vector_type(2)));
typedef GAS unsigned gu32;
typedef LAS char* lptr;
#define RLX_AGENT __ATOMIC_RELAXED, __HIP_MEMORY_SCOPE_AGENT
#define RLX_WG __ATOMIC_RELAXED, __HIP_MEMORY_SCOPE_WORKGROUP
#define MFMA(a, b, c) __builtin_amdgcn_mfma_f32_32x32x16_bf16(__builtin_bit_cast(bf16x8, (a)), __builtin_bit_cast(bf16x8, (b)), (c), 0, 0, 0)
#define SB() __builtin_amdgcn_sched_barrier(0)
#define PIN(x) asm volatile("" : "+v"(x))
#define PINS(x) do { x = __builtin_amdgcn_readfirstlane(x); asm volatile("" : "+s"(x)); } while (0)
#define DRAIN() asm volatile("s_waitcnt vmcnt(0)" ::: "memory")
#define LGKM0() asm volatile("s_waitcnt lgkmcnt(0)" ::: "memory")
#define BAR() do { SB(); asm volatile("s_waitcnt lgkmcnt(0)\n\ts_barrier" ::: "memory"); SB(); } while (0)
constexpr int SC1 = 16;
constexpr unsigned ONE2 = 0x3f803f80u;
#define STAMP(i) do {} while (0)
#define RT(k) do {} while (0)

#define PSL 4
#define PS0 0
#define L2PF 0
#define XMAP 1
#define MAP_G(b) (4 * (((b) & 7) >> 1) + ((b) >> 6))
#define MAP_J(b) (8 * ((b) & 1) + (((b) >> 3) & 7))
constexpr int FL = 256;
constexpr int MB2 = 2 << 20;
constexpr int WS_FX = 4096;
constexpr int WS_FO = WS_FX + 4 * 256 * FL;
constexpr int WS_FH = WS_FO + 4 * 256 * FL;
constexpr int WS_FP = WS_FH + 2 * 256 * FL;
constexpr int WS_FR = WS_FP + 2 * 256 * FL;
constexpr int WS_XS = WS_FR + 256 * FL;
constexpr int PSTR = 2304;
constexpr int WS_P = WS_XS + 4 * 1024 * 256;
constexpr int WS_X = 6 << 20;
constexpr int WS_O = WS_X + 4 * MB2;
constexpr int WS_KV = WS_O + 4 * MB2;
constexpr int WS_WT = WS_KV + 4 * MB2;
constexpr int WS_END = WS_WT + 2 * MB2;
constexpr int T_WB = 0, T_WA = 96, T_OA = 192, T_OB = 224;
static_assert(WS_P + 2 * 256 * PSTR <= WS_X, "ws map");

constexpr int L_WQ = 0, L_WK = 32768, L_WV = 65536, L_WO = 98304, L_K = 131072, L_V = L_WO + 8192, L_MISC = 155648;
constexpr int L_QF = L_MISC, L_MSK = L_MISC + 4096, L_ONES = L_MISC + 6144;
constexpr int L_RS = L_ONES + 256;
constexpr int L_WX = L_MISC + 7168, L_MU = L_WX + 768, L_CTR = L_MU + 192;
constexpr int L_NM = L_QF;
constexpr int L_LN = L_K;
constexpr int L_PW = L_K, L_PR = L_K + 4 * PSTR;
constexpr int LDS_BYTES = 163840;
enum { C_W = 0, C_CS = 1, C_CA = 2, C_KV = 3, C_OP = 4, C_P = 5, C_PX = 6, C_PO = 7, C_PH = 8, C_PR = 9, C_R = 10, C_WO = 11, C_XI = 12, C_ST = 13, C_QD = 14, C_LNQ = 15 };

template <int I> struct IC { static constexpr int value = I; };
template <int N, int I = 0, class F> DI void static_for(F&& f) { if constexpr (I < N) { f(IC<I>{}); static_for<N, I + 1>(f); } }
DI unsigned pk2(float lo, float hi) { f32x2 v = {lo, hi}; return __builtin_bit_cast(unsigned, __builtin_convertvector(v, bf2)); }
DI float bflo(unsigned u) { return __uint_as_float(u << 16); }
DI float bfhi(unsigned u) { return __uint_as_float(u & 0xffff0000u); }
DI float rbf(float x) { return bflo(pk2(x, 0.f)); }
DI float dot2(unsigned a, unsigned b, float c) { return __builtin_amdgcn_fdot2_f32_bf16(__builtin_bit_cast(bf2, a), __builtin_bit_cast(bf2, b), c, false); }
DI float half_sum(float v) { auto r = __builtin_amdgcn_permlane32_swap(__float_as_uint(v), __float_as_uint(v), false, false); return __uint_as_float(r[0]) + __uint_as_float(r[1]); }
DI float half_max(float v) { auto r = __builtin_amdgcn_permlane32_swap(__float_as_uint(v), __float_as_uint(v), false, false); return __builtin_fmaxf(__uint_as_float(r[0]), __uint_as_float(r[1])); }
template <int C> DI float dpp_add(float v) { return v + __uint_as_float(__builtin_amdgcn_update_dpp(0u, __float_as_uint(v), C, 0xf, 0xf, false)); }
DI float sum32(float v) {
  v = dpp_add<0x128>(v); v = dpp_add<0x124>(v); v = dpp_add<0x122>(v); v = dpp_add<0x121>(v);
  auto r = __builtin_amdgcn_permlane16_swap(__float_as_uint(v), __float_as_uint(v), false, false);
  return __uint_as_float(r[0]) + __uint_as_float(r[1]);
}
DI float max3(float a, float b, float c) { return __builtin_fmaxf(__builtin_fmaxf(a, b), c); }
DI v4u pack8(const f32x16& a, int o) { v4u r = {pk2(a[o], a[o + 1]), pk2(a[o + 2], a[o + 3]), pk2(a[o + 4], a[o + 5]), pk2(a[o + 6], a[o + 7])}; return r; }
DI v4u lds_r128(lptr p) { return *(const LAS v4u*)p; }
DI f32x4 lds_r128f(lptr p) { return *(const LAS f32x4*)p; }
DI void lds_w128(lptr p, v4u v) { *(LAS v4u*)p = v; }
DI f32x16 splat16(float x) { f32x16 z;
#pragma unroll
  for (int i = 0; i < 16; ++i) z[i] = x;
  return z; }
DI v4u wfrag(lptr p, int off) {
  const s16x4 a = __builtin_amdgcn_ds_read_tr16_b64_v4i16((LAS s16x4*)(p + off));
  const s16x4 b = __builtin_amdgcn_ds_read_tr16_b64_v4i16((LAS s16x4*)(p + off + 512));
  const v2u x = __builtin_bit_cast(v2u, a), y = __builtin_bit_cast(v2u, b);
  v4u r = {x[0], x[1], y[0], y[1]};
  return r;
}
DI void glds16(const void* base, unsigned voff, unsigned lds_dst) {
  unsigned keep;
  asm volatile("s_mov_b32 %0, m0\n\ts_mov_b32 m0, %3\n\ts_nop 0\n\tglobal_load_lds_dwordx4 %1, %2\n\ts_mov_b32 m0, %0" : "=&s"(keep) : "v"(voff), "s"(base), "s"(lds_dst) : "memory");
}
DI void dma_qkv(const u16* Wj, unsigned voff, unsigned L0, int n) { glds16(Wj + (size_t)(n & 31) * (16 * 1536) + (n >> 5) * 512, voff, L0 + n * 1024); }
template <int PACE> DI void dma_tiled(const unsigned char* wt, int src, unsigned dst, int n4, int l16) {
#pragma unroll 1
  for (int n = 0; n < n4; ++n) {
    unsigned keep;
    asm volatile("s_mov_b32 %0, m0\n\ts_mov_b32 m0, %3\n\ts_nop 0\n\tglobal_load_lds_dwordx4 %1, %2 sc1\n\tglobal_load_lds_dwordx4 %1, %2 offset:1024 sc1\n\t"
                 "global_load_lds_dwordx4 %1, %2 offset:2048 sc1\n\tglobal_load_lds_dwordx4 %1, %2 offset:3072 sc1\n\ts_mov_b32 m0, %0"
                 : "=&s"(keep) : "v"(l16), "s"(wt + src * 1024 + n * 4096), "s"(dst + n * 4096) : "memory");
    if (PACE) __builtin_amdgcn_s_sleep(PACE);
  }
}
template <int PACE> DI void dma_rows(const u16* Wj, int ld, int p0, unsigned dst, int n4, int lane, int rot = 0) {
  const unsigned v0 = ((lane >> 2) * ld + (lane & 3) * 8) * 2, st = 32 * ld - 1024;
  const unsigned v1 = v0 + st, v2 = v1 + st, v3 = v2 + st;
#pragma unroll 1
  for (int i = 0; i < n4; ++i) {
    const int n = (i + rot) & (n4 - 1 | -(rot == 0));
    const int p = p0 + 4 * n;
    unsigned keep;
    asm volatile("s_mov_b32 %0, m0\n\ts_mov_b32 m0, %6\n\ts_nop 0\n\tglobal_load_lds_dwordx4 %1, %5\n\tglobal_load_lds_dwordx4 %2, %5 offset:1024\n\t"
                 "global_load_lds_dwordx4 %3, %5 offset:2048\n\tglobal_load_lds_dwordx4 %4, %5 offset:3072\n\ts_mov_b32 m0, %0"
                 : "=&s"(keep) : "v"(v0), "v"(v1), "v"(v2), "v"(v3), "s"(Wj + (size_t)(p & 31) * 16 * ld + (p >> 5) * 512), "s"(dst + n * 4096) : "memory");
    if (PACE) __builtin_amdgcn_s_sleep(PACE);
  }
}
#define TLBPF 0
#define FLAGPF 1
#define CODEPF 40700
#define PWAIT C_P
#define ROT 1
#define RETILE 1
#define DMA_W(PACE, T, W, ld, p0, dst, n4) dma_tiled<PACE>(wt, (T) + (p0), dst, n4, l16)
template <int N> DI void nap() { __builtin_amdgcn_s_sleep(N); }
DI void ctr_arrive(lptr L, int c, int lane) {
  unsigned a, one;
  asm volatile("v_mov_b32 %0, %2\n\tv_mov_b32 %1, 1\n\ts_mov_b64 exec, 1\n\tds_add_u32 %0, %1\n\ts_mov_b64 exec, -1"
               : "=&v"(a), "=&v"(one) : "s"((unsigned)(uintptr_t)L + L_CTR + 4 * c) : "memory");
}
DI void ctr_wait(lptr L, int c, unsigned target) {
  unsigned spins = 0;
  while (__builtin_amdgcn_readfirstlane(__hip_atomic_load((LAS unsigned*)(L + L_CTR + 4 * c), RLX_WG)) < target && ++spins < (1u << 22)) __builtin_amdgcn_s_sleep(1);
  asm volatile("" ::: "memory");
}
struct Probes { unsigned a, b, c; };
DI void retire(const Probes& q) { asm volatile("" :: "v"(q.a), "v"(q.b), "v"(q.c)); }
DI Probes poll(unsigned char* ws, int line_off, int lane, int n, unsigned tag, unsigned zero) {
  unsigned v0 = zero, v1 = zero, v2 = zero;
  if (lane < n) {
    gu32* p = (gu32*)(ws + line_off) + lane;
#define PROBE() __hip_atomic_fetch_add(p, zero, RLX_AGENT)
#define SEEN(v) (__builtin_amdgcn_ballot_w64((v) != tag) == 0ull)
    __builtin_amdgcn_s_sleep(PS0);
    v0 = PROBE();
    __builtin_amdgcn_s_sleep(PSL);
    v1 = PROBE();
    __builtin_amdgcn_s_sleep(PSL);
    v2 = PROBE();
    if (!SEEN(v0)) { v0 = PROBE();
      if (!SEEN(v1)) { v1 = PROBE();
        if (!SEEN(v2)) { v2 = PROBE();
          if (!SEEN(v0)) { v0 = PROBE();
            if (!SEEN(v1)) { v1 = PROBE();
              if (!SEEN(v2)) { v2 = PROBE();
#pragma unroll 4
                for (unsigned spins = 0; spins < (1u << 16); ++spins) {
                  if (SEEN(v0)) break;
                  v0 = PROBE(); __builtin_amdgcn_s_sleep(PSL);
                  if (SEEN(v1)) break;
                  v1 = PROBE(); __builtin_amdgcn_s_sleep(PSL);
                  if (SEEN(v2)) break;
                  v2 = PROBE(); __builtin_amdgcn_s_sleep(PSL);
                } } } } } } }
#undef PROBE
#undef SEEN
  }
  __builtin_amdgcn_fence(__ATOMIC_ACQUIRE, "wavefront");
  asm volatile("" ::: "memory");
  return Probes{v0, v1, v2};
}
DI void publish(unsigned char* ws, int word_off, bool active, unsigned tag) {
  DRAIN();
  if (active) (void)__hip_atomic_exchange((gu32*)(ws + word_off), tag, RLX_AGENT);
}
DI void publish_wg(lptr L, int c, unsigned target, int lane, unsigned char* ws, int word_off, bool active, unsigned tag) {
  DRAIN();
  unsigned old = 0;
  if (lane == 0) old = __hip_atomic_fetch_add((LAS unsigned*)(L + L_CTR + 4 * c), 1u, RLX_WG);
  if (__builtin_amdgcn_readfirstlane(old) + 1u == target && active) (void)__hip_atomic_exchange((gu32*)(ws + word_off), tag, RLX_AGENT);
}
DI v4u ld_sc1(__amdgpu_buffer_rsrc_t rs, int voff, int soff) { return __builtin_amdgcn_raw_buffer_load_b128(rs, voff, soff, SC1); }
DI void st_sc1(__amdgpu_buffer_rsrc_t rs, v4u v, int voff, int soff) { __builtin_amdgcn_raw_buffer_store_b128(v, rs, voff + soff, 0, SC1); }

DI void blank(__amdgpu_buffer_rsrc_t rs, int voff, int soff) {
  unsigned o; asm volatile("v_mov_b32 %0, -1" : "=v"(o));
  st_sc1(rs, v4u{o, o, o, o}, voff, soff);
}
DI unsigned umax3(unsigned a, unsigned b, unsigned c) { return __builtin_elementwise_max(__builtin_elementwise_max(a, b), c); }
DI unsigned chk4(unsigned m, v4u v) { return umax3(umax3(m, v[0], v[1]), v[2], v[3]); }
DI bool again(bool bad, unsigned& tries) { if (!__any(bad) || ++tries > 8192u) return false; if (tries > 16u) __builtin_amdgcn_s_sleep(32); else __builtin_amdgcn_s_sleep(4); asm volatile("" ::: "memory"); return true; }
DI bool again(unsigned m, unsigned& tries) { return again(m == 0xffffffffu, tries); }
DI bool isnan_(float v) { return __builtin_isnan(v); }
DI float add1(float a, float b) { float r; asm("v_add_f32 %0, %1, %2" : "=v"(r) : "v"(a), "v"(b)); return r; }
DI gu32* slot_of(unsigned char* ws, int word_off) { gu32* p = (gu32*)(ws + word_off); PIN(p); return p; }
DI void announce(gu32* p, bool active, unsigned tag) { if (active) (void)__hip_atomic_exchange(p, tag, RLX_AGENT); }

DI void row_stats(float s1, float s2, float& mu, float& rs) {
  const float m = s1 * (1.0f / 512.0f);
  const float var = __builtin_fmaxf(__builtin_fmaf(-m, m, s2 * (1.0f / 512.0f)), 0.f);
  mu = rbf(m);
  rs = __builtin_amdgcn_rcpf(rbf(__builtin_amdgcn_sqrtf(rbf(rbf(var) + 1e-5f))));
}
DI v4u ln_frag(v4u x, float mu, float rs) {
  v4u r;
#pragma unroll
  for (int i = 0; i < 4; ++i) { const unsigned d = pk2(bflo(x[i]) - mu, bfhi(x[i]) - mu); r[i] = pk2(bflo(d) * rs, bfhi(d) * rs); }
  return r;
}
DI void scale_tok(f32x16& a, float rs) {
#pragma unroll
  for (int i = 0; i < 16; ++i) a[i] *= rs;
}
DI void scale_feat(f32x16& a, const f32x4 (&u)[4]) {
#pragma unroll
  for (int i = 0; i < 16; ++i) a[i] *= u[i >> 2][i & 3];
}
DI unsigned phi2(float a0, float a1) {
    const unsigned t = pk2(a0, a1);
    const f32x2 v = {bflo(t), bfhi(t)}, u = v * 1.4426950408889634f;
    const f32x2 e = {__builtin_amdgcn_fmed3f(__builtin_amdgcn_exp2f(u[0]), 0.f, 1.f), __builtin_amdgcn_fmed3f(__builtin_amdgcn_exp2f(u[1]), 0.f, 1.f)};
    float p0, p1; asm("v_max_f32 %0, 0, %1" : "=v"(p0) : "v"(v[0])); asm("v_max_f32 %0, 0, %1" : "=v"(p1) : "v"(v[1]));
    const f32x2 y = (f32x2{p0, p1} + e) + 1e-6f;
    return pk2(y[0], y[1]);
}
DI v4u phi_pack8(const f32x16& a, int o) { return v4u{phi2(a[o], a[o + 1]), phi2(a[o + 2], a[o + 3]), phi2(a[o + 4], a[o + 5]), phi2(a[o + 6], a[o + 7])}; }
#define PEARLY -1
#define OBL 0
#define B4S 32
#define B1S 8
#define OBD -1
#define B3N 6
#define WOD 7
#define EARLY3 1
#define EARLY4 1
#define EARLY1 1
#define OPRIO 3
#define APRIO 0
#define NEARLY 4
#define PACE2 0
#define NB1 3
#define NB2 15
#define PACE1 0
#define NA1 6
#define NA0 6
#define PFX 14
#define PF0X 8
constexpr int PF = PFX, PF0 = PF0X;
DI v4u ld_frag(__amdgpu_buffer_rsrc_t rs, int l16, int base, int s) { return ld_sc1(rs, l16 + (s & 3) * 1024, base + (s >> 2) * 4096); }
DI f32x16 gemm_out(lptr L, lptr p, __amdgpu_buffer_rsrc_t rs, int l16, int base, unsigned wo_target, bool tell, bool bar) {
  constexpr int D = 5, P = 16;
  v4u wr[D + 1], xr[P + 1];
#pragma unroll
  for (int s = 0; s < P; ++s) xr[s] = ld_frag(rs, l16, base, s);
  SB();
  ctr_wait(L, C_WO, wo_target);
#pragma unroll
  for (int s = 0; s < D; ++s) wr[s] = wfrag(p, s * 1024);
  SB();
  f32x16 a = splat16(0.f);
#pragma unroll
  for (int s = 0; s < 32; ++s) {
    if (s + P < 32) xr[(s + P) % (P + 1)] = ld_frag(rs, l16, base, s + P);
    if (s + P == 32 && tell) ctr_arrive(L, C_OP, 0);
    if (s + D < 32) wr[(s + D) % (D + 1)] = wfrag(p, (s + D) * 1024);
    a = MFMA(wr[s % (D + 1)], xr[s % (P + 1)], a);
    SB();
    if (s == B4S && bar) { asm volatile("s_barrier" ::: "memory"); SB(); }
  }
  return a;
}
DI float colsum(lptr p) {
  constexpr int D = 5;
  v4u wr[D + 1];
  const v4u ones = {ONE2, ONE2, ONE2, ONE2};
#pragma unroll
  for (int s = 0; s < D; ++s) wr[s] = wfrag(p, s * 1024);
  SB();
  f32x16 a = splat16(0.f);
#pragma unroll
  for (int s = 0; s < 32; ++s) {
    if (s + D < 32) wr[(s + D) % (D + 1)] = wfrag(p, (s + D) * 1024);
    a = MFMA(ones, wr[s % (D + 1)], a);
    SB();
  }
  return a[0];
}
DI float x16_sum(float v) { auto r = __builtin_amdgcn_permlane16_swap(__float_as_uint(v), __float_as_uint(v), false, false); return __uint_as_float(r[0]) + __uint_as_float(r[1]); }
DI v2u split3(float c) { const float hi = rbf(c), mid = rbf(c - hi), lo = (c - hi) - mid; return v2u{pk2(hi, mid), pk2(lo, 0.f)}; }
DI v4u wide(v2u v) { return v4u{v[0], v[1], 0u, 0u}; }
constexpr int KS = 24;
template <bool ISB, class F> DI void gemm_kv(v4u (&xf)[32], lptr L, lptr pq, lptr pv, f32x16& aK, f32x16& aV, __amdgpu_buffer_rsrc_t rs, int lane, int base,
                                    unsigned& nm, v2u& eq, float& rstd, f32x4 (&rs16)[4], unsigned cs_target, unsigned st_target, int t, bool tell, const Probes& pb, F&& kpart) {
  constexpr int D = 3;
  const int l16 = lane * 16, r = lane & 31, h = lane >> 5;
  v4u wk[D + 1], wv[D + 1];
#pragma unroll
  for (int s = 0; s < PF0; ++s) xf[s] = ld_frag(rs, l16, base, s);
  SB();
  const lptr rp = L + L_RS + (t & 1) * 128 + (t >> 1) * 512, np = L + L_NM + t * 1024, cs = L + L_WX + (ISB ? 384 : 0) + r * 4;
  v2u ek = split3(*(const LAS float*)(cs + 128)), ev = split3(*(const LAS float*)(cs + 256));
  eq = split3(*(const LAS float*)cs);
  PIN(ek); PIN(ev); PIN(eq);
#pragma unroll
  for (int s = 0; s < D; ++s) { wk[s] = wfrag(pq, L_WK + s * 1024); wv[s] = wfrag(pv, s * 1024); }
  SB();
  aK = splat16(0.f); aV = splat16(0.f);
  unsigned cv = 0;
#define MK(s) aK = ISB ? MFMA(xf[s], wk[(s) % (D + 1)], aK) : MFMA(wk[(s) % (D + 1)], xf[s], aK)
  static_for<KS>([&](auto SI) __attribute__((always_inline)) {
    constexpr int s = decltype(SI)::value;
    wk[(s + D) % (D + 1)] = wfrag(pq, L_WK + (s + D) * 1024);
    if constexpr (s < PF - PF0) xf[PF0 + 2 * s] = ld_frag(rs, l16, base, PF0 + 2 * s);
    if constexpr (s == KS - 3) cv = __hip_atomic_load((LAS unsigned*)(L + L_CTR + 4 * C_ST), RLX_WG);
    MK(s);
    SB();
    wv[(s + D) % (D + 1)] = wfrag(pv, (s + D) * 1024);
    if constexpr (s < PF - PF0) xf[PF0 + 2 * s + 1] = ld_frag(rs, l16, base, PF0 + 2 * s + 1);
    else if constexpr (s + PF < 32) xf[s + PF] = ld_frag(rs, l16, base, s + PF);
    if constexpr (s + PF == 32) { if (tell) ctr_arrive(L, C_XI, lane); }
    aV = MFMA(xf[s], wv[s % (D + 1)], aV);
    if constexpr (s == 0) retire(pb);
    SB();
  });
  static_for<32 - KS>([&](auto SI) __attribute__((always_inline)) {
    constexpr int s = KS + decltype(SI)::value;
    if constexpr (s + D < 32) wk[(s + D) % (D + 1)] = wfrag(pq, L_WK + (s + D) * 1024);
    if constexpr (s == 27) {
      if (__builtin_amdgcn_readfirstlane(cv) < st_target) ctr_wait(L, C_ST, st_target);
      asm volatile("" ::: "memory");
      nm = *(const LAS unsigned*)(np + r * 4); rstd = *(const LAS float*)(rp + r * 4);
    }
    if constexpr (s == 29) { rs16[0] = lds_r128f(rp + h * 16); rs16[1] = lds_r128f(rp + h * 16 + 32); }
    if constexpr (s == 30) { rs16[2] = lds_r128f(rp + h * 16 + 64); rs16[3] = lds_r128f(rp + h * 16 + 96); }
    if constexpr (s == 31) { nm = h ? 0u : nm; const v4u xe = {nm, nm & 0xffffu, 0u, 0u}; aK = ISB ? MFMA(xe, wide(ek), aK) : MFMA(wide(ek), xe, aK); }
    MK(s);
    SB();
  });
#undef MK
  static_for<32 - KS>([&](auto SI) __attribute__((always_inline)) {
    constexpr int i = decltype(SI)::value, s = KS + i;
    if constexpr (s + D < 32) wv[(s + D) % (D + 1)] = wfrag(pv, (s + D) * 1024);
    if constexpr (i == 0) { const v4u xe = {nm, nm & 0xffffu, 0u, 0u}; aV = MFMA(xe, wide(ev), aV); }
    aV = MFMA(xf[s], wv[s % (D + 1)], aV);
    kpart(SI);
    SB();
  });
}
DI f32x16 gemm_q(const v4u (&xf)[32], lptr p, unsigned nm, v2u eq) {
  constexpr int D = 5;
  v4u wr[D + 1];
#pragma unroll
  for (int s = 0; s < D; ++s) wr[s] = wfrag(p, s * 1024);
  const v4u xe = {nm, nm & 0xffffu, 0u, 0u};
  f32x16 a = MFMA(wide(eq), xe, splat16(0.f));
  SB();
#pragma unroll
  for (int s = 0; s < 32; ++s) {
    if (s + D < 32) wr[(s + D) % (D + 1)] = wfrag(p, (s + D) * 1024);
    a = MFMA(wr[s % (D + 1)], xf[s], a);
    SB();
  }
  return a;
}

constexpr unsigned long long diag_mask(int i) { return ((unsigned long long)(0xffffffffu << ((i & 3) + 8 * (i >> 2) + 4)) << 32) | (0xffffffffu << ((i & 3) + 8 * (i >> 2))); }
template <bool EDGE>
DI f32x16 attention(lptr L, unsigned v_target, bool late, lptr kp, lptr vp, int vs, lptr mp, v4u qf, int nv) {
  v4u kf[9];
  kf[8] = lds_r128(kp + 8 * 1024);
#pragma unroll
  for (int n = 0; n < 8; ++n) kf[n] = lds_r128(kp + n * 1024);
  SB();
  f32x16 s[9];
  float m0, m1, m2, m3;
#define SC(n) s[n] = MFMA(kf[n], qf, splat16(EDGE ? ((n) < nv ? -3.0e38f : 0.f) : 0.f))
#define MX(n) do { m0 = max3(m0, s[n][0], s[n][1]); m1 = max3(m1, s[n][2], s[n][3]); m2 = max3(m2, s[n][4], s[n][5]); m3 = max3(m3, s[n][6], s[n][7]); \
    m0 = max3(m0, s[n][8], s[n][9]); m1 = max3(m1, s[n][10], s[n][11]); m2 = max3(m2, s[n][12], s[n][13]); m3 = max3(m3, s[n][14], s[n][15]); } while (0)
#define MG(i) s[0][i] = __builtin_amdgcn_inverse_ballot_w64(diag_mask(i)) ? s[8][i] : s[0][i]
  s[8] = MFMA(kf[8], qf, splat16(0.f)); SC(0); SC(1); SB();
  SC(2); MG(0); MG(1); MG(2); MG(3); MG(4); MG(5); MG(6); MG(7); SB();
  SC(3); MG(8); MG(9); MG(10); MG(11); MG(12); MG(13); MG(14); MG(15); SB();
  SC(4); m0 = max3(s[1][0], s[1][1], s[1][2]); m1 = max3(s[1][3], s[1][4], s[1][5]); m2 = max3(s[1][6], s[1][7], s[1][8]); m3 = max3(s[1][9], s[1][10], s[1][11]);
  m0 = max3(m0, s[1][12], s[1][13]); m1 = max3(m1, s[1][14], s[1][15]); SB();
  SC(5); MX(0); SB();
  SC(6); MX(2); SB();
  SC(7); MX(3); SB();
  const unsigned cv = __hip_atomic_load((LAS unsigned*)(L + L_CTR + 4 * C_KV), RLX_WG);
  const v4u mlo = lds_r128(mp), mhi = lds_r128(mp + 1024);
  const v4u da = lds_r128(vp + 8 * vs), db = lds_r128(vp + 8 * vs + 512);
  MX(4); MX(5); PIN(m0); PIN(m1); PIN(m2); PIN(m3); SB();
  if (__builtin_amdgcn_readfirstlane(cv) < v_target) ctr_wait(L, C_KV, v_target);
  asm volatile("" ::: "memory");
  v4u va = lds_r128(vp), vb = lds_r128(vp + 512);
  MX(6); MX(7);
#undef SC
#undef MX
#undef MG
  constexpr float C = 0.25f * 1.4426950408889634f;
  const float nm = -C * half_max(__builtin_fmaxf(max3(m0, m1, m2), m3));
  f32x2 C2 = {C, C}, nm2 = {nm, nm};
  f32x16 o = splat16(0.f);
#pragma unroll
  for (int n = 0; n < 8; ++n) {
    v4u na, nb;
    if (n < 7) { vp += vs; na = lds_r128(vp); nb = lds_r128(vp + 512); }
    f32x2 e[8];
#pragma unroll
    for (int i = 0; i < 8; ++i) { const f32x2 v = {s[n][2 * i], s[n][2 * i + 1]}; e[i] = __builtin_elementwise_fma(v, C2, nm2); }
#pragma unroll
    for (int i = 0; i < 8; ++i) PIN(e[i]);
    v4u p0, p1;
#pragma unroll
    for (int i = 0; i < 4; ++i) p0[i] = pk2(__builtin_amdgcn_exp2f(e[i][0]), __builtin_amdgcn_exp2f(e[i][1]));
#pragma unroll
    for (int i = 0; i < 4; ++i) p1[i] = pk2(__builtin_amdgcn_exp2f(e[4 + i][0]), __builtin_amdgcn_exp2f(e[4 + i][1]));
    if (n == 0) {
      const v4u d0 = p0 & mlo, d1 = p1 & mhi;
      o = MFMA(da, d0, o); o = MFMA(va, p0 ^ d0, o); o = MFMA(db, d1, o); o = MFMA(vb, p1 ^ d1, o);
    } else { o = MFMA(va, p0, o); o = MFMA(vb, p1, o); }
    if (n < 7) { va = na; vb = nb; }
    SB();
    if (n == B3N && late) { BAR(); __builtin_amdgcn_s_setprio(3); }
  }
  return o;
}

__global__ void __launch_bounds__(512, 2) mega(const u16* __restrict__ x_in, const u16* __restrict__ Aw, const u16* __restrict__ Ao,
                                               const u16* __restrict__ Bw, const u16* __restrict__ Bo, u16* __restrict__ out, unsigned char* ws) {
  extern __shared__ __attribute__((aligned(16))) char smem[];
  const lptr L = (lptr)smem;
  const unsigned L0 = (unsigned)(uintptr_t)L;
  int b = blockIdx.x, lane = threadIdx.x & 63, w = __builtin_amdgcn_readfirstlane(threadIdx.x >> 6);
  const __amdgpu_buffer_rsrc_t rs = __builtin_amdgcn_make_buffer_rsrc(ws, 0, WS_END, 0x00020000);
  STAMP(63);
  unsigned zero = 0; PIN(zero);
  unsigned tag;
  v4u x0 = {0u, 0u, 0u, 0u}, x1 = x0;

  {
    const int g = MAP_G(b), j = MAP_J(b), t = w & 3, r = lane & 31, h = lane >> 5;
    unsigned epoch = __hip_atomic_load((gu32*)ws, RLX_AGENT);
    if (w < 4) {
      unsigned tlb = 0;
      if (w == 0) { const v4u z = {0u, 0u, 0u, 0u}; lds_w128(L + L_WX + lane * 16, z); }
      BAR();
      unsigned warm = 0;
      unsigned warm0 = 0;
      if (w == 1 && lane < 48) warm = *(const GAS unsigned*)((const char*)Aw + (b * 48 + lane) * 128);
      if (w == 2 && lane < 48) warm = *(const GAS unsigned*)((const char*)Bw + (b * 48 + lane) * 128);
      if (w == 3 && lane < 32) warm = *(const GAS unsigned*)((const char*)(lane < 16 ? Ao : Bo) + (b * 16 + (lane & 15)) * 128);
      if (w == 3 && lane >= 32 && lane < 45) {
        const int i = lane - 32;
        const int o = i < 4 ? WS_FX + i * 256 * FL : i < 8 ? WS_FO + (i - 4) * 256 * FL : i < 10 ? WS_FH + (i - 8) * 256 * FL : i < 12 ? WS_FP + (i - 10) * 256 * FL : WS_FR;
        (void)__hip_atomic_fetch_add((gu32*)(ws + o + (g * 16 + j) * FL), zero, RLX_AGENT);
      }
      if (w == 0) { unsigned long long pc; asm volatile("s_getpc_b64 %0" : "=s"(pc));
        const int o = ((b >> 3) * 11 + lane) * 128; if (lane < 11 && o < CODEPF) warm = *(const GAS unsigned*)((const char*)(pc & ~127ull) + o); }
      dma_rows<0>(Aw + 32 * j, 1536, 32 + 16 * t, L0 + 32768 + 16384 * t, 4, lane, ROT * (g & 3));
      dma_rows<0>(Aw + 32 * j, 1536, 8 * t, L0 + 8192 * t, 2, lane);
      ctr_arrive(L, C_PR, lane);
      blank(rs, lane * 16, WS_O + (g * 4 + t) * 32768 + 2 * j * 1024);
#pragma unroll
      for (int i = 0; i < 4; ++i) blank(rs, lane * 16 + i * 1024, WS_KV + ((g * 16 + j) * 4 + t) * 4096);
      asm volatile("s_waitcnt vmcnt(13)" :: "v"(warm), "v"(warm0), "v"(tlb) : "memory");
      STAMP(59);
      ctr_arrive(L, C_R, lane); ctr_wait(L, C_R, 5u);
    } else {
      __builtin_amdgcn_s_setprio(3);
      const u16* xrow = x_in + (size_t)(g * 128 + 8 * j + 2 * t + h) * 512 + 8 * r;
      const v4u xa = *(const GAS v4u*)xrow, xb = *(const GAS v4u*)(xrow + 256);
      BAR();
      { const unsigned fill = (lane & 16) ? 0x3f800000u : ONE2; const v4u o = {fill, fill, fill, fill}; if (w == 4) lds_w128(L + L_ONES + lane * 16, o); }
      if (h == 0) *(LAS unsigned*)(L + L_NM + t * 1024 + r * 4) = 0u;
      if (w >= 6) { v4u mk;
#pragma unroll
        for (int i = 0; i < 4; ++i) { const int key = ((2 * i) & 3) + 8 * (i >> 1) + 16 * (w - 6) + 4 * h; mk[i] = (key + 1 <= r) ? 0xffffffffu : ((key <= r) ? 0xffffu : 0u); }
        lds_w128(L + L_MSK + (w - 6) * 1024 + lane * 16, mk); }
      float s1 = 0.f, s2 = 0.f;
#pragma unroll
      for (int i = 0; i < 4; ++i) { s1 = dot2(xa[i], ONE2, s1); s2 = dot2(xa[i], xa[i], s2); s1 = dot2(xb[i], ONE2, s1); s2 = dot2(xb[i], xb[i], s2); }
      float m0, r0; row_stats(sum32(s1), sum32(s2), m0, r0);
      const v4u na = ln_frag(xa, m0, r0), nb = ln_frag(xb, m0, r0);
      { const lptr d = L + L_LN + (r >> 1) * 256 + (2 * t + h) * 16 + (r & 1) * 8;
        v2u u = {na[0], na[1]}; *(LAS v2u*)d = u; u[0] = na[2]; u[1] = na[3]; *(LAS v2u*)(d + 128) = u;
        u[0] = nb[0]; u[1] = nb[1]; *(LAS v2u*)(d + 4096) = u; u[0] = nb[2]; u[1] = nb[3]; *(LAS v2u*)(d + 4096 + 128) = u; }
      STAMP(56);
      ctr_arrive(L, C_ST, lane); ctr_wait(L, C_ST, 4u);
      const int vo = (lane >> 4) * 1024 + (((lane >> 3) & 1) * 32 + 8 * (j & 3) + (lane & 7)) * 16;
      const int xb0 = WS_X + (g * 4 + (j >> 2)) * 32768 + t * 8192;
      st_sc1(rs, lds_r128(L + L_LN + t * 2048 + lane * 16), vo, xb0);
      st_sc1(rs, lds_r128(L + L_LN + t * 2048 + 1024 + lane * 16), vo, xb0 + 4096);
      PIN(epoch);
      publish_wg(L, C_PX, 4u, lane, ws, WS_FX + (g * 16 + lane) * FL + j * 4, lane < 16, epoch + 1u);
      ctr_arrive(L, C_P, lane);
      STAMP(57);
      if (t == 3) { (void)poll(ws, WS_FX + (g * 16 + j) * FL, lane, 16, __builtin_amdgcn_readfirstlane(epoch) + 1u, zero); ctr_arrive(L, C_R, lane); STAMP(59); }
      __builtin_amdgcn_s_setprio(0);
      ctr_wait(L, C_PR, 4u);
      v4u rt[4];
#pragma unroll
      for (int i = 0; i < 4; ++i) {
        const bool po = (i >= 2) && (t & 1);
        const u16* src = po ? (t == 1 ? Ao : Bo) + (32 * g + 16 * (i & 1)) * 512
                            : (t < 2 ? Bw : Aw) + (32 * g + 16 * (i & 1)) * 1536 + (2 * (t & 1) + (i >> 1)) * 512;
        rt[i] = *(const GAS v4u*)(src + 32 * j + (lane >> 2) * (po ? 512 : 1536) + (lane & 3) * 8);
      }
      STAMP(58);
      ctr_wait(L, C_XI, 4u);
#pragma unroll
      for (int i = 0; i < 4; ++i) {
        const bool po = (i >= 2) && (t & 1);
        const int piece = (po ? (t == 1 ? T_OA : T_OB) : (t < 2 ? T_WB : T_WA) + 32 * (2 * (t & 1) + (i >> 1))) + 2 * g + (i & 1);
        st_sc1(rs, rt[i], lane * 16, WS_WT + (j * 256 + piece) * 1024);
      }
    }
    PIN(epoch);
    tag = __builtin_amdgcn_readfirstlane(epoch) + 1u;
  }

  for (int pr = 0; pr < 2; ++pr) {
#pragma unroll
    for (int isB = 0; isB < 2; ++isB) {
      const int l = 2 * pr + isB;
      asm volatile("" : "+v"(lane), "+s"(b), "+s"(w));
      const int g = MAP_G(b), j = MAP_J(b);
      const int t = w & 3, hh = w >> 2, r = lane & 31, h = lane >> 5;
      const bool isQ = w < 4;
      const int gt = g * 4 + t, gj = g * 16 + j;
      const int trl = ((lane & 15) >> 2) * 64 + ((lane >> 4) & 1) * 32 + (lane & 3) * 8 + h * 256;
      const int l16 = lane * 16;
      const unsigned char* wt = ws + WS_WT + j * (256 * 1024);
      v4u qf;
      gu32* a3 = nullptr;
      int mo_ = WS_O + l * MB2 + gt * 32768 + (2 * j + hh) * 1024 + l16;
      lptr ma0 = L + L_PR + (r < 16 ? (hh * 16 + r + 32 * h) * 16 : 2048 + (hh * 16 + 4 * h) * 4), ma1 = ma0 + (r < 16 ? 1024 : 32);
      if (isQ) {
        const lptr pq = L + trl;
        lptr pv = pq + L_WV; PIN(pv);
        int xb = WS_X + l * MB2 + gt * 32768; PINS(xb);
        STAMP(l * 14 + 1);
        Probes pb_ = poll(ws, WS_FX + (l * 256 + gj) * FL, lane, l > 0 ? 16 : 0, tag, zero);
        STAMP(l * 14 + 2);
        f32x16 aK, aV; float rstd; f32x4 rs16[4]; v4u xf[32]; unsigned xe; v2u eq;
        unsigned tries = 0;
        v4u k0, k1, vf0, vf1; f32x16 d; float z;
        do {
          if (!isB) gemm_kv<false>(xf, L, pq, pv, aK, aV, rs, lane, xb, xe, eq, rstd, rs16, 0u, 4u * l + 4u, t, tries == 0, pb_,
            [&](auto SI) __attribute__((always_inline)) { constexpr int i = decltype(SI)::value;
              if constexpr (i == B1S) { if (tries == 0) { asm volatile("s_waitcnt vmcnt(0)\n\ts_barrier" ::: "memory"); } }
              if constexpr (i >= 2 && i < 6) { constexpr int c = 4 * (i - 2); unsigned u = pk2(aK[c] * rstd, aK[c + 1] * rstd), v = pk2(aK[c + 2] * rstd, aK[c + 3] * rstd); PIN(u); PIN(v);
                if constexpr (i < 4) { k0[2 * i - 4] = u; k0[2 * i - 3] = v; } else { k1[2 * i - 8] = u; k1[2 * i - 7] = v; } } });
          else {
            gemm_kv<true>(xf, L, pq, pv, aK, aV, rs, lane, xb, xe, eq, rstd, rs16, 3u, 4u * l + 4u, t, tries == 0, pb_,
              [&](auto SI) __attribute__((always_inline)) { constexpr int i = decltype(SI)::value;
                if constexpr (i >= 2 && i < 6) { constexpr int c = 2 * (i - 2); unsigned u = phi2(aK[c] * rs16[c >> 2][c & 3], aK[c + 1] * rs16[c >> 2][(c & 3) + 1]); PIN(u); k0[i - 2] = u; } });
            scale_feat(aV, rs16);
            vf0 = pack8(aV, 0);
#pragma unroll
            for (int i = 8; i < 16; ++i) aK[i] *= rs16[i >> 2][i & 3];
            d = MFMA(k0, vf0, splat16(0.f));
            k1 = phi_pack8(aK, 8); vf1 = pack8(aV, 8);
            d = MFMA(k1, vf1, d);
            float z0 = 0.f, z1 = 0.f;
#pragma unroll
            for (int i = 0; i < 4; ++i) { z0 = dot2(k0[i], ONE2, z0); z1 = dot2(k1[i], ONE2, z1); }
            z = half_sum(z0 + z1); PIN(z);
          }
          pb_ = Probes{0u, 0u, 0u};
        } while (again(isnan_(isB ? d[0] : aK[0]), tries));
        RT(0);
        STAMP(l * 14 + 12);
        if (!isB) {
          DRAIN();
          if (B1S > 7) BAR();
          const int kb = WS_KV + ((pr * 256 + gj) * 4 + t) * 4096;
          if (g < 15) { st_sc1(rs, k0, l16, kb); st_sc1(rs, k1, l16 + 1024, kb); }
          scale_feat(aV, rs16);
          vf0 = pack8(aV, 0); vf1 = pack8(aV, 8);
          if (g < 15) { st_sc1(rs, vf0, l16 + 2048, kb); st_sc1(rs, vf1, l16 + 3072, kb); }
          STAMP(l * 14 + 4);
          lds_w128(L + L_K + (8 + t) * 1024 + l16, k0); lds_w128(L + L_K + (20 + t) * 1024 + l16, k1);
          const lptr vd = L + L_V + ((r >> 4) * 12 + 8 + t) * 1024 + h * 256 + (r & 15) * 16;
          lds_w128(vd, vf0); lds_w128(vd + 512, vf1);
          f32x16 aQ = gemm_q(xf, pq, xe, eq);
          scale_tok(aQ, rstd);
          qf = pack8(aQ, 0); lds_w128(L + L_QF + t * 1024 + l16, pack8(aQ, 8));
          STAMP(l * 14 + 5);
          if (pr == 0 && t < 3) {
            const float c = colsum(pq + t * 32768);
            if (h == 0) *(LAS float*)(L + L_WX + (t * 32 + r) * 4) = c;
          }
        } else {
          const lptr pw = L + L_PW + t * PSTR;
          if (h == 0) *(LAS float*)(pw + 2048 + r * 4) = z;
          if (r < 16) { *(LAS f32x4*)(pw + l16) = f32x4{d[0], d[1], d[2], d[3]}; *(LAS f32x4*)(pw + 1024 + l16) = f32x4{d[4], d[5], d[6], d[7]}; asm volatile("" ::: "memory"); }
          else { *(LAS f32x4*)(pw + l16) = f32x4{d[8], d[9], d[10], d[11]}; *(LAS f32x4*)(pw + 1024 + l16) = f32x4{d[12], d[13], d[14], d[15]}; }
          BAR();
          STAMP(l * 14 + 4);
          f32x16 aQ = gemm_q(xf, pq, xe, eq);
          if (pr == 0) ctr_arrive(L, C_QD, lane);
          scale_tok(aQ, rstd);
          qf = phi_pack8(aQ, 0); lds_w128(L + L_QF + t * 1024 + l16, phi_pack8(aQ, 8));
          STAMP(l * 14 + 5);
          DMA_W(0, T_OB, Bo, 512, 8 * t, L0 + L_WO + 8192 * t, 2);
          if (pr == 0) {
            ctr_wait(L, C_QD, 4u);
            DMA_W(PACE2, T_WA, Aw, 1536, 24 * t, L0 + 24576 * t, NB1);
          }
          PIN(ma0); PIN(ma1);
        }
        PIN(mo_);
        if (isB) a3 = slot_of(ws, WS_FO + (l * 256 + g * 16 + lane) * FL + j * 4);
        BAR();
      } else {
        __builtin_amdgcn_s_setprio(OPRIO);
        if (l > 0) {
          const Probes pb_ = poll(ws, WS_FX + (l * 256 + gj) * FL, lane, 16, tag, zero);
          v4u st[4];
          unsigned tries = 0, m;
          do {
#pragma unroll
            for (int i = 0; i < 4; ++i) st[i] = ld_sc1(rs, l16 + i * 1024, WS_XS + (l * 64 + gt) * 4096);
            m = chk4(chk4(chk4(chk4(0u, st[0]), st[1]), st[2]), st[3]);
          } while (again(m, tries));
          retire(pb_);
          RT(1);
          f32x4 a;
#pragma unroll
          for (int i = 0; i < 4; ++i) a[i] = add1(add1(__uint_as_float(st[0][i]), __uint_as_float(st[1][i])), add1(__uint_as_float(st[2][i]), __uint_as_float(st[3][i])));
          float m0, r0, m1, r1;
          row_stats(half_sum(x16_sum(a[0])), half_sum(x16_sum(a[1])), m0, r0);
          row_stats(half_sum(x16_sum(a[2])), half_sum(x16_sum(a[3])), m1, r1);
          if (lane < 16) { *(LAS v2u*)(L + L_NM + t * 1024 + lane * 8) = v2u{pk2(-m0, -m0), pk2(-m1, -m1)};
            *(LAS f32x2*)(L + L_RS + (t & 1) * 128 + (t >> 1) * 512 + lane * 8) = f32x2{r0, r1}; }
          ctr_arrive(L, C_ST, lane);
        }
        STAMP(l * 14 + 13);
        ctr_wait(L, C_XI, 4u * l + 4u);
        blank(rs, l16, WS_O + l * MB2 + gt * 32768 + (2 * j + 1) * 1024);
        if (l < 3) {
          blank(rs, l16, WS_X + (l + 1) * MB2 + gt * 32768 + j * 2048); blank(rs, l16 + 1024, WS_X + (l + 1) * MB2 + gt * 32768 + j * 2048);
          if (lane < 16) blank(rs, l16, WS_XS + (((l + 1) * 64 + gt) * 16 + j) * 256);
        }
        if (!isB && t == 0) { const int pb = WS_P + ((pr * 16 + j) * 16 + g) * PSTR; blank(rs, l16, pb); blank(rs, l16 + 1024, pb); if (lane < 8) blank(rs, l16 + 2048, pb); }
        gu32* const a1 = slot_of(ws, isB ? WS_FP + (pr * 256 + lane * 16 + j) * FL + g * 4 : WS_FH + (pr * 256 + gj + 16 * (lane + 1)) * FL + lane * 4);
        STAMP(l * 14 + 1);
        BAR();
        STAMP(l * 14 + 4);
        if (!isB) {
          if (t == 0) announce(a1, lane < 2 && g + lane < 15, tag);
          v4u kv[8];
          int kb0 = WS_KV + ((pr * 256 + gj - 16) * 4 + t) * 4096, kb1 = kb0 - 16 * 4 * 4096; PINS(kb0); PINS(kb1);
          const Probes pb_ = poll(ws, WS_FH + (pr * 256 + gj) * FL, lane, g < 2 ? g : 2, tag, zero);
          STAMP(l * 14 + 2);
          const v4u z4 = {0u, 0u, 0u, 0u};
          unsigned tries = 0;
#define LDH(i0) do { if (g > 0) { kv[i0] = ld_sc1(rs, l16 + (i0) * 1024, kb0); kv[i0 + 1] = ld_sc1(rs, l16 + (i0) * 1024 + 1024, kb0); } else { kv[i0] = z4; kv[i0 + 1] = z4; } \
                     if (g > 1) { kv[4 + i0] = ld_sc1(rs, l16 + (i0) * 1024, kb1); kv[5 + i0] = ld_sc1(rs, l16 + (i0) * 1024 + 1024, kb1); } else { kv[4 + i0] = z4; kv[5 + i0] = z4; } } while (0)
#define BADH(i0) chk4(chk4(chk4(chk4(0u, kv[i0]), kv[i0 + 1]), kv[4 + i0]), kv[5 + i0])
          LDH(0); LDH(2);
          SB();
          while (again(BADH(0), tries)) LDH(0);
          retire(pb_);
          lds_w128(L + L_K + (4 + t) * 1024 + l16, kv[0]); lds_w128(L + L_K + (16 + t) * 1024 + l16, kv[1]);
          lds_w128(L + L_K + t * 1024 + l16, kv[4]); lds_w128(L + L_K + (12 + t) * 1024 + l16, kv[5]);
          STAMP(l * 14 + 5);
          BAR();
          while (again(BADH(2), tries)) LDH(2);
          { const lptr vd = L + L_V + ((r >> 4) * 12 + t) * 1024 + h * 256 + (r & 15) * 16;
            lds_w128(vd + 4096, kv[2]); lds_w128(vd + 4096 + 512, kv[3]); lds_w128(vd, kv[6]); lds_w128(vd + 512, kv[7]); }
          ctr_arrive(L, C_KV, lane);
          if (pr == 0) publish_wg(L, C_LNQ, 4u, lane, ws, WS_FR + (lane * 16 + j) * FL + g * 4, lane < 16, tag); else DRAIN();
          RT(2);
          if (pr == 0) {
          const u16* xr = x_in + (size_t)(gt * 32 + r) * 512 + 32 * j + 4 * h;
          const v2u a = *(const GAS v2u*)xr, c = *(const GAS v2u*)(xr + 8), d = *(const GAS v2u*)(xr + 16), e = *(const GAS v2u*)(xr + 24);
          x0 = v4u{a[0], a[1], c[0], c[1]}; x1 = v4u{d[0], d[1], e[0], e[1]};
        }
#undef LDH
#undef BADH
        } else {
          { const int pb = WS_P + ((pr * 16 + j) * 16 + g) * PSTR;
            if (t < 3) {
              const lptr pp = L + L_PW + t * 1024 + l16;
              f32x4 u[4];
#pragma unroll
              for (int q = 0; q < 4; ++q) u[q] = lds_r128f(pp + q * PSTR);
              SB();
              f32x4 sm;
#pragma unroll
              for (int i = 0; i < 4; ++i) sm[i] = add1(add1(u[0][i], u[1][i]), add1(u[2][i], u[3][i]));
              if (t < 2 || lane < 8) st_sc1(rs, __builtin_bit_cast(v4u, sm), l16 + t * 1024, pb);
            } else announce(a1, lane < 16, tag);
            STAMP(l * 14 + 3); }
          int pb = WS_P + ((pr * 16 + j) * 16 + 4 * t) * PSTR; PINS(pb);
          int zo = ((lane >> 3) & 3) * PSTR + 2048 + (lane & 7) * 16; PIN(zo);
          const Probes pb_ = poll(ws, WS_FP + (pr * 256 + gj) * FL + 16 * t, lane, 4, tag, zero);
          STAMP(l * 14 + 2);
          unsigned tries = 0;
          const lptr pw = L + L_PR + t * PSTR;
          float chk;
          do {
            v4u a[8];
#pragma unroll
            for (int q = 0; q < 4; ++q) { a[2 * q] = ld_sc1(rs, l16, pb + q * PSTR); a[2 * q + 1] = ld_sc1(rs, l16 + 1024, pb + q * PSTR); }
            const v4u zz = ld_sc1(rs, zo, pb);
            SB();
            f32x4 u0, u1, z;
#pragma unroll
            for (int i = 0; i < 4; ++i) {
#define F(x) __uint_as_float(x)
              u0[i] = add1(add1(F(a[0][i]), F(a[2][i])), add1(F(a[4][i]), F(a[6][i]))); u1[i] = add1(add1(F(a[1][i]), F(a[3][i])), add1(F(a[5][i]), F(a[7][i])));
              z[i] = x16_sum(dpp_add<0x128>(F(zz[i])));
#undef F
            }
            *(LAS f32x4*)(pw + l16) = u0; *(LAS f32x4*)(pw + 1024 + l16) = u1;
            if (lane < 8) *(LAS f32x4*)(pw + 2048 + l16) = z;
            chk = add1(add1(add1(u0[0], u0[1]), add1(u0[2], u0[3])), add1(add1(u1[0], u1[1]), add1(u1[2], u1[3])));
            chk = add1(chk, add1(add1(z[0], z[1]), add1(z[2], z[3])));
          } while (again(isnan_(chk), tries));
          retire(pb_);
          DRAIN();
          PIN(ma0); PIN(ma1); PIN(mo_);
          STAMP(l * 14 + 5);
          BAR();
        }
        qf = lds_r128(L + L_QF + t * 1024 + l16);
        if (!isB) __builtin_amdgcn_s_setprio(APRIO);
      }

      STAMP(l * 14 + 6);
      f32x16 o;
      if (!isB) {
        const lptr kp = L + L_K + (hh * 12 + t) * 1024 + l16;
        const lptr vp = (r < 16 ? L + L_V + (hh * 12 + t) * 1024 + h * 256 : L + L_ONES) + (r & 15) * 16;
        const int vs = r < 16 ? 1024 : 0;
        if (g >= 2) o = attention<false>(L, 4u * pr + 4u, !isQ, kp, vp, vs, L + L_MSK + l16, qf, 0);
        else o = attention<true>(L, 4u * pr + 4u, !isQ, kp, vp, vs, L + L_MSK + l16, qf, 8 - 4 * g - t);
      } else {
        f32x4 p0[4], p1[4];
#pragma unroll
        for (int q = 0; q < 4; ++q) { p0[q] = lds_r128f(ma0 + q * PSTR); p1[q] = lds_r128f(ma1 + q * PSTR); }
        SB();
        const f32x4 u0 = (p0[0] + p0[1]) + (p0[2] + p0[3]), u1 = (p1[0] + p1[1]) + (p1[2] + p1[3]);
        const v4u af = {pk2(u0[0], u0[1]), pk2(u0[2], u0[3]), pk2(u1[0], u1[1]), pk2(u1[2], u1[3])};
        o = MFMA(af, qf, splat16(0.f));
        o[8] += 1e-6f;
      }
      { const float ri = __builtin_amdgcn_rcpf(o[8]);
#pragma unroll
        for (int i = 0; i < 8; ++i) o[i] *= ri;
        asm volatile("" :: "v"(o[0]));
        STAMP(l * 14 + 7);
        DRAIN();
        if (isB && w == 0) announce(a3, lane < 16, tag);
        st_sc1(rs, pack8(o, 0), mo_, 0);
      }

      if (isQ) {
        const int na = pr == 0 ? NA0 : NA1;
        if (isB) ctr_arrive(L, C_WO, lane);
        if (!isB) {
          if (pr == 0) (void)poll(ws, WS_FR + gj * FL, lane, 16, tag, zero);
          DMA_W(0, T_WB, Bw, 1536, 24 * t, L0 + 24576 * t, na);
        }
        STAMP(l * 14 + 8);
        if (!isB) {
          a3 = slot_of(ws, WS_FO + (l * 256 + g * 16 + lane) * FL + j * 4);
          BAR();
          if (t == 0) announce(a3, lane < 16, tag);
          nap<WOD>();
        }
        if (!isB) {
          DMA_W(0, T_OA, Ao, 512, 8 * t, L0 + L_WO + 8192 * t, 2);
          DRAIN(); ctr_arrive(L, C_WO, lane);
          DMA_W(PACE1, T_WB, Bw, 1536, 24 * t + 4 * na, L0 + 24576 * t + 4096 * na, 6 - na);
        } else if (pr == 0) { nap<3>(); DMA_W(PACE2, T_WA, Aw, 1536, 24 * t + 4 * NB1, L0 + 24576 * t + 4096 * NB1, 6 - NB1); }
        STAMP(l * 14 + 9);
        if (l < 3) {
          DRAIN(); ctr_arrive(L, C_W, lane); ctr_wait(L, C_W, 4u * l + 4u);
          if (l == 0 && t < 3) {
            const float c = colsum(L + trl + t * 32768);
            if (h == 0) *(LAS float*)(L + L_WX + 384 + (t * 32 + r) * 4) = c;
          }
          ctr_wait(L, C_OP, 4u * l + 4u);
          blank(rs, l16, WS_O + (l + 1) * MB2 + gt * 32768 + 2 * j * 1024);
          if (l == 1) {
#pragma unroll
            for (int i = 0; i < 4; ++i) blank(rs, l16 + i * 1024, WS_KV + ((256 + gj) * 4 + t) * 4096);
          }
          gu32* const a4 = slot_of(ws, WS_FX + ((l + 1) * 256 + g * 16 + lane) * FL + j * 4);
          STAMP(l * 14 + 11);
          BAR();
          if (t == 0) announce(a4, lane < 16, tag);
        }
      } else {
        __builtin_amdgcn_s_setprio(OPRIO);
        STAMP(l * 14 + 8);
        lptr po = L + L_WO + trl; PIN(po);
        int ob = WS_O + l * MB2 + gt * 32768; PINS(ob);
        const Probes pb_ = poll(ws, WS_FO + (l * 256 + gj) * FL, lane, 16, tag, zero);
        STAMP(l * 14 + 9);
        f32x16 y; unsigned tries = 0;
        const int xo_ = WS_X + (l + 1) * MB2 + gt * 32768 + j * 2048 + l16;
        do y = gemm_out(L, po, rs, l16, ob, 4u * l + 4u, tries == 0, tries == 0 && l < 3); while (again(isnan_(y[0]), tries));
        retire(pb_);
        RT(isB ? 4 : 3);
        asm volatile("" :: "v"(y[0]));
        STAMP(l * 14 + 10);
        if (B4S > 31 && l < 3) BAR();
        { v4u y0 = pack8(y, 0), y1 = pack8(y, 8);
          PIN(y0); PIN(y1);
          f32x16 a;
#pragma unroll
          for (int i = 0; i < 4; ++i) { a[2 * i] = add1(bflo(x0[i]), bflo(y0[i])); a[2 * i + 1] = add1(bfhi(x0[i]), bfhi(y0[i])); }
          x0 = pack8(a, 0);
          if (l < 3) st_sc1(rs, x0, xo_, 0);
#pragma unroll
          for (int i = 0; i < 4; ++i) { a[8 + 2 * i] = add1(bflo(x1[i]), bflo(y1[i])); a[9 + 2 * i] = add1(bfhi(x1[i]), bfhi(y1[i])); }
          x1 = pack8(a, 8); }
        if (l < 3) {
          st_sc1(rs, x1, xo_ + 1024, 0);
          SB();
          float s1 = 0.f, s2 = 0.f, s3 = 0.f, s4 = 0.f;
#pragma unroll
          for (int i = 0; i < 4; ++i) { s1 = dot2(x0[i], ONE2, s1); s2 = dot2(x0[i], x0[i], s2); s3 = dot2(x1[i], ONE2, s3); s4 = dot2(x1[i], x1[i], s4); }
          s1 = half_sum(s1 + s3); s2 = half_sum(s2 + s4);
          if (h == 0) { v2u v = {__float_as_uint(s1), __float_as_uint(s2)}; __builtin_amdgcn_raw_buffer_store_b64(v, rs, r * 8, WS_XS + (((l + 1) * 64 + gt) * 16 + j) * 256, SC1); }
          STAMP(l * 14 + 11);
        } else {
          const auto p = __builtin_amdgcn_permlane32_swap(x0[0], x0[2], false, false), q = __builtin_amdgcn_permlane32_swap(x0[1], x0[3], false, false);
          const auto u = __builtin_amdgcn_permlane32_swap(x1[0], x1[2], false, false), v = __builtin_amdgcn_permlane32_swap(x1[1], x1[3], false, false);
          u16* op = out + (size_t)(gt * 32 + r) * 512 + 32 * j + 8 * h;
          *(GAS v4u*)op = v4u{p[0], q[0], p[1], q[1]}; *(GAS v4u*)(op + 16) = v4u{u[0], v[0], u[1], v[1]};
          if (b == 0 && w == 4 && lane == 0) __hip_atomic_store((gu32*)ws, tag, RLX_AGENT);
        }
      }
    }
  }
}

extern "C" void kernel_launch(void* const* d_in, const int* in_sizes, int n_in, void* d_out, int out_size, void* d_ws, size_t ws_size,
                              hipStream_t stream) {
  (void)in_sizes; (void)n_in; (void)out_size; (void)ws_size;
  (void)hipFuncSetAttribute((const void*)mega, hipFuncAttributeMaxDynamicSharedMemorySize, LDS_BYTES);
  mega<<<dim3(256), dim3(512), LDS_BYTES, stream>>>((const u16*)d_in[0], (const u16*)d_in[1], (const u16*)d_in[2], (const u16*)d_in[4],
                                                    (const u16*)d_in[5], (u16*)d_out, (unsigned char*)d_ws);
}
```

```cpp
#include <hip/hip_runtime.h>
#include <hip/hip_bf16.h>
#include <stdint.h>

#define DI __device__ __forceinline__
#define LAS __attribute__((address_space(3)))
#define GAS __attribute__((address_space(1)))
typedef unsigned short u16;
typedef unsigned v4u __attribute__((ext_vector_type(4)));
typedef unsigned v2u __attribute__((ext_vector_type(2)));
typedef float f32x16 __attribute__((ext_vector_type(16)));
typedef float f32x4 __attribute__((ext_vector_type(4)));
typedef float f32x2 __attribute__((ext_vector_type(2)));
typedef short bf16x8 __attribute__((ext_vector_type(8)));
typedef short s16x4 __attribute__((ext_vector_type(4)));
typedef __bf16 bf2 __attribute__((ext_vector_type(2)));
typedef GAS unsigned gu32;
typedef LAS char* lptr;
#define RLX_AGENT __ATOMIC_RELAXED, __HIP_MEMORY_SCOPE_AGENT
#define RLX_WG __ATOMIC_RELAXED, __HIP_MEMORY_SCOPE_WORKGROUP
#define MFMA(a, b, c) __builtin_amdgcn_mfma_f32_32x32x16_bf16(__builtin_bit_cast(bf16x8, (a)), __builtin_bit_cast(bf16x8, (b)), (c), 0, 0, 0)
#define SB() __builtin_amdgcn_sched_barrier(0)
#define PIN(x) asm volatile("" : "+v"(x))
#define PINS(x) do { x = __builtin_amdgcn_readfirstlane(x); asm volatile("" : "+s"(x)); } while (0)
#define DRAIN() asm volatile("s_waitcnt vmcnt(0)" ::: "memory")
#define BAR() do { SB(); asm volatile("s_waitcnt lgkmcnt(0)\n\ts_barrier" ::: "memory"); SB(); } while (0)
constexpr int SC1 = 16;
constexpr unsigned ONE2 = 0x3f803f80u;

#define MAP_G(b) (4 * (((b) & 7) >> 1) + ((b) >> 6))
#define MAP_J(b) (8 * ((b) & 1) + (((b) >> 3) & 7))
constexpr int FL = 256;
constexpr int MB2 = 2 << 20;
constexpr int WS_FX = 4096;
constexpr int WS_FO = WS_FX + 4 * 256 * FL;
constexpr int WS_FH = WS_FO + 4 * 256 * FL;
constexpr int WS_FP = WS_FH + 2 * 256 * FL;
constexpr int WS_FR = WS_FP + 2 * 256 * FL;
constexpr int WS_XS = WS_FR + 256 * FL;
constexpr int PSTR = 2304;
constexpr int WS_P = WS_XS + 4 * 1024 * 256;
constexpr int WS_X = 6 << 20;
constexpr int WS_O = WS_X + 4 * MB2;
constexpr int WS_KV = WS_O + 4 * MB2;
constexpr int WS_WT = WS_KV + 4 * MB2;
constexpr int WS_END = WS_WT + 2 * MB2;
constexpr int T_WB = 0, T_WA = 96, T_OA = 192, T_OB = 224;
static_assert(WS_P + 2 * 256 * PSTR <= WS_X, "ws map");

constexpr int L_WQ = 0, L_WK = 32768, L_WV = 65536, L_WO = 98304, L_K = 131072, L_V = L_WO + 8192, L_MISC = 155648;
constexpr int L_QF = L_MISC, L_MSK = L_MISC + 4096, L_ONES = L_MISC + 6144;
constexpr int L_RS = L_ONES + 256;
constexpr int L_WX = L_MISC + 7168, L_CTR = L_WX + 960;
constexpr int L_NM = L_QF;
constexpr int L_LN = L_K;
constexpr int L_PW = L_K, L_PR = L_K + 4 * PSTR;
constexpr int LDS_BYTES = 163840;
enum { C_W, C_KV, C_OP, C_PX, C_PR, C_R, C_WO, C_XI, C_ST, C_QD, C_RT };
constexpr int CODE_AHEAD = 40000;

template <int I> struct IC { static constexpr int value = I; };
template <int N, int I = 0, class F> DI void static_for(F&& f) { if constexpr (I < N) { f(IC<I>{}); static_for<N, I + 1>(f); } }
DI unsigned pk2(float lo, float hi) { f32x2 v = {lo, hi}; return __builtin_bit_cast(unsigned, __builtin_convertvector(v, bf2)); }
DI float bflo(unsigned u) { return __uint_as_float(u << 16); }
DI float bfhi(unsigned u) { return __uint_as_float(u & 0xffff0000u); }
DI float rbf(float x) { return bflo(pk2(x, 0.f)); }
DI float dot2(unsigned a, unsigned b, float c) { return __builtin_amdgcn_fdot2_f32_bf16(__builtin_bit_cast(bf2, a), __builtin_bit_cast(bf2, b), c, false); }
DI float half_sum(float v) { auto r = __builtin_amdgcn_permlane32_swap(__float_as_uint(v), __float_as_uint(v), false, false); return __uint_as_float(r[0]) + __uint_as_float(r[1]); }
DI float half_max(float v) { auto r = __builtin_amdgcn_permlane32_swap(__float_as_uint(v), __float_as_uint(v), false, false); return __builtin_fmaxf(__uint_as_float(r[0]), __uint_as_float(r[1])); }
template <int C> DI float dpp_add(float v) { return v + __uint_as_float(__builtin_amdgcn_update_dpp(0u, __float_as_uint(v), C, 0xf, 0xf, false)); }
DI float sum32(float v) {
  v = dpp_add<0x128>(v); v = dpp_add<0x124>(v); v = dpp_add<0x122>(v); v = dpp_add<0x121>(v);
  auto r = __builtin_amdgcn_permlane16_swap(__float_as_uint(v), __float_as_uint(v), false, false);
  return __uint_as_float(r[0]) + __uint_as_float(r[1]);
}
DI float max3(float a, float b, float c) { return __builtin_fmaxf(__builtin_fmaxf(a, b), c); }
DI v4u pack8(const f32x16& a, int o) { v4u r = {pk2(a[o], a[o + 1]), pk2(a[o + 2], a[o + 3]), pk2(a[o + 4], a[o + 5]), pk2(a[o + 6], a[o + 7])}; return r; }
DI v4u lds_r128(lptr p) { return *(const LAS v4u*)p; }
DI f32x4 lds_r128f(lptr p) { return *(const LAS f32x4*)p; }
DI void lds_w128(lptr p, v4u v) { *(LAS v4u*)p = v; }
DI f32x16 splat16(float x) { f32x16 z;
#pragma unroll
  for (int i = 0; i < 16; ++i) z[i] = x;
  return z; }
DI v4u wfrag(lptr p, int off) {
  const s16x4 a = __builtin_amdgcn_ds_read_tr16_b64_v4i16((LAS s16x4*)(p + off));
  const s16x4 b = __builtin_amdgcn_ds_read_tr16_b64_v4i16((LAS s16x4*)(p + off + 512));
  const v2u x = __builtin_bit_cast(v2u, a), y = __builtin_bit_cast(v2u, b);
  v4u r = {x[0], x[1], y[0], y[1]};
  return r;
}
DI void dma_tiled(const unsigned char* wt, int src, unsigned dst, int n4, int l16) {
#pragma unroll 1
  for (int n = 0; n < n4; ++n) {
    unsigned keep;
    asm volatile("s_mov_b32 %0, m0\n\ts_mov_b32 m0, %3\n\ts_nop 0\n\tglobal_load_lds_dwordx4 %1, %2 sc1\n\tglobal_load_lds_dwordx4 %1, %2 offset:1024 sc1\n\t"
                 "global_load_lds_dwordx4 %1, %2 offset:2048 sc1\n\tglobal_load_lds_dwordx4 %1, %2 offset:3072 sc1\n\ts_mov_b32 m0, %0"
                 : "=&s"(keep) : "v"(l16), "s"(wt + src * 1024 + n * 4096), "s"(dst + n * 4096) : "memory");
  }
}
DI void dma_rows(const u16* Wj, int ld, int p0, unsigned dst, int n4, int lane, int rot = 0) {
  const unsigned v0 = ((lane >> 2) * ld + (lane & 3) * 8) * 2, st = 32 * ld - 1024;
  const unsigned v1 = v0 + st, v2 = v1 + st, v3 = v2 + st;
#pragma unroll 1
  for (int i = 0; i < n4; ++i) {
    const int n = (i + rot) & (n4 - 1 | -(rot == 0));
    const int p = p0 + 4 * n;
    unsigned keep;
    asm volatile("s_mov_b32 %0, m0\n\ts_mov_b32 m0, %6\n\ts_nop 0\n\tglobal_load_lds_dwordx4 %1, %5\n\tglobal_load_lds_dwordx4 %2, %5 offset:1024\n\t"
                 "global_load_lds_dwordx4 %3, %5 offset:2048\n\tglobal_load_lds_dwordx4 %4, %5 offset:3072\n\ts_mov_b32 m0, %0"
                 : "=&s"(keep) : "v"(v0), "v"(v1), "v"(v2), "v"(v3), "s"(Wj + (size_t)(p & 31) * 16 * ld + (p >> 5) * 512), "s"(dst + n * 4096) : "memory");
  }
}
template <int N> DI void nap() { __builtin_amdgcn_s_sleep(N); }
DI void ctr_arrive(lptr L, int c) {
  unsigned a, one;
  asm volatile("v_mov_b32 %0, %2\n\tv_mov_b32 %1, 1\n\ts_mov_b64 exec, 1\n\tds_add_u32 %0, %1\n\ts_mov_b64 exec, -1"
               : "=&v"(a), "=&v"(one) : "s"((unsigned)(uintptr_t)L + L_CTR + 4 * c) : "memory");
}
DI void ctr_wait(lptr L, int c, unsigned target) {
  unsigned spins = 0;
  while (__builtin_amdgcn_readfirstlane(__hip_atomic_load((LAS unsigned*)(L + L_CTR + 4 * c), RLX_WG)) < target && ++spins < (1u << 22)) __builtin_amdgcn_s_sleep(1);
  asm volatile("" ::: "memory");
}
struct Probes { unsigned a, b, c; };
DI void retire(const Probes& q) { asm volatile("" :: "v"(q.a), "v"(q.b), "v"(q.c)); }
DI Probes poll(unsigned char* ws, int line_off, int lane, int n, unsigned tag, unsigned zero) {
  unsigned v0 = zero, v1 = zero, v2 = zero;
  if (lane < n) {
    gu32* p = (gu32*)(ws + line_off) + lane;
#define PROBE() __hip_atomic_fetch_add(p, zero, RLX_AGENT)
#define SEEN(v) (__builtin_amdgcn_ballot_w64((v) != tag) == 0ull)
    v0 = PROBE();
    __builtin_amdgcn_s_sleep(4);
    v1 = PROBE();
    __builtin_amdgcn_s_sleep(4);
    v2 = PROBE();
    if (!SEEN(v0)) { v0 = PROBE();
      if (!SEEN(v1)) { v1 = PROBE();
        if (!SEEN(v2)) { v2 = PROBE();
          if (!SEEN(v0)) { v0 = PROBE();
            if (!SEEN(v1)) { v1 = PROBE();
              if (!SEEN(v2)) { v2 = PROBE();
#pragma unroll 4
                for (unsigned spins = 0; spins < (1u << 16); ++spins) {
                  if (SEEN(v0)) break;
                  v0 = PROBE(); __builtin_amdgcn_s_sleep(4);
                  if (SEEN(v1)) break;
                  v1 = PROBE(); __builtin_amdgcn_s_sleep(4);
                  if (SEEN(v2)) break;
                  v2 = PROBE(); __builtin_amdgcn_s_sleep(4);
                } } } } } } }
#undef PROBE
#undef SEEN
  }
  __builtin_amdgcn_fence(__ATOMIC_ACQUIRE, "wavefront");
  asm volatile("" ::: "memory");
  return Probes{v0, v1, v2};
}
DI void publish_wg(lptr L, int c, unsigned target, int lane, unsigned char* ws, int word_off, bool active, unsigned tag) {
  DRAIN();
  unsigned old = 0;
  if (lane == 0) old = __hip_atomic_fetch_add((LAS unsigned*)(L + L_CTR + 4 * c), 1u, RLX_WG);
  if (__builtin_amdgcn_readfirstlane(old) + 1u == target && active) (void)__hip_atomic_exchange((gu32*)(ws + word_off), tag, RLX_AGENT);
}
DI v4u ld_sc1(__amdgpu_buffer_rsrc_t rs, int voff, int soff) { return __builtin_amdgcn_raw_buffer_load_b128(rs, voff, soff, SC1); }
DI void st_sc1(__amdgpu_buffer_rsrc_t rs, v4u v, int voff, int soff) { __builtin_amdgcn_raw_buffer_store_b128(v, rs, voff + soff, 0, SC1); }

DI void blank(__amdgpu_buffer_rsrc_t rs, int voff, int soff) {
  unsigned o; asm volatile("v_mov_b32 %0, -1" : "=v"(o));
  st_sc1(rs, v4u{o, o, o, o}, voff, soff);
}
DI unsigned umax3(unsigned a, unsigned b, unsigned c) { return __builtin_elementwise_max(__builtin_elementwise_max(a, b), c); }
DI unsigned chk4(unsigned m, v4u v) { return umax3(umax3(m, v[0], v[1]), v[2], v[3]); }
DI bool again(bool bad, unsigned& tries) { if (!__any(bad) || ++tries > 8192u) return false; if (tries > 16u) __builtin_amdgcn_s_sleep(32); else __builtin_amdgcn_s_sleep(4); asm volatile("" ::: "memory"); return true; }
DI bool again(unsigned m, unsigned& tries) { return again(m == 0xffffffffu, tries); }
DI bool isnan_(float v) { return __builtin_isnan(v); }
DI float add1(float a, float b) { float r; asm("v_add_f32 %0, %1, %2" : "=v"(r) : "v"(a), "v"(b)); return r; }
DI gu32* slot_of(unsigned char* ws, int word_off) { gu32* p = (gu32*)(ws + word_off); PIN(p); return p; }
DI void announce(gu32* p, bool active, unsigned tag) { if (active) (void)__hip_atomic_exchange(p, tag, RLX_AGENT); }

DI void row_stats(float s1, float s2, float& mu, float& rs) {
  const float m = s1 * (1.0f / 512.0f);
  const float var = __builtin_fmaxf(__builtin_fmaf(-m, m, s2 * (1.0f / 512.0f)), 0.f);
  mu = rbf(m);
  rs = __builtin_amdgcn_rcpf(rbf(__builtin_amdgcn_sqrtf(rbf(rbf(var) + 1e-5f))));
}
DI v4u ln_frag(v4u x, float mu, float rs) {
  v4u r;
#pragma unroll
  for (int i = 0; i < 4; ++i) { const unsigned d = pk2(bflo(x[i]) - mu, bfhi(x[i]) - mu); r[i] = pk2(bflo(d) * rs, bfhi(d) * rs); }
  return r;
}
DI void scale_tok(f32x16& a, float rs) {
#pragma unroll
  for (int i = 0; i < 16; ++i) a[i] *= rs;
}
DI void scale_feat(f32x16& a, const f32x4 (&u)[4]) {
#pragma unroll
  for (int i = 0; i < 16; ++i) a[i] *= u[i >> 2][i & 3];
}
DI unsigned phi2(float a0, float a1) {
    const unsigned t = pk2(a0, a1);
    const f32x2 v = {bflo(t), bfhi(t)}, u = v * 1.4426950408889634f;
    const f32x2 e = {__builtin_amdgcn_fmed3f(__builtin_amdgcn_exp2f(u[0]), 0.f, 1.f), __builtin_amdgcn_fmed3f(__builtin_amdgcn_exp2f(u[1]), 0.f, 1.f)};
    float p0, p1; asm("v_max_f32 %0, 0, %1" : "=v"(p0) : "v"(v[0])); asm("v_max_f32 %0, 0, %1" : "=v"(p1) : "v"(v[1]));
    const f32x2 y = (f32x2{p0, p1} + e) + 1e-6f;
    return pk2(y[0], y[1]);
}
DI v4u phi_pack8(const f32x16& a, int o) { return v4u{phi2(a[o], a[o + 1]), phi2(a[o + 2], a[o + 3]), phi2(a[o + 4], a[o + 5]), phi2(a[o + 6], a[o + 7])}; }
constexpr int PF = 14, PF0 = 8;
DI v4u ld_frag(__amdgpu_buffer_rsrc_t rs, int l16, int base, int s) { return ld_sc1(rs, l16 + (s & 3) * 1024, base + (s >> 2) * 4096); }
DI f32x16 gemm_out(lptr L, lptr p, __amdgpu_buffer_rsrc_t rs, int l16, int base, unsigned wo_target, bool tell) {
  constexpr int D = 5, P = 16;
  v4u wr[D + 1], xr[P + 1];
#pragma unroll
  for (int s = 0; s < P; ++s) xr[s] = ld_frag(rs, l16, base, s);
  SB();
  ctr_wait(L, C_WO, wo_target);
#pragma unroll
  for (int s = 0; s < D; ++s) wr[s] = wfrag(p, s * 1024);
  SB();
  f32x16 a = splat16(0.f);
#pragma unroll
  for (int s = 0; s < 32; ++s) {
    if (s + P < 32) xr[(s + P) % (P + 1)] = ld_frag(rs, l16, base, s + P);
    if (s + P == 32 && tell) ctr_arrive(L, C_OP);
    if (s + D < 32) wr[(s + D) % (D + 1)] = wfrag(p, (s + D) * 1024);
    a = MFMA(wr[s % (D + 1)], xr[s % (P + 1)], a);
    SB();
  }
  return a;
}
DI float colsum(lptr p) {
  constexpr int D = 5;
  v4u wr[D + 1];
  const v4u ones = {ONE2, ONE2, ONE2, ONE2};
#pragma unroll
  for (int s = 0; s < D; ++s) wr[s] = wfrag(p, s * 1024);
  SB();
  f32x16 a = splat16(0.f);
#pragma unroll
  for (int s = 0; s < 32; ++s) {
    if (s + D < 32) wr[(s + D) % (D + 1)] = wfrag(p, (s + D) * 1024);
    a = MFMA(ones, wr[s % (D + 1)], a);
    SB();
  }
  return a[0];
}
DI float x16_sum(float v) { auto r = __builtin_amdgcn_permlane16_swap(__float_as_uint(v), __float_as_uint(v), false, false); return __uint_as_float(r[0]) + __uint_as_float(r[1]); }
DI v2u split3(float c) { const float hi = rbf(c), mid = rbf(c - hi), lo = (c - hi) - mid; return v2u{pk2(hi, mid), pk2(lo, 0.f)}; }
DI v4u wide(v2u v) { return v4u{v[0], v[1], 0u, 0u}; }
constexpr int KS = 24;
template <bool ISB, class F> DI void gemm_kv(v4u (&xf)[32], lptr L, lptr pq, lptr pv, f32x16& aK, f32x16& aV, __amdgpu_buffer_rsrc_t rs, int lane, int base,
                                    unsigned& nm, v2u& eq, float& rstd, f32x4 (&rs16)[4], unsigned st_target, int t, bool tell, const Probes& pb, F&& kpart) {
  constexpr int D = 3;
  const int l16 = lane * 16, r = lane & 31, h = lane >> 5;
  v4u wk[D + 1], wv[D + 1];
#pragma unroll
  for (int s = 0; s < PF0; ++s) xf[s] = ld_frag(rs, l16, base, s);
  SB();
  const lptr rp = L + L_RS + (t & 1) * 128 + (t >> 1) * 512, np = L + L_NM + t * 1024, cs = L + L_WX + (ISB ? 384 : 0) + r * 4;
  v2u ek = split3(*(const LAS float*)(cs + 128)), ev = split3(*(const LAS float*)(cs + 256));
  eq = split3(*(const LAS float*)cs);
  PIN(ek); PIN(ev); PIN(eq);
#pragma unroll
  for (int s = 0; s < D; ++s) { wk[s] = wfrag(pq, L_WK + s * 1024); wv[s] = wfrag(pv, s * 1024); }
  SB();
  aK = splat16(0.f); aV = splat16(0.f);
  unsigned cv = 0;
#define MK(s) aK = ISB ? MFMA(xf[s], wk[(s) % (D + 1)], aK) : MFMA(wk[(s) % (D + 1)], xf[s], aK)
  static_for<KS>([&](auto SI) __attribute__((always_inline)) {
    constexpr int s = decltype(SI)::value;
    wk[(s + D) % (D + 1)] = wfrag(pq, L_WK + (s + D) * 1024);
    if constexpr (s < PF - PF0) xf[PF0 + 2 * s] = ld_frag(rs, l16, base, PF0 + 2 * s);
    if constexpr (s == KS - 3) cv = __hip_atomic_load((LAS unsigned*)(L + L_CTR + 4 * C_ST), RLX_WG);
    MK(s);
    SB();
    wv[(s + D) % (D + 1)] = wfrag(pv, (s + D) * 1024);
    if constexpr (s < PF - PF0) xf[PF0 + 2 * s + 1] = ld_frag(rs, l16, base, PF0 + 2 * s + 1);
    else if constexpr (s + PF < 32) xf[s + PF] = ld_frag(rs, l16, base, s + PF);
    if constexpr (s + PF == 32) { if (tell) ctr_arrive(L, C_XI); }
    aV = MFMA(xf[s], wv[s % (D + 1)], aV);
    if constexpr (s == 0) retire(pb);
    SB();
  });
  static_for<32 - KS>([&](auto SI) __attribute__((always_inline)) {
    constexpr int s = KS + decltype(SI)::value;
    if constexpr (s + D < 32) wk[(s + D) % (D + 1)] = wfrag(pq, L_WK + (s + D) * 1024);
    if constexpr (s == 27) {
      if (__builtin_amdgcn_readfirstlane(cv) < st_target) ctr_wait(L, C_ST, st_target);
      asm volatile("" ::: "memory");
      nm = *(const LAS unsigned*)(np + r * 4); rstd = *(const LAS float*)(rp + r * 4);
    }
    if constexpr (s == 29) { rs16[0] = lds_r128f(rp + h * 16); rs16[1] = lds_r128f(rp + h * 16 + 32); }
    if constexpr (s == 30) { rs16[2] = lds_r128f(rp + h * 16 + 64); rs16[3] = lds_r128f(rp + h * 16 + 96); }
    if constexpr (s == 31) { nm = h ? 0u : nm; const v4u xe = {nm, nm & 0xffffu, 0u, 0u}; aK = ISB ? MFMA(xe, wide(ek), aK) : MFMA(wide(ek), xe, aK); }
    MK(s);
    SB();
  });
#undef MK
  static_for<32 - KS>([&](auto SI) __attribute__((always_inline)) {
    constexpr int i = decltype(SI)::value, s = KS + i;
    if constexpr (s + D < 32) wv[(s + D) % (D + 1)] = wfrag(pv, (s + D) * 1024);
    if constexpr (i == 0) { const v4u xe = {nm, nm & 0xffffu, 0u, 0u}; aV = MFMA(xe, wide(ev), aV); }
    aV = MFMA(xf[s], wv[s % (D + 1)], aV);
    kpart(SI);
    SB();
  });
}
DI f32x16 gemm_q(const v4u (&xf)[32], lptr p, unsigned nm, v2u eq) {
  constexpr int D = 5;
  v4u wr[D + 1];
#pragma unroll
  for (int s = 0; s < D; ++s) wr[s] = wfrag(p, s * 1024);
  const v4u xe = {nm, nm & 0xffffu, 0u, 0u};
  f32x16 a = MFMA(wide(eq), xe, splat16(0.f));
  SB();
#pragma unroll
  for (int s = 0; s < 32; ++s) {
    if (s + D < 32) wr[(s + D) % (D + 1)] = wfrag(p, (s + D) * 1024);
    a = MFMA(wr[s % (D + 1)], xf[s], a);
    SB();
  }
  return a;
}

constexpr unsigned long long diag_mask(int i) { return ((unsigned long long)(0xffffffffu << ((i & 3) + 8 * (i >> 2) + 4)) << 32) | (0xffffffffu << ((i & 3) + 8 * (i >> 2))); }
template <bool EDGE>
DI f32x16 attention(lptr L, unsigned v_target, bool late, lptr kp, lptr vp, int vs, lptr mp, v4u qf, int nv) {
  v4u kf[9];
  kf[8] = lds_r128(kp + 8 * 1024);
#pragma unroll
  for (int n = 0; n < 8; ++n) kf[n] = lds_r128(kp + n * 1024);
  SB();
  f32x16 s[9];
  float m0, m1, m2, m3;
#define SC(n) s[n] = MFMA(kf[n], qf, splat16(EDGE ? ((n) < nv ? -3.0e38f : 0.f) : 0.f))
#define MX(n) do { m0 = max3(m0, s[n][0], s[n][1]); m1 = max3(m1, s[n][2], s[n][3]); m2 = max3(m2, s[n][4], s[n][5]); m3 = max3(m3, s[n][6], s[n][7]); \
    m0 = max3(m0, s[n][8], s[n][9]); m1 = max3(m1, s[n][10], s[n][11]); m2 = max3(m2, s[n][12], s[n][13]); m3 = max3(m3, s[n][14], s[n][15]); } while (0)
#define MG(i) s[0][i] = __builtin_amdgcn_inverse_ballot_w64(diag_mask(i)) ? s[8][i] : s[0][i]
  s[8] = MFMA(kf[8], qf, splat16(0.f)); SC(0); SC(1); SB();
  SC(2); MG(0); MG(1); MG(2); MG(3); MG(4); MG(5); MG(6); MG(7); SB();
  SC(3); MG(8); MG(9); MG(10); MG(11); MG(12); MG(13); MG(14); MG(15); SB();
  SC(4); m0 = max3(s[1][0], s[1][1], s[1][2]); m1 = max3(s[1][3], s[1][4], s[1][5]); m2 = max3(s[1][6], s[1][7], s[1][8]); m3 = max3(s[1][9], s[1][10], s[1][11]);
  m0 = max3(m0, s[1][12], s[1][13]); m1 = max3(m1, s[1][14], s[1][15]); SB();
  SC(5); MX(0); SB();
  SC(6); MX(2); SB();
  SC(7); MX(3); SB();
  const unsigned cv = __hip_atomic_load((LAS unsigned*)(L + L_CTR + 4 * C_KV), RLX_WG);
  const v4u mlo = lds_r128(mp), mhi = lds_r128(mp + 1024);
  const v4u da = lds_r128(vp + 8 * vs), db = lds_r128(vp + 8 * vs + 512);
  MX(4); MX(5); PIN(m0); PIN(m1); PIN(m2); PIN(m3); SB();
  if (__builtin_amdgcn_readfirstlane(cv) < v_target) ctr_wait(L, C_KV, v_target);
  asm volatile("" ::: "memory");
  v4u va = lds_r128(vp), vb = lds_r128(vp + 512);
  MX(6); MX(7);
#undef SC
#undef MX
#undef MG
  constexpr float C = 0.25f * 1.4426950408889634f;
  const float nm = -C * half_max(__builtin_fmaxf(max3(m0, m1, m2), m3));
  f32x2 C2 = {C, C}, nm2 = {nm, nm};
  f32x16 o = splat16(0.f);
#pragma unroll
  for (int n = 0; n < 8; ++n) {
    v4u na, nb;
    if (n < 7) { vp += vs; na = lds_r128(vp); nb = lds_r128(vp + 512); }
    f32x2 e[8];
#pragma unroll
    for (int i = 0; i < 8; ++i) { const f32x2 v = {s[n][2 * i], s[n][2 * i + 1]}; e[i] = __builtin_elementwise_fma(v, C2, nm2); }
#pragma unroll
    for (int i = 0; i < 8; ++i) PIN(e[i]);
    v4u p0, p1;
#pragma unroll
    for (int i = 0; i < 4; ++i) p0[i] = pk2(__builtin_amdgcn_exp2f(e[i][0]), __builtin_amdgcn_exp2f(e[i][1]));
#pragma unroll
    for (int i = 0; i < 4; ++i) p1[i] = pk2(__builtin_amdgcn_exp2f(e[4 + i][0]), __builtin_amdgcn_exp2f(e[4 + i][1]));
    if (n == 0) {
      const v4u d0 = p0 & mlo, d1 = p1 & mhi;
      o = MFMA(da, d0, o); o = MFMA(va, p0 ^ d0, o); o = MFMA(db, d1, o); o = MFMA(vb, p1 ^ d1, o);
    } else { o = MFMA(va, p0, o); o = MFMA(vb, p1, o); }
    if (n < 7) { va = na; vb = nb; }
    SB();
    if (n == 6 && late) { BAR(); __builtin_amdgcn_s_setprio(3); }
  }
  return o;
}

__global__ void __launch_bounds__(512, 2) mega(const u16* __restrict__ x_in, const u16* __restrict__ Aw, const u16* __restrict__ Ao,
                                               const u16* __restrict__ Bw, const u16* __restrict__ Bo, u16* __restrict__ out, unsigned char* ws) {
  extern __shared__ __attribute__((aligned(16))) char smem[];
  const lptr L = (lptr)smem;
  const unsigned L0 = (unsigned)(uintptr_t)L;
  int b = blockIdx.x, lane = threadIdx.x & 63, w = __builtin_amdgcn_readfirstlane(threadIdx.x >> 6);
  const __amdgpu_buffer_rsrc_t rs = __builtin_amdgcn_make_buffer_rsrc(ws, 0, WS_END, 0x00020000);
  unsigned zero = 0; PIN(zero);
  unsigned tag;
  v4u x0 = {0u, 0u, 0u, 0u}, x1 = x0;

  {
    const int g = MAP_G(b), j = MAP_J(b), t = w & 3, r = lane & 31, h = lane >> 5;
    unsigned epoch = __hip_atomic_load((gu32*)ws, RLX_AGENT);
    if (w < 4) {
      if (w == 0) { const v4u z = {0u, 0u, 0u, 0u}; lds_w128(L + L_WX + lane * 16, z); }
      BAR();
      unsigned warm = 0;
      if (w == 1 && lane < 48) warm = *(const GAS unsigned*)((const char*)Aw + (b * 48 + lane) * 128);
      if (w == 2 && lane < 48) warm = *(const GAS unsigned*)((const char*)Bw + (b * 48 + lane) * 128);
      if (w == 3 && lane < 32) warm = *(const GAS unsigned*)((const char*)(lane < 16 ? Ao : Bo) + (b * 16 + (lane & 15)) * 128);
      if (w == 3 && lane >= 32 && lane < 45) {
        const int i = lane - 32;
        const int o = i < 4 ? WS_FX + i * 256 * FL : i < 8 ? WS_FO + (i - 4) * 256 * FL : i < 10 ? WS_FH + (i - 8) * 256 * FL : i < 12 ? WS_FP + (i - 10) * 256 * FL : WS_FR;
        (void)__hip_atomic_fetch_add((gu32*)(ws + o + (g * 16 + j) * FL), zero, RLX_AGENT);
      }
      if (w == 0) { unsigned long long pc; asm volatile("s_getpc_b64 %0" : "=s"(pc));
        const int o = ((b >> 3) * 11 + lane) * 128; if (lane < 11 && o < CODE_AHEAD) warm = *(const GAS unsigned*)((const char*)(pc & ~127ull) + o); }
      dma_rows(Aw + 32 * j, 1536, 32 + 16 * t, L0 + 32768 + 16384 * t, 4, lane, g & 3);
      dma_rows(Aw + 32 * j, 1536, 8 * t, L0 + 8192 * t, 2, lane);
      ctr_arrive(L, C_PR);
      blank(rs, lane * 16, WS_O + (g * 4 + t) * 32768 + 2 * j * 1024);
#pragma unroll
      for (int i = 0; i < 4; ++i) blank(rs, lane * 16 + i * 1024, WS_KV + ((g * 16 + j) * 4 + t) * 4096);
      asm volatile("s_waitcnt vmcnt(13)" :: "v"(warm) : "memory");
      ctr_arrive(L, C_R); ctr_wait(L, C_R, 5u);
    } else {
      __builtin_amdgcn_s_setprio(3);
      const u16* xrow = x_in + (size_t)(g * 128 + 8 * j + 2 * t + h) * 512 + 8 * r;
      const v4u xa = *(const GAS v4u*)xrow, xb = *(const GAS v4u*)(xrow + 256);
      BAR();
      { const unsigned fill = (lane & 16) ? 0x3f800000u : ONE2; const v4u o = {fill, fill, fill, fill}; if (w == 4) lds_w128(L + L_ONES + lane * 16, o); }
      if (h == 0) *(LAS unsigned*)(L + L_NM + t * 1024 + r * 4) = 0u;
      if (w >= 6) { v4u mk;
#pragma unroll
        for (int i = 0; i < 4; ++i) { const int key = ((2 * i) & 3) + 8 * (i >> 1) + 16 * (w - 6) + 4 * h; mk[i] = (key + 1 <= r) ? 0xffffffffu : ((key <= r) ? 0xffffu : 0u); }
        lds_w128(L + L_MSK + (w - 6) * 1024 + lane * 16, mk); }
      float s1 = 0.f, s2 = 0.f;
#pragma unroll
      for (int i = 0; i < 4; ++i) { s1 = dot2(xa[i], ONE2, s1); s2 = dot2(xa[i], xa[i], s2); s1 = dot2(xb[i], ONE2, s1); s2 = dot2(xb[i], xb[i], s2); }
      float m0, r0; row_stats(sum32(s1), sum32(s2), m0, r0);
      const v4u na = ln_frag(xa, m0, r0), nb = ln_frag(xb, m0, r0);
      { const lptr d = L + L_LN + (r >> 1) * 256 + (2 * t + h) * 16 + (r & 1) * 8;
        v2u u = {na[0], na[1]}; *(LAS v2u*)d = u; u[0] = na[2]; u[1] = na[3]; *(LAS v2u*)(d + 128) = u;
        u[0] = nb[0]; u[1] = nb[1]; *(LAS v2u*)(d + 4096) = u; u[0] = nb[2]; u[1] = nb[3]; *(LAS v2u*)(d + 4096 + 128) = u; }
      ctr_arrive(L, C_ST); ctr_wait(L, C_ST, 4u);
      const int vo = (lane >> 4) * 1024 + (((lane >> 3) & 1) * 32 + 8 * (j & 3) + (lane & 7)) * 16;
      const int xb0 = WS_X + (g * 4 + (j >> 2)) * 32768 + t * 8192;
      st_sc1(rs, lds_r128(L + L_LN + t * 2048 + lane * 16), vo, xb0);
      st_sc1(rs, lds_r128(L + L_LN + t * 2048 + 1024 + lane * 16), vo, xb0 + 4096);
      PIN(epoch);
      publish_wg(L, C_PX, 4u, lane, ws, WS_FX + (g * 16 + lane) * FL + j * 4, lane < 16, epoch + 1u);
      if (t == 3) { (void)poll(ws, WS_FX + (g * 16 + j) * FL, lane, 16, __builtin_amdgcn_readfirstlane(epoch) + 1u, zero); ctr_arrive(L, C_R); }
      __builtin_amdgcn_s_setprio(0);
      ctr_wait(L, C_PR, 4u);
      v4u rt[4];
#pragma unroll
      for (int i = 0; i < 4; ++i) {
        const bool po = (i >= 2) && (t & 1);
        const u16* src = po ? (t == 1 ? Ao : Bo) + (32 * g + 16 * (i & 1)) * 512
                            : (t < 2 ? Bw : Aw) + (32 * g + 16 * (i & 1)) * 1536 + (2 * (t & 1) + (i >> 1)) * 512;
        rt[i] = *(const GAS v4u*)(src + 32 * j + (lane >> 2) * (po ? 512 : 1536) + (lane & 3) * 8);
      }
      ctr_wait(L, C_XI, 4u);
#pragma unroll
      for (int i = 0; i < 4; ++i) {
        const bool po = (i >= 2) && (t & 1);
        const int piece = (po ? (t == 1 ? T_OA : T_OB) : (t < 2 ? T_WB : T_WA) + 32 * (2 * (t & 1) + (i >> 1))) + 2 * g + (i & 1);
        st_sc1(rs, rt[i], lane * 16, WS_WT + (j * 256 + piece) * 1024);
      }
    }
    PIN(epoch);
    tag = __builtin_amdgcn_readfirstlane(epoch) + 1u;
  }

  for (int pr = 0; pr < 2; ++pr) {
#pragma unroll
    for (int isB = 0; isB < 2; ++isB) {
      const int l = 2 * pr + isB;
      asm volatile("" : "+v"(lane), "+s"(b), "+s"(w));
      const int g = MAP_G(b), j = MAP_J(b);
      const int t = w & 3, hh = w >> 2, r = lane & 31, h = lane >> 5;
      const bool isQ = w < 4;
      const int gt = g * 4 + t, gj = g * 16 + j;
      const int trl = ((lane & 15) >> 2) * 64 + ((lane >> 4) & 1) * 32 + (lane & 3) * 8 + h * 256;
      const int l16 = lane * 16;
      const unsigned char* wt = ws + WS_WT + j * (256 * 1024);
      v4u qf;
      gu32* a3 = nullptr;
      int mo_ = WS_O + l * MB2 + gt * 32768 + (2 * j + hh) * 1024 + l16;
      lptr ma0 = L + L_PR + (r < 16 ? (hh * 16 + r + 32 * h) * 16 : 2048 + (hh * 16 + 4 * h) * 4), ma1 = ma0 + (r < 16 ? 1024 : 32);
      if (isQ) {
        const lptr pq = L + trl;
        lptr pv = pq + L_WV; PIN(pv);
        int xb = WS_X + l * MB2 + gt * 32768; PINS(xb);
        Probes pb_ = poll(ws, WS_FX + (l * 256 + gj) * FL, lane, l > 0 ? 16 : 0, tag, zero);
        f32x16 aK, aV; float rstd; f32x4 rs16[4]; v4u xf[32]; unsigned xe; v2u eq;
        unsigned tries = 0;
        v4u k0, k1, vf0, vf1; f32x16 d; float z;
        do {
          if (!isB) gemm_kv<false>(xf, L, pq, pv, aK, aV, rs, lane, xb, xe, eq, rstd, rs16, 4u * l + 4u, t, tries == 0, pb_,
            [&](auto SI) __attribute__((always_inline)) { constexpr int i = decltype(SI)::value;
              if constexpr (i >= 2 && i < 6) { constexpr int c = 4 * (i - 2); unsigned u = pk2(aK[c] * rstd, aK[c + 1] * rstd), v = pk2(aK[c + 2] * rstd, aK[c + 3] * rstd); PIN(u); PIN(v);
                if constexpr (i < 4) { k0[2 * i - 4] = u; k0[2 * i - 3] = v; } else { k1[2 * i - 8] = u; k1[2 * i - 7] = v; } } });
          else {
            gemm_kv<true>(xf, L, pq, pv, aK, aV, rs, lane, xb, xe, eq, rstd, rs16, 4u * l + 4u, t, tries == 0, pb_,
              [&](auto SI) __attribute__((always_inline)) { constexpr int i = decltype(SI)::value;
                if constexpr (i >= 2 && i < 6) { constexpr int c = 2 * (i - 2); unsigned u = phi2(aK[c] * rs16[c >> 2][c & 3], aK[c + 1] * rs16[c >> 2][(c & 3) + 1]); PIN(u); k0[i - 2] = u; } });
            scale_feat(aV, rs16);
            vf0 = pack8(aV, 0);
#pragma unroll
            for (int i = 8; i < 16; ++i) aK[i] *= rs16[i >> 2][i & 3];
            d = MFMA(k0, vf0, splat16(0.f));
            k1 = phi_pack8(aK, 8); vf1 = pack8(aV, 8);
            d = MFMA(k1, vf1, d);
            float z0 = 0.f, z1 = 0.f;
#pragma unroll
            for (int i = 0; i < 4; ++i) { z0 = dot2(k0[i], ONE2, z0); z1 = dot2(k1[i], ONE2, z1); }
            z = half_sum(z0 + z1); PIN(z);
          }
          pb_ = Probes{0u, 0u, 0u};
        } while (again(isnan_(isB ? d[0] : aK[0]), tries));
        if (!isB) {
          DRAIN();
          BAR();
          const int kb = WS_KV + ((pr * 256 + gj) * 4 + t) * 4096;
          if (g < 15) { st_sc1(rs, k0, l16, kb); st_sc1(rs, k1, l16 + 1024, kb); }
          scale_feat(aV, rs16);
          vf0 = pack8(aV, 0); vf1 = pack8(aV, 8);
          if (g < 15) { st_sc1(rs, vf0, l16 + 2048, kb); st_sc1(rs, vf1, l16 + 3072, kb); }
          lds_w128(L + L_K + (8 + t) * 1024 + l16, k0); lds_w128(L + L_K + (20 + t) * 1024 + l16, k1);
          const lptr vd = L + L_V + ((r >> 4) * 12 + 8 + t) * 1024 + h * 256 + (r & 15) * 16;
          lds_w128(vd, vf0); lds_w128(vd + 512, vf1);
          f32x16 aQ = gemm_q(xf, pq, xe, eq);
          scale_tok(aQ, rstd);
          qf = pack8(aQ, 0); lds_w128(L + L_QF + t * 1024 + l16, pack8(aQ, 8));
          if (pr == 0 && t < 3) {
            const float c = colsum(pq + t * 32768);
            if (h == 0) *(LAS float*)(L + L_WX + (t * 32 + r) * 4) = c;
          }
        } else {
          DRAIN();
          const lptr pw = L + L_PW + t * PSTR;
          if (h == 0) *(LAS float*)(pw + 2048 + r * 4) = z;
          if (r < 16) { *(LAS f32x4*)(pw + l16) = f32x4{d[0], d[1], d[2], d[3]}; *(LAS f32x4*)(pw + 1024 + l16) = f32x4{d[4], d[5], d[6], d[7]}; asm volatile("" ::: "memory"); }
          else { *(LAS f32x4*)(pw + l16) = f32x4{d[8], d[9], d[10], d[11]}; *(LAS f32x4*)(pw + 1024 + l16) = f32x4{d[12], d[13], d[14], d[15]}; }
          BAR();
          f32x16 aQ = gemm_q(xf, pq, xe, eq);
          if (pr == 0) ctr_arrive(L, C_QD);
          scale_tok(aQ, rstd);
          qf = phi_pack8(aQ, 0); lds_w128(L + L_QF + t * 1024 + l16, phi_pack8(aQ, 8));
          dma_tiled(wt, T_OB + 8 * t, L0 + L_WO + 8192 * t, 2, l16);
          if (pr == 0) {
            ctr_wait(L, C_QD, 4u);
            dma_tiled(wt, T_WA + 24 * t, L0 + 24576 * t, 3, l16);
          }
          PIN(ma0); PIN(ma1);
        }
        PIN(mo_);
        if (isB) a3 = slot_of(ws, WS_FO + (l * 256 + g * 16 + lane) * FL + j * 4);
        BAR();
      } else {
        __builtin_amdgcn_s_setprio(3);
        if (l > 0) {
          const Probes pb_ = poll(ws, WS_FX + (l * 256 + gj) * FL, lane, 16, tag, zero);
          v4u st[4];
          unsigned tries = 0, m;
          do {
#pragma unroll
            for (int i = 0; i < 4; ++i) st[i] = ld_sc1(rs, l16 + i * 1024, WS_XS + (l * 64 + gt) * 4096);
            m = chk4(chk4(chk4(chk4(0u, st[0]), st[1]), st[2]), st[3]);
          } while (again(m, tries));
          retire(pb_);
          f32x4 a;
#pragma unroll
          for (int i = 0; i < 4; ++i) a[i] = add1(add1(__uint_as_float(st[0][i]), __uint_as_float(st[1][i])), add1(__uint_as_float(st[2][i]), __uint_as_float(st[3][i])));
          float m0, r0, m1, r1;
          row_stats(half_sum(x16_sum(a[0])), half_sum(x16_sum(a[1])), m0, r0);
          row_stats(half_sum(x16_sum(a[2])), half_sum(x16_sum(a[3])), m1, r1);
          if (lane < 16) { *(LAS v2u*)(L + L_NM + t * 1024 + lane * 8) = v2u{pk2(-m0, -m0), pk2(-m1, -m1)};
            *(LAS f32x2*)(L + L_RS + (t & 1) * 128 + (t >> 1) * 512 + lane * 8) = f32x2{r0, r1}; }
          ctr_arrive(L, C_ST);
        }
        ctr_wait(L, C_XI, 4u * l + 4u);
        blank(rs, l16, WS_O + l * MB2 + gt * 32768 + (2 * j + 1) * 1024);
        if (l < 3) {
          blank(rs, l16, WS_X + (l + 1) * MB2 + gt * 32768 + j * 2048); blank(rs, l16 + 1024, WS_X + (l + 1) * MB2 + gt * 32768 + j * 2048);
          if (lane < 16) blank(rs, l16, WS_XS + (((l + 1) * 64 + gt) * 16 + j) * 256);
        }
        if (!isB && t == 0) { const int pb = WS_P + ((pr * 16 + j) * 16 + g) * PSTR; blank(rs, l16, pb); blank(rs, l16 + 1024, pb); if (lane < 8) blank(rs, l16 + 2048, pb); }
        gu32* const a1 = slot_of(ws, isB ? WS_FP + (pr * 256 + lane * 16 + j) * FL + g * 4 : WS_FH + (pr * 256 + gj + 16 * (lane + 1)) * FL + lane * 4);
        BAR();
        if (!isB) {
          if (t == 0) announce(a1, lane < 2 && g + lane < 15, tag);
          v4u kv[8];
          int kb0 = WS_KV + ((pr * 256 + gj - 16) * 4 + t) * 4096, kb1 = kb0 - 16 * 4 * 4096; PINS(kb0); PINS(kb1);
          const Probes pb_ = poll(ws, WS_FH + (pr * 256 + gj) * FL, lane, g < 2 ? g : 2, tag, zero);
          const v4u z4 = {0u, 0u, 0u, 0u};
          unsigned tries = 0;
#define LDH(i0) do { if (g > 0) { kv[i0] = ld_sc1(rs, l16 + (i0) * 1024, kb0); kv[i0 + 1] = ld_sc1(rs, l16 + (i0) * 1024 + 1024, kb0); } else { kv[i0] = z4; kv[i0 + 1] = z4; } \
                     if (g > 1) { kv[4 + i0] = ld_sc1(rs, l16 + (i0) * 1024, kb1); kv[5 + i0] = ld_sc1(rs, l16 + (i0) * 1024 + 1024, kb1); } else { kv[4 + i0] = z4; kv[5 + i0] = z4; } } while (0)
#define BADH(i0) chk4(chk4(chk4(chk4(0u, kv[i0]), kv[i0 + 1]), kv[4 + i0]), kv[5 + i0])
          LDH(0); LDH(2);
          SB();
          while (again(BADH(0), tries)) LDH(0);
          retire(pb_);
          lds_w128(L + L_K + (4 + t) * 1024 + l16, kv[0]); lds_w128(L + L_K + (16 + t) * 1024 + l16, kv[1]);
          lds_w128(L + L_K + t * 1024 + l16, kv[4]); lds_w128(L + L_K + (12 + t) * 1024 + l16, kv[5]);
          BAR();
          while (again(BADH(2), tries)) LDH(2);
          { const lptr vd = L + L_V + ((r >> 4) * 12 + t) * 1024 + h * 256 + (r & 15) * 16;
            lds_w128(vd + 4096, kv[2]); lds_w128(vd + 4096 + 512, kv[3]); lds_w128(vd, kv[6]); lds_w128(vd + 512, kv[7]); }
          ctr_arrive(L, C_KV);
          if (pr == 0) publish_wg(L, C_RT, 4u, lane, ws, WS_FR + (lane * 16 + j) * FL + g * 4, lane < 16, tag); else DRAIN();
          if (pr == 0) {
            const u16* xr = x_in + (size_t)(gt * 32 + r) * 512 + 32 * j + 4 * h;
            const v2u a = *(const GAS v2u*)xr, c = *(const GAS v2u*)(xr + 8), d = *(const GAS v2u*)(xr + 16), e = *(const GAS v2u*)(xr + 24);
            x0 = v4u{a[0], a[1], c[0], c[1]}; x1 = v4u{d[0], d[1], e[0], e[1]};
          }
#undef LDH
#undef BADH
        } else {
          { const int pb = WS_P + ((pr * 16 + j) * 16 + g) * PSTR;
            if (t < 3) {
              const lptr pp = L + L_PW + t * 1024 + l16;
              f32x4 u[4];
#pragma unroll
              for (int q = 0; q < 4; ++q) u[q] = lds_r128f(pp + q * PSTR);
              SB();
              f32x4 sm;
#pragma unroll
              for (int i = 0; i < 4; ++i) sm[i] = add1(add1(u[0][i], u[1][i]), add1(u[2][i], u[3][i]));
              if (t < 2 || lane < 8) st_sc1(rs, __builtin_bit_cast(v4u, sm), l16 + t * 1024, pb);
            } else announce(a1, lane < 16, tag);
            }
          int pb = WS_P + ((pr * 16 + j) * 16 + 4 * t) * PSTR; PINS(pb);
          int zo = ((lane >> 3) & 3) * PSTR + 2048 + (lane & 7) * 16; PIN(zo);
          const Probes pb_ = poll(ws, WS_FP + (pr * 256 + gj) * FL + 16 * t, lane, 4, tag, zero);
          unsigned tries = 0;
          const lptr pw = L + L_PR + t * PSTR;
          float chk;
          do {
            v4u a[8];
#pragma unroll
            for (int q = 0; q < 4; ++q) { a[2 * q] = ld_sc1(rs, l16, pb + q * PSTR); a[2 * q + 1] = ld_sc1(rs, l16 + 1024, pb + q * PSTR); }
            const v4u zz = ld_sc1(rs, zo, pb);
            SB();
            f32x4 u0, u1, z;
#pragma unroll
            for (int i = 0; i < 4; ++i) {
#define F(x) __uint_as_float(x)
              u0[i] = add1(add1(F(a[0][i]), F(a[2][i])), add1(F(a[4][i]), F(a[6][i]))); u1[i] = add1(add1(F(a[1][i]), F(a[3][i])), add1(F(a[5][i]), F(a[7][i])));
              z[i] = x16_sum(dpp_add<0x128>(F(zz[i])));
#undef F
            }
            *(LAS f32x4*)(pw + l16) = u0; *(LAS f32x4*)(pw + 1024 + l16) = u1;
            if (lane < 8) *(LAS f32x4*)(pw + 2048 + l16) = z;
            chk = add1(add1(add1(u0[0], u0[1]), add1(u0[2], u0[3])), add1(add1(u1[0], u1[1]), add1(u1[2], u1[3])));
            chk = add1(chk, add1(add1(z[0], z[1]), add1(z[2], z[3])));
          } while (again(isnan_(chk), tries));
          retire(pb_);
          DRAIN();
          PIN(ma0); PIN(ma1); PIN(mo_);
          BAR();
        }
        qf = lds_r128(L + L_QF + t * 1024 + l16);
        if (!isB) __builtin_amdgcn_s_setprio(0);
      }

      f32x16 o;
      if (!isB) {
        const lptr kp = L + L_K + (hh * 12 + t) * 1024 + l16;
        const lptr vp = (r < 16 ? L + L_V + (hh * 12 + t) * 1024 + h * 256 : L + L_ONES) + (r & 15) * 16;
        const int vs = r < 16 ? 1024 : 0;
        if (g >= 2) o = attention<false>(L, 4u * pr + 4u, !isQ, kp, vp, vs, L + L_MSK + l16, qf, 0);
        else o = attention<true>(L, 4u * pr + 4u, !isQ, kp, vp, vs, L + L_MSK + l16, qf, 8 - 4 * g - t);
      } else {
        f32x4 p0[4], p1[4];
#pragma unroll
        for (int q = 0; q < 4; ++q) { p0[q] = lds_r128f(ma0 + q * PSTR); p1[q] = lds_r128f(ma1 + q * PSTR); }
        SB();
        const f32x4 u0 = (p0[0] + p0[1]) + (p0[2] + p0[3]), u1 = (p1[0] + p1[1]) + (p1[2] + p1[3]);
        const v4u af = {pk2(u0[0], u0[1]), pk2(u0[2], u0[3]), pk2(u1[0], u1[1]), pk2(u1[2], u1[3])};
        o = MFMA(af, qf, splat16(0.f));
        o[8] += 1e-6f;
      }
      { const float ri = __builtin_amdgcn_rcpf(o[8]);
#pragma unroll
        for (int i = 0; i < 8; ++i) o[i] *= ri;
        DRAIN();
        if (isB && w == 0) announce(a3, lane < 16, tag);
        st_sc1(rs, pack8(o, 0), mo_, 0);
      }

      if (isQ) {
        if (!isB) {
          if (pr == 0) (void)poll(ws, WS_FR + gj * FL, lane, 16, tag, zero);
          dma_tiled(wt, T_WB + 24 * t, L0 + 24576 * t, 6, l16);
          a3 = slot_of(ws, WS_FO + (l * 256 + g * 16 + lane) * FL + j * 4);
          BAR();
          if (t == 0) announce(a3, lane < 16, tag);
          nap<7>();
          dma_tiled(wt, T_OA + 8 * t, L0 + L_WO + 8192 * t, 2, l16);
          DRAIN(); ctr_arrive(L, C_WO);
        } else {
          ctr_arrive(L, C_WO);
          if (pr == 0) { nap<3>(); dma_tiled(wt, T_WA + 24 * t + 12, L0 + 24576 * t + 12288, 3, l16); }
        }
        if (l < 3) {
          DRAIN(); ctr_arrive(L, C_W); ctr_wait(L, C_W, 4u * l + 4u);
          if (l == 0 && t < 3) {
            const float c = colsum(L + trl + t * 32768);
            if (h == 0) *(LAS float*)(L + L_WX + 384 + (t * 32 + r) * 4) = c;
          }
          ctr_wait(L, C_OP, 4u * l + 4u);
          blank(rs, l16, WS_O + (l + 1) * MB2 + gt * 32768 + 2 * j * 1024);
          if (l == 1) {
#pragma unroll
            for (int i = 0; i < 4; ++i) blank(rs, l16 + i * 1024, WS_KV + ((256 + gj) * 4 + t) * 4096);
          }
          gu32* const a4 = slot_of(ws, WS_FX + ((l + 1) * 256 + g * 16 + lane) * FL + j * 4);
          BAR();
          if (t == 0) announce(a4, lane < 16, tag);
        }
      } else {
        __builtin_amdgcn_s_setprio(3);
        lptr po = L + L_WO + trl; PIN(po);
        int ob = WS_O + l * MB2 + gt * 32768; PINS(ob);
        const Probes pb_ = poll(ws, WS_FO + (l * 256 + gj) * FL, lane, 16, tag, zero);
        f32x16 y; unsigned tries = 0;
        const int xo_ = WS_X + (l + 1) * MB2 + gt * 32768 + j * 2048 + l16;
        do y = gemm_out(L, po, rs, l16, ob, 4u * l + 4u, tries == 0); while (again(isnan_(y[0]), tries));
        retire(pb_);
        if (l < 3) BAR();
        { v4u y0 = pack8(y, 0), y1 = pack8(y, 8);
          PIN(y0); PIN(y1);
          f32x16 a;
#pragma unroll
          for (int i = 0; i < 4; ++i) { a[2 * i] = add1(bflo(x0[i]), bflo(y0[i])); a[2 * i + 1] = add1(bfhi(x0[i]), bfhi(y0[i])); }
          x0 = pack8(a, 0);
          if (l < 3) st_sc1(rs, x0, xo_, 0);
#pragma unroll
          for (int i = 0; i < 4; ++i) { a[8 + 2 * i] = add1(bflo(x1[i]), bflo(y1[i])); a[9 + 2 * i] = add1(bfhi(x1[i]), bfhi(y1[i])); }
          x1 = pack8(a, 8); }
        if (l < 3) {
          st_sc1(rs, x1, xo_ + 1024, 0);
          SB();
          float s1 = 0.f, s2 = 0.f, s3 = 0.f, s4 = 0.f;
#pragma unroll
          for (int i = 0; i < 4; ++i) { s1 = dot2(x0[i], ONE2, s1); s2 = dot2(x0[i], x0[i], s2); s3 = dot2(x1[i], ONE2, s3); s4 = dot2(x1[i], x1[i], s4); }
          s1 = half_sum(s1 + s3); s2 = half_sum(s2 + s4);
          if (h == 0) { v2u v = {__float_as_uint(s1), __float_as_uint(s2)}; __builtin_amdgcn_raw_buffer_store_b64(v, rs, r * 8, WS_XS + (((l + 1) * 64 + gt) * 16 + j) * 256, SC1); }
        } else {
          const auto p = __builtin_amdgcn_permlane32_swap(x0[0], x0[2], false, false), q = __builtin_amdgcn_permlane32_swap(x0[1], x0[3], false, false);
          const auto u = __builtin_amdgcn_permlane32_swap(x1[0], x1[2], false, false), v = __builtin_amdgcn_permlane32_swap(x1[1], x1[3], false, false);
          u16* op = out + (size_t)(gt * 32 + r) * 512 + 32 * j + 8 * h;
          *(GAS v4u*)op = v4u{p[0], q[0], p[1], q[1]}; *(GAS v4u*)(op + 16) = v4u{u[0], v[0], u[1], v[1]};
          if (b == 0 && w == 4 && lane == 0) __hip_atomic_store((gu32*)ws, tag, RLX_AGENT);
        }
      }
    }
  }
}

extern "C" void kernel_launch(void* const* d_in, const int* in_sizes, int n_in, void* d_out, int out_size, void* d_ws, size_t ws_size,
                              hipStream_t stream) {
  (void)in_sizes; (void)n_in; (void)out_size; (void)ws_size;
  (void)hipFuncSetAttribute((const void*)mega, hipFuncAttributeMaxDynamicSharedMemorySize, LDS_BYTES);
  mega<<<dim3(256), dim3(512), LDS_BYTES, stream>>>((const u16*)d_in[0], (const u16*)d_in[1], (const u16*)d_in[2], (const u16*)d_in[4],
                                                    (const u16*)d_in[5], (u16*)d_out, (unsigned char*)d_ws);
}
```
